```python
import torch

def _softmax(x, axis=-1):
    return torch.softmax(x, dim=axis)
def _gelu(x):
    return torch.nn.functional.gelu(x, approximate="tanh")
def _silu(x):
    return torch.nn.functional.silu(x)
def _sigmoid(x):
    return torch.sigmoid(x)
def _layernorm(x, axis=-1, eps=1e-5):
    m = x.mean(dim=axis, keepdim=True)
    v = x.var(dim=axis, keepdim=True, unbiased=False)
    return (x - m) / torch.sqrt(v + eps)
def _scatter_add(tgt, idx, src, axis=0):
    return tgt.clone().index_add_(axis, idx, src)
def _segment_sum(src, seg, n):
    out = torch.zeros((n,) + src.shape[1:], dtype=src.dtype)
    return out.index_add_(0, seg, src)
def _pad(x, pw):
    flat = tuple(v for pair in reversed(pw) for v in pair)
    return torch.nn.functional.pad(x, flat)

kernel_name = 'interleave_layers__ssm_conv_h3__glu_gated_reglu__prompt'

def setup_inputs():

    _w = (lambda a: (lambda t: t.to(torch.bfloat16) if t.is_floating_point() else t)(torch.as_tensor(a)))
    def _A_setup():
        B, S, D = 1, 2048, 448
        ks = 4
        return {
            "q": _w((lambda *s: torch.randn(*s, dtype=torch.bfloat16) * 0.05)(B, S, D)),
            "k": _w((lambda *s: torch.randn(*s, dtype=torch.bfloat16) * 0.2)(B, S, D)),
            "v": _w((lambda *s: torch.randn(*s, dtype=torch.bfloat16) * 0.2)(B, S, D)),
            "shift_k": _w((lambda *s: torch.randn(*s, dtype=torch.bfloat16) * 0.2)(D, ks) * 0.5),
            "ssm_k": _w((lambda *s: torch.randn(*s, dtype=torch.bfloat16) * 0.2)(D, S) * (1.0 / S ** 0.5)),
            "ssm_D": _w((lambda *s: torch.randn(*s, dtype=torch.bfloat16) * 0.2)(D)),
        }
    _A_in = _A_setup()

    def _B_setup():
        B, S, D, F = 1, 2048, 448, 1792
        return {
            "x": _w((lambda *s: torch.randn(*s, dtype=torch.bfloat16) * 0.05)(B, S, D)),
            "W_gate": _w((lambda *s: torch.randn(*s, dtype=torch.bfloat16) * 0.2)(D, F) * (2.0 / D) ** 0.5),
            "W_up": _w((lambda *s: torch.randn(*s, dtype=torch.bfloat16) * 0.2)(D, F) * (2.0 / D) ** 0.5),
            "W_down": _w((lambda *s: torch.randn(*s, dtype=torch.bfloat16) * 0.2)(F, D) * (1.0 / F) ** 0.5),
        }
    _B_in = _B_setup()

    return {
        "A_q": _A_in["q"],
        "A_k": _A_in["k"],
        "A_v": _A_in["v"],
        "A_shift_k": _A_in["shift_k"],
        "A_ssm_k": _A_in["ssm_k"],
        "A_ssm_D": _A_in["ssm_D"],
        "B_x": _B_in["x"],
        "B_W_gate": _B_in["W_gate"],
        "B_W_up": _B_in["W_up"],
        "B_W_down": _B_in["W_down"],
    }

def reference(A_q, A_k, A_v, A_shift_k, A_ssm_k, A_ssm_D, B_x, B_W_gate, B_W_up, B_W_down):
    def _A_ref(q, k, v, shift_k, ssm_k, ssm_D):
        B, S, D = q.shape
        ks = shift_k.shape[-1]
        k_pad = _pad(k, ((0, 0), (ks - 1, 0), (0, 0)))
        k_shift = torch.zeros_like(k)
        for t in range(ks):
            k_shift = k_shift + k_pad[:, t:t + S, :] * shift_k[:, ks - 1 - t][None, None, :]
        kv = k_shift * v
        L = 2 * S
        kv_pad = _pad(kv, ((0, 0), (0, S), (0, 0)))
        kf_pad = _pad(ssm_k, ((0, 0), (0, S)))
        kv_f = (lambda x, axes=(-2,-1): torch.fft.rfft2(x.float(), dim=axes))(kv_pad[:, None, :, :], axes=(1, 2))
        kf_f = (lambda x, axes=(-2,-1): torch.fft.rfft2(x.float(), dim=axes))(kf_pad[:, None, :], axes=(1, 2))
        kf_f = (lambda x, *d: x.permute(*d))(kf_f, 1, 2, 0)
        y_f = kv_f * kf_f[None, :, :, :]
        y = (lambda x, s, axes=(-2,-1): torch.fft.irfft2(x, s=s, dim=axes).to(torch.bfloat16))(y_f, (1, L), axes=(1, 2))[:, 0, :S, :]
        y = y + kv * ssm_D[None, None, :]
        return q * y

    def _B_ref(x, W_gate, W_up, W_down):
        gate = torch.einsum("bsd,df->bsf", x, W_gate)
        up = torch.einsum("bsd,df->bsf", x, W_up)
        gate = gate * (lambda x, dt: x.to(dt))(gate > 0, gate.dtype)
        h = gate * up
        return torch.einsum("bsf,fd->bsd", h, W_down)

    x = A_q
    x = x + _A_ref(_layernorm(x, axis=-1), A_k, A_v, A_shift_k, A_ssm_k, A_ssm_D)
    x = x + _B_ref(_layernorm(x, axis=-1), B_W_gate, B_W_up, B_W_down)
    x = x + _A_ref(_layernorm(x, axis=-1), A_k, A_v, A_shift_k, A_ssm_k, A_ssm_D)
    x = x + _B_ref(_layernorm(x, axis=-1), B_W_gate, B_W_up, B_W_down)
    return x
```

```cpp
#include <hip/hip_runtime.h>
#include <hip/hip_bf16.h>
#include <stdint.h>

#define DI __device__ __forceinline__
#define LAS __attribute__((address_space(3)))
typedef unsigned v4u __attribute__((ext_vector_type(4)));
typedef unsigned v2u __attribute__((ext_vector_type(2)));
typedef float f32x4 __attribute__((ext_vector_type(4)));
typedef float f32x2 __attribute__((ext_vector_type(2)));
typedef float f32x16 __attribute__((ext_vector_type(16)));
typedef short bf16x8 __attribute__((ext_vector_type(8)));
typedef short s16x4 __attribute__((ext_vector_type(4)));
typedef __bf16 bf2 __attribute__((ext_vector_type(2)));
typedef __amdgpu_buffer_rsrc_t rsrc_t;

constexpr int S = 2048, D = 448, F = 1792;
constexpr int KS1 = D / 16;
constexpr int KS2 = F / 16;
constexpr float EPS = 1e-5f;

constexpr int ACT_BYTES = S * D * 2;
constexpr int OFF_KVT = 0;
constexpr int OFF_X0 = OFF_KVT + ACT_BYTES;
constexpr int OFF_Y = OFF_X0 + ACT_BYTES;
constexpr int OFF_X2 = OFF_Y + ACT_BYTES;
constexpr int OFF_W1 = OFF_X2 + ACT_BYTES;
constexpr int OFF_W2 = OFF_W1 + 112 * KS1 * 1024;
constexpr int OFF_H = OFF_W2 + 14 * KS2 * 1024;
constexpr int OFF_CSP = OFF_H + 64 * KS2 * 1024;
constexpr int OFF_CS = OFF_CSP + 14 * 112 * 32 * 4;
constexpr int OFF_ST0 = OFF_CS + 112 * 32 * 4;
constexpr int OFF_ST2 = OFF_ST0 + 7 * S * 8;
constexpr int WS_BYTES = OFF_ST2 + 7 * S * 8;

DI rsrc_t mk_rsrc(const void* p, int bytes) { return __builtin_amdgcn_make_buffer_rsrc((void*)p, 0, bytes, 0x00020000); }
DI v4u ld128(rsrc_t r, int voff, int soff) { return __builtin_amdgcn_raw_buffer_load_b128(r, voff, soff, 0); }
DI f32x2 ld64(rsrc_t r, int voff, int soff) { return __builtin_bit_cast(f32x2, __builtin_amdgcn_raw_buffer_load_b64(r, voff, soff, 0)); }
DI void st128(v4u v, rsrc_t r, int voff, int soff) { __builtin_amdgcn_raw_buffer_store_b128(v, r, voff, soff, 0); }
DI float bflo(unsigned u) { return __builtin_bit_cast(float, u << 16); }
DI float bfhi(unsigned u) { return __builtin_bit_cast(float, u & 0xffff0000u); }
DI unsigned pk2(float a, float b) {
  f32x2 v = {a, b};
  return __builtin_bit_cast(unsigned, __builtin_convertvector(v, bf2));
}
DI float rbf(float a) { return bflo(pk2(a, a)); }
DI float dot2(unsigned a, unsigned b, float c) {
  return __builtin_amdgcn_fdot2_f32_bf16(__builtin_bit_cast(bf2, a), __builtin_bit_cast(bf2, b), c, false);
}
DI f32x16 mfma32(v4u a, v4u b, f32x16 c) {
  return __builtin_amdgcn_mfma_f32_32x32x16_bf16(__builtin_bit_cast(bf16x8, a), __builtin_bit_cast(bf16x8, b), c, 0, 0, 0);
}
DI f32x4 mfma16(v4u a, v4u b, f32x4 c) {
  return __builtin_amdgcn_mfma_f32_16x16x32_bf16(__builtin_bit_cast(bf16x8, a), __builtin_bit_cast(bf16x8, b), c, 0, 0, 0);
}
constexpr unsigned ONES = 0x3f803f80u;

DI void row_stats(float s, float ss, float& m, float& r) {
  m = s * (1.0f / D);
  float var = fmaxf(ss * (1.0f / D) - m * m, 0.0f);
  r = 1.0f / sqrtf(var + EPS);
}
DI unsigned gate_pair(unsigned x, unsigned y, float r, float c) {
  const float xl = bflo(x), xh = bfhi(x);
  const float tl = fmaf(xl, r, c), th = fmaf(xh, r, c);
  return pk2(fmaf(tl, bflo(y), xl), fmaf(th, bfhi(y), xh));
}

constexpr int PREP_ACT = 224, PREP_W1 = 196, PREP_W2 = 98;
__global__ void __launch_bounds__(512) k_prep(const uint16_t* __restrict__ q, const uint16_t* __restrict__ k,
                                              const uint16_t* __restrict__ v, const uint16_t* __restrict__ shk,
                                              const uint16_t* __restrict__ wg, const uint16_t* __restrict__ wu,
                                              const uint16_t* __restrict__ wd, unsigned char* __restrict__ ws) {
  __shared__ __attribute__((aligned(16))) unsigned char sm[2 * 64 * 144];
  const int tid = threadIdx.x, lane = tid & 63, w = tid >> 6;
  int b = blockIdx.x;
  if (b < PREP_ACT) {
    const int tt = b / 7, cg = b % 7;
    const int r = tid >> 3, qq = tid & 7;
    const int tok = tt * 64 + r, c0 = cg * 64 + qq * 8;
    v4u kk[4];
#pragma unroll
    for (int j = 0; j < 4; ++j) {
      const int t = tok - j;
      kk[j] = v4u{0u, 0u, 0u, 0u};
      if (t >= 0) kk[j] = *(const v4u*)(k + (size_t)t * D + c0);
    }
    const v4u vv = *(const v4u*)(v + (size_t)tok * D + c0);
    const v4u xx = *(const v4u*)(q + (size_t)tok * D + c0);
    v4u sw[4];
#pragma unroll
    for (int j = 0; j < 4; ++j) sw[j] = *(const v4u*)(shk + (size_t)c0 * 4 + j * 8);
    {
      const int T = tok >> 5, ks = c0 >> 4, kh = (c0 >> 3) & 1;
      *(v4u*)(ws + OFF_X0 + ((T * KS1 + ks) * 64 + kh * 32 + (tok & 31)) * 16) = xx;
      float s = 0.f, ss = 0.f;
#pragma unroll
      for (int i = 0; i < 4; ++i) { s = dot2(xx[i], ONES, s); ss = dot2(xx[i], xx[i], ss); }
#pragma unroll
      for (int o = 1; o < 8; o <<= 1) { s += __shfl_xor(s, o); ss += __shfl_xor(ss, o); }
      if (qq == 0) *(f32x2*)(ws + OFF_ST0 + (cg * S + tok) * 8) = f32x2{s, ss};
    }
    uint16_t* tl = (uint16_t*)sm;
#pragma unroll
    for (int e = 0; e < 8; ++e) {
      const unsigned w01 = sw[e >> 1][(e & 1) * 2], w23 = sw[e >> 1][(e & 1) * 2 + 1];
      const float wj[4] = {bflo(w01), bfhi(w01), bflo(w23), bfhi(w23)};
      float acc = 0.f;
#pragma unroll
      for (int j = 3; j >= 0; --j) {
        const unsigned d = kk[j][e >> 1];
        const float kf = (e & 1) ? bfhi(d) : bflo(d);
        acc = rbf(acc + rbf(kf * wj[j]));
      }
      const unsigned dv = vv[e >> 1];
      const float vf = (e & 1) ? bfhi(dv) : bflo(dv);
      tl[(qq * 8 + e) * 72 + (63 - r)] = (uint16_t)(pk2(acc * vf, 0.f) & 0xffffu);
    }
    __syncthreads();
    {
      const int ch = tid >> 3, p = tid & 7;
      const v4u o = *(const v4u*)(tl + ch * 72 + p * 8);
      *(v4u*)(ws + OFF_KVT + ((cg * 64 + ch) * S + (S - 64 - tt * 64) + p * 8) * 2) = o;
    }
    return;
  }
  b -= PREP_ACT;
  const int i16 = lane & 15, tq = i16 >> 2, tp = i16 & 3, grp = (lane >> 4) & 1, kh = lane >> 5;
  const int chunk = 8 * (tp & 1) + 4 * (tp >> 1);
  if (b < PREP_W1) {
    const int kb = b / 28, fb = b % 28;
    const int r = tid >> 3, qq = tid & 7;
    const size_t src = (size_t)(kb * 64 + r) * F + fb * 64 + qq * 8;
    const v4u a0 = *(const v4u*)(wg + src), a1 = *(const v4u*)(wu + src);
    *(v4u*)(sm + r * 144 + qq * 16) = a0;
    *(v4u*)(sm + 64 * 144 + r * 144 + qq * 16) = a1;
    __syncthreads();
    const int fgl = w & 3, kp = w >> 2;
    float cs = 0.f;
#pragma unroll
    for (int i = 0; i < 2; ++i) {
      const int ksl = kp * 2 + i;
      LAS unsigned char* base = (LAS unsigned char*)sm + grp * (64 * 144) + (ksl * 16 + kh * 8 + tq) * 144 + (fgl * 16 + chunk) * 2;
      const s16x4 lo = __builtin_amdgcn_ds_read_tr16_b64_v4i16((LAS s16x4*)base);
      const s16x4 hi = __builtin_amdgcn_ds_read_tr16_b64_v4i16((LAS s16x4*)(base + 4 * 144));
      const v2u l2 = __builtin_bit_cast(v2u, lo), h2 = __builtin_bit_cast(v2u, hi);
      const v4u o = {l2[0], l2[1], h2[0], h2[1]};
      *(v4u*)(ws + OFF_W1 + (((fb * 4 + fgl) * KS1 + kb * 4 + ksl) * 64 + lane) * 16) = o;
#pragma unroll
      for (int e = 0; e < 4; ++e) cs = dot2(o[e], ONES, cs);
    }
    cs += __shfl_xor(cs, 32);
    if (lane < 32) *(float*)(ws + OFF_CSP + (((kb * 2 + kp) * 112 + fb * 4 + fgl) * 32 + lane) * 4) = cs;
    return;
  }
  b -= PREP_W1;
  {
    const int kb = b / 7, cb = b % 7;
    const int r = tid >> 3, qq = tid & 7;
    const size_t src = (size_t)(kb * 128 + r) * D + cb * 64 + qq * 8;
    const v4u a0 = *(const v4u*)(wd + src), a1 = *(const v4u*)(wd + src + 64 * D);
    *(v4u*)(sm + r * 144 + qq * 16) = a0;
    *(v4u*)(sm + 64 * 144 + r * 144 + qq * 16) = a1;
    __syncthreads();
#pragma unroll
    for (int ct = 0; ct < 2; ++ct) {
      LAS unsigned char* base = (LAS unsigned char*)sm + (w * 16 + kh * 8 + tq) * 144 + (ct * 32 + grp * 16 + chunk) * 2;
      const s16x4 lo = __builtin_amdgcn_ds_read_tr16_b64_v4i16((LAS s16x4*)base);
      const s16x4 hi = __builtin_amdgcn_ds_read_tr16_b64_v4i16((LAS s16x4*)(base + 4 * 144));
      const v2u l2 = __builtin_bit_cast(v2u, lo), h2 = __builtin_bit_cast(v2u, hi);
      const v4u o = {l2[0], l2[1], h2[0], h2[1]};
      *(v4u*)(ws + OFF_W2 + (((cb * 2 + ct) * KS2 + kb * 8 + w) * 64 + lane) * 16) = o;
    }
  }
}

constexpr int CV_HSTR = 4160;
constexpr int CV_USTR = 2304 * 2;
constexpr int CV_H = 0, CV_U = 2 * 4 * CV_HSTR, CV_RED = CV_U + 2 * CV_USTR, CV_BYTES = CV_RED + 65536;
__global__ void __launch_bounds__(512, 2) k_conv(const uint16_t* __restrict__ ssk, const uint16_t* __restrict__ ssd,
                                                 unsigned char* __restrict__ ws) {
  extern __shared__ __attribute__((aligned(16))) unsigned char sm[];
  const int tid = threadIdx.x, lane = tid & 63, w = __builtin_amdgcn_readfirstlane(tid >> 6);
  const int pair = (blockIdx.x & 7) * 28 + (blockIdx.x >> 3);
  const int c0 = pair * 2;
  {
    const int ch = tid >> 8, i8 = tid & 255;
    const uint16_t* hp = ssk + (size_t)(c0 + ch) * S + i8 * 8;
    const v4u d = *(const v4u*)hp;
    v2u e = {0u, 0u};
    if (i8 < 255) e = *(const v2u*)(hp + 8);
    const v4u u = *(const v4u*)(ws + OFF_KVT + ((c0 + ch) * S + i8 * 8) * 2);
    if (tid < 16) {
      const int col = pair * 16 + tid;
      float cs = 0.f;
#pragma unroll
      for (int i = 0; i < 14; ++i) cs += *(const float*)(ws + OFF_CSP + (i * 3584 + col) * 4);
      const int rho = col & 15, f = 8 * ((rho >> 2) & 1) + (rho & 3) + 4 * (rho >> 3);
      *(float*)(ws + OFF_CS + ((col & ~15) + f) * 4) = cs;
    }
    const unsigned a0 = __builtin_amdgcn_alignbit(d[1], d[0], 16), a1 = __builtin_amdgcn_alignbit(d[2], d[1], 16),
                   a2 = __builtin_amdgcn_alignbit(d[3], d[2], 16), a3 = __builtin_amdgcn_alignbit(e[0], d[3], 16),
                   a4 = __builtin_amdgcn_alignbit(e[1], e[0], 16);
    unsigned char* hb = sm + CV_H + ch * 4 * CV_HSTR + 32 + i8 * 16;
    *(v4u*)(hb) = d;
    *(v4u*)(hb + CV_HSTR) = v4u{a0, a1, a2, a3};
    *(v4u*)(hb + 2 * CV_HSTR) = v4u{d[1], d[2], d[3], e[0]};
    *(v4u*)(hb + 3 * CV_HSTR) = v4u{a1, a2, a3, a4};
    *(v4u*)(sm + CV_U + ch * CV_USTR + i8 * 16) = u;
    if (i8 < 32) *(v4u*)(sm + CV_U + ch * CV_USTR + 4096 + i8 * 16) = v4u{0u, 0u, 0u, 0u};
    const unsigned d0 = __shfl(d[0], 0), d1 = __shfl(d[1], 0);
    if (i8 < 8) {
      const int sg = i8 >> 1;
      v4u pv = {0u, 0u, 0u, 0u};
      if (i8 & 1) {
        if (sg == 1) pv[3] = d0 << 16;
        if (sg == 2) pv[3] = d0;
        if (sg == 3) { pv[2] = d0 << 16; pv[3] = __builtin_amdgcn_alignbit(d1, d0, 16); }
      }
      *(v4u*)(sm + CV_H + (ch * 4 + sg) * CV_HSTR + (i8 & 1) * 16) = pv;
    }
  }
  __syncthreads();
  {
    const int ch = w >> 2, sp = w & 3;
    const int p = lane & 15, g = lane >> 4;
    const int sg = (1 + p) & 3;
    const unsigned char* fb = sm + CV_H + (ch * 4 + sg) * CV_HSTR + (1 + p + 8 * g - sg) * 2 + sp * 128;
    const unsigned char* gb = sm + CV_U + ch * CV_USTR + (2032 - 16 * p + 8 * g) * 2 + sp * 128;
    f32x4 acc[8];
#pragma unroll
    for (int i = 0; i < 8; ++i) acc[i] = f32x4{0.f, 0.f, 0.f, 0.f};
#pragma unroll
    for (int rr = 0; rr < 2; ++rr) {
      v4u Ff[8], Gf[8];
#pragma unroll
      for (int j = 0; j < 8; ++j) {
        const v2u lo = *(const v2u*)(fb + rr * 64 + j * 512), hi = *(const v2u*)(fb + rr * 64 + j * 512 + 8);
        Ff[j] = v4u{lo[0], lo[1], hi[0], hi[1]};
        Gf[j] = *(const v4u*)(gb + rr * 64 - j * 512);
      }
#pragma unroll
      for (int j = 0; j < 8; ++j)
#pragma unroll
        for (int dl = 0; dl + j < 8; ++dl) acc[j + dl] = mfma16(Gf[dl], Ff[j], acc[j + dl]);
    }
    f32x4* red = (f32x4*)(sm + CV_RED);
#pragma unroll
    for (int i = 0; i < 8; ++i) red[(w * 8 + i) * 64 + lane] = acc[i];
  }
  __syncthreads();
  {
    const f32x4* red = (const f32x4*)(sm + CV_RED);
    const unsigned dd = *(const unsigned*)(ssd + c0);
    f32x4 y[2];
#pragma unroll
    for (int ch = 0; ch < 2; ++ch) {
      y[ch] = red[((ch * 4 + 0) * 8 + w) * 64 + lane];
#pragma unroll
      for (int sp = 1; sp < 4; ++sp) y[ch] += red[((ch * 4 + sp) * 8 + w) * 64 + lane];
    }
    const int p = lane & 15, g = lane >> 4;
    const int ks = c0 >> 4, kh = (c0 >> 3) & 1;
#pragma unroll
    for (int r = 0; r < 4; ++r) {
      const int tok = 256 * w + 16 * (4 * g + r) + p;
      const float u0 = bflo(*(const uint16_t*)(sm + CV_U + (S - 1 - tok) * 2));
      const float u1 = bflo(*(const uint16_t*)(sm + CV_U + CV_USTR + (S - 1 - tok) * 2));
      const float o0 = rbf(y[0][r]) + rbf(u0 * bflo(dd)), o1 = rbf(y[1][r]) + rbf(u1 * bfhi(dd));
      *(unsigned*)(ws + OFF_Y + (((tok >> 5) * KS1 + ks) * 64 + kh * 32 + (tok & 31)) * 16 + (c0 & 7) * 2) = pk2(o0, o1);
    }
  }
}

constexpr int G1_X = 0, G1_ST = 4 * KS1 * 1024, G1_BYTES = G1_ST + 4 * 32 * 4 * 8;
__global__ void __launch_bounds__(512, 2) k_g1(unsigned char* __restrict__ ws, int off_x, int off_st) {
  extern __shared__ __attribute__((aligned(16))) unsigned char sm[];
  const int tid = threadIdx.x, lane = tid & 63, w = __builtin_amdgcn_readfirstlane(tid >> 6);
  const int xcd = blockIdx.x & 7, li = blockIdx.x >> 3;
  const int g = (xcd >> 1) * 4 + li / 7, j = (xcd & 1) * 7 + li % 7;
  const rsrc_t rs = mk_rsrc(ws, WS_BYTES);
  const int l16 = lane * 16, tokl = lane & 31, hh = lane >> 5;
  const int fg = j * 8 + w;
  v4u xs[7], ys[7];
  f32x2 st[7];
  auto stage_load = [&](int set) {
    const int T = g * 4 + set * 2 + (w >> 2);
    const int fo = (T * KS1 + (w & 3) * 7) * 1024;
#pragma unroll
    for (int i = 0; i < 7; ++i) st[i] = ld64(rs, tokl * 8, off_st + (i * S + T * 32) * 8);
#pragma unroll
    for (int i = 0; i < 7; ++i) {
      xs[i] = ld128(rs, l16, off_x + fo + i * 1024);
      ys[i] = ld128(rs, l16, OFF_Y + fo + i * 1024);
    }
  };
  auto stage_write = [&](int set) {
    float s = 0.f, ss = 0.f;
#pragma unroll
    for (int i = 0; i < 7; ++i) { s += st[i][0]; ss += st[i][1]; }
    float m, r;
    row_stats(s, ss, m, r);
    const float c = -m * r;
    const int Tl = set * 2 + (w >> 2);
    s = 0.f; ss = 0.f;
#pragma unroll
    for (int i = 0; i < 7; ++i) {
      v4u o;
#pragma unroll
      for (int e = 0; e < 4; ++e) {
        o[e] = gate_pair(xs[i][e], ys[i][e], r, c);
        s = dot2(o[e], ONES, s);
        ss = dot2(o[e], o[e], ss);
      }
      *(v4u*)(sm + G1_X + (Tl * KS1 + (w & 3) * 7 + i) * 1024 + l16) = o;
    }
    s += __shfl_xor(s, 32);
    ss += __shfl_xor(ss, 32);
    if (lane < 32) *(f32x2*)(sm + G1_ST + ((Tl * 32 + tokl) * 4 + (w & 3)) * 8) = f32x2{s, ss};
  };
  stage_load(0);
  v4u W[KS1];
#pragma unroll
  for (int i = 0; i < KS1; ++i) W[i] = ld128(rs, l16, OFF_W1 + (fg * KS1 + i) * 1024);
  __builtin_amdgcn_sched_barrier(0);
  stage_write(0);
  stage_load(1);
  __builtin_amdgcn_sched_barrier(0);
  __syncthreads();
#pragma unroll
  for (int set = 0; set < 2; ++set) {
    f32x16 acc[2];
#pragma unroll
    for (int t = 0; t < 2; ++t)
#pragma unroll
      for (int i = 0; i < 16; ++i) acc[t][i] = 0.f;
#pragma unroll
    for (int i = 0; i < KS1; ++i)
#pragma unroll
      for (int t = 0; t < 2; ++t) {
        const v4u xf = *(const v4u*)(sm + G1_X + ((set * 2 + t) * KS1 + i) * 1024 + l16);
        acc[t] = mfma32(W[i], xf, acc[t]);
      }
    if (set == 0) {
      stage_write(1);
    }
    f32x4 cs[4];
#pragma unroll
    for (int i = 0; i < 4; ++i) cs[i] = __builtin_bit_cast(f32x4, ld128(rs, hh * 32 + (i & 1) * 16 + (i >> 1) * 64, OFF_CS + fg * 128));
#pragma unroll
    for (int t = 0; t < 2; ++t) {
      const int Tl = set * 2 + t;
      const f32x4 p0 = *(const f32x4*)(sm + G1_ST + (Tl * 32 + tokl) * 32), p1 = *(const f32x4*)(sm + G1_ST + (Tl * 32 + tokl) * 32 + 16);
      float m, r;
      row_stats((p0[0] + p0[2]) + (p1[0] + p1[2]), (p0[1] + p0[3]) + (p1[1] + p1[3]), m, r);
      const float r2 = r * r;
      float hv[8];
#pragma unroll
      for (int i = 0; i < 8; ++i) {
        const float a = fmaf(-m, cs[i >> 2][i & 3], acc[t][i]);
        const float b = fmaf(-m, cs[2 + (i >> 2)][i & 3], acc[t][8 + i]);
        hv[i] = fmaxf(a, 0.f) * r2 * b;
      }
      const v4u o = {pk2(hv[0], hv[1]), pk2(hv[2], hv[3]), pk2(hv[4], hv[5]), pk2(hv[6], hv[7])};
      st128(o, rs, l16, OFF_H + ((g * 4 + Tl) * KS2 + fg) * 1024);
    }
    if (set == 0) __syncthreads();
  }
}

constexpr int G2_RED = 0, G2_ST = 2 * 65536, G2_BYTES = G2_ST + 2 * 32 * 4 * 8;
template <bool LAST>
__global__ void __launch_bounds__(512, 2) k_g2(unsigned char* __restrict__ ws, int off_x, int off_st, uint16_t* __restrict__ out) {
  extern __shared__ __attribute__((aligned(16))) unsigned char sm[];
  const int tid = threadIdx.x, lane = tid & 63, w = __builtin_amdgcn_readfirstlane(tid >> 6);
  const int xcd = blockIdx.x & 7, li = blockIdx.x >> 3;
  const int tp = xcd * 4 + li / 7, js = li % 7;
  const rsrc_t rs = mk_rsrc(ws, WS_BYTES);
  const int l16 = lane * 16, tokl = lane & 31, hh = lane >> 5;
  v4u W[28], hf[14];
  const int ho = OFF_H + (tp * 2 * KS2 + w * 14) * 1024;
  const int wo = OFF_W2 + (js * 2 * KS2 + w * 14) * 1024;
#pragma unroll
  for (int i = 0; i < 14; ++i) {
    hf[i] = ld128(rs, l16, ho + i * 1024);
    W[2 * i] = ld128(rs, l16, wo + i * 1024);
    W[2 * i + 1] = ld128(rs, l16, wo + (KS2 + i) * 1024);
  }
  __builtin_amdgcn_sched_barrier(0);
  const int Tf = tp * 2 + (w >> 2), fr = w & 3;
  v4u xr, yr;
  f32x2 st[7];
#pragma unroll
  for (int t = 0; t < 2; ++t) {
    f32x16 acc[2];
#pragma unroll
    for (int c = 0; c < 2; ++c)
#pragma unroll
      for (int i = 0; i < 16; ++i) acc[c][i] = 0.f;
#pragma unroll
    for (int i = 0; i < 14; ++i) {
      const v4u cur = hf[i];
      if (t == 0) hf[i] = ld128(rs, l16, ho + (KS2 + i) * 1024);
      acc[0] = mfma32(W[2 * i], cur, acc[0]);
      acc[1] = mfma32(W[2 * i + 1], cur, acc[1]);
      __builtin_amdgcn_sched_barrier(0);
    }
    if (t == 0) {
#pragma unroll
      for (int i = 0; i < 7; ++i) st[i] = ld64(rs, tokl * 8, off_st + (i * S + Tf * 32) * 8);
      xr = ld128(rs, l16, off_x + (Tf * KS1 + js * 4 + fr) * 1024);
      yr = ld128(rs, l16, OFF_Y + (Tf * KS1 + js * 4 + fr) * 1024);
    }
    f32x4* red = (f32x4*)(sm + G2_RED + t * 65536);
#pragma unroll
    for (int c = 0; c < 2; ++c)
#pragma unroll
      for (int qd = 0; qd < 4; ++qd)
        red[(w * 8 + c * 4 + qd) * 64 + lane] = f32x4{acc[c][4 * qd], acc[c][4 * qd + 1], acc[c][4 * qd + 2], acc[c][4 * qd + 3]};
  }
  __syncthreads();
  {
    const f32x4* red = (const f32x4*)(sm + G2_RED + (w >> 2) * 65536);
    f32x4 a0 = red[(fr * 2) * 64 + lane], a1 = red[(fr * 2 + 1) * 64 + lane];
#pragma unroll
    for (int ww = 1; ww < 8; ++ww) {
      a0 += red[(ww * 8 + fr * 2) * 64 + lane];
      a1 += red[(ww * 8 + fr * 2 + 1) * 64 + lane];
    }
    float s = 0.f, ss = 0.f;
#pragma unroll
    for (int i = 0; i < 7; ++i) { s += st[i][0]; ss += st[i][1]; }
    float m, r;
    row_stats(s, ss, m, r);
    const float c = -m * r;
    v4u o;
    s = 0.f; ss = 0.f;
#pragma unroll
    for (int e = 0; e < 4; ++e) {
      const unsigned xm = gate_pair(xr[e], yr[e], r, c);
      const float f0 = e < 2 ? a0[2 * e] : a1[2 * e - 4], f1 = e < 2 ? a0[2 * e + 1] : a1[2 * e - 3];
      o[e] = pk2(bflo(xm) + rbf(f0), bfhi(xm) + rbf(f1));
      s = dot2(o[e], ONES, s);
      ss = dot2(o[e], o[e], ss);
    }
    if (LAST) {
      *(v4u*)(out + (size_t)(Tf * 32 + tokl) * D + js * 64 + fr * 16 + hh * 8) = o;
    } else {
      st128(o, rs, l16, OFF_X2 + (Tf * KS1 + js * 4 + fr) * 1024);
      s += __shfl_xor(s, 32);
      ss += __shfl_xor(ss, 32);
      if (lane < 32) *(f32x2*)(sm + G2_ST + (((w >> 2) * 32 + tokl) * 4 + fr) * 8) = f32x2{s, ss};
      __syncthreads();
      if (tid < 64) {
        const f32x4 p0 = *(const f32x4*)(sm + G2_ST + tid * 32), p1 = *(const f32x4*)(sm + G2_ST + tid * 32 + 16);
        *(f32x2*)(ws + OFF_ST2 + (js * S + tp * 64 + tid) * 8) = f32x2{(p0[0] + p0[2]) + (p1[0] + p1[2]), (p0[1] + p0[3]) + (p1[1] + p1[3])};
      }
    }
  }
}

extern "C" void kernel_launch(void* const* d_in, const int* in_sizes, int n_in, void* d_out, int out_size, void* d_ws,
                              size_t ws_size, hipStream_t stream) {
  const uint16_t* q = (const uint16_t*)d_in[0];
  const uint16_t* k = (const uint16_t*)d_in[1];
  const uint16_t* v = (const uint16_t*)d_in[2];
  const uint16_t* shk = (const uint16_t*)d_in[3];
  const uint16_t* ssk = (const uint16_t*)d_in[4];
  const uint16_t* ssd = (const uint16_t*)d_in[5];
  const uint16_t* wg = (const uint16_t*)d_in[7];
  const uint16_t* wu = (const uint16_t*)d_in[8];
  const uint16_t* wd = (const uint16_t*)d_in[9];
  unsigned char* ws = (unsigned char*)d_ws;
  uint16_t* out = (uint16_t*)d_out;
  (void)hipFuncSetAttribute((const void*)k_conv, hipFuncAttributeMaxDynamicSharedMemorySize, CV_BYTES);
  (void)hipFuncSetAttribute((const void*)k_g1, hipFuncAttributeMaxDynamicSharedMemorySize, G1_BYTES);
  (void)hipFuncSetAttribute((const void*)k_g2<false>, hipFuncAttributeMaxDynamicSharedMemorySize, G2_BYTES);
  (void)hipFuncSetAttribute((const void*)k_g2<true>, hipFuncAttributeMaxDynamicSharedMemorySize, G2_BYTES);
  k_prep<<<PREP_ACT + PREP_W1 + PREP_W2, 512, 0, stream>>>(q, k, v, shk, wg, wu, wd, ws);
  k_conv<<<224, 512, CV_BYTES, stream>>>(ssk, ssd, ws);
  k_g1<<<224, 512, G1_BYTES, stream>>>(ws, OFF_X0, OFF_ST0);
  k_g2<false><<<224, 512, G2_BYTES, stream>>>(ws, OFF_X0, OFF_ST0, out);
  k_g1<<<224, 512, G1_BYTES, stream>>>(ws, OFF_X2, OFF_ST2);
  k_g2<true><<<224, 512, G2_BYTES, stream>>>(ws, OFF_X2, OFF_ST2, out);
}
```

```cpp
#include <hip/hip_runtime.h>
#include <hip/hip_bf16.h>
#include <stdint.h>

#define DI __device__ __forceinline__
#define LAS __attribute__((address_space(3)))
typedef unsigned v4u __attribute__((ext_vector_type(4)));
typedef unsigned v2u __attribute__((ext_vector_type(2)));
typedef float f32x4 __attribute__((ext_vector_type(4)));
typedef float f32x2 __attribute__((ext_vector_type(2)));
typedef float f32x16 __attribute__((ext_vector_type(16)));
typedef short bf16x8 __attribute__((ext_vector_type(8)));
typedef short s16x4 __attribute__((ext_vector_type(4)));
typedef __bf16 bf2 __attribute__((ext_vector_type(2)));
typedef __amdgpu_buffer_rsrc_t rsrc_t;

constexpr int S = 2048, D = 448, F = 1792;
constexpr int KS1 = D / 16;
constexpr int KS2 = F / 16;
constexpr float EPS = 1e-5f;

constexpr int ACT_BYTES = S * D * 2;
constexpr int OFF_X1B = 0;
constexpr int OFF_YB = OFF_X1B + ACT_BYTES;
constexpr int OFF_X3 = OFF_YB + ACT_BYTES;
constexpr int OFF_SP = OFF_X3 + ACT_BYTES;
constexpr int SP_BYTES = 64 * 7 * 32 * 8;
constexpr int FSTR = 32;
constexpr int OFF_FH = OFF_SP + 3 * SP_BYTES;
constexpr int FILL_END = OFF_FH + 2 * 64 * KS2 * FSTR;
constexpr int OFF_H = FILL_END;
constexpr int H_BYTES = 64 * KS2 * 1024;
constexpr int OFF_KVT = OFF_H + 2 * H_BYTES;
constexpr int OFF_X0T = OFF_KVT + ACT_BYTES;
constexpr int OFF_W1 = OFF_X0T + ACT_BYTES;
constexpr int OFF_W2 = OFF_W1 + 112 * KS1 * 1024;
constexpr int OFF_CSP = OFF_W2 + 14 * KS2 * 1024;
constexpr int OFF_ST0 = OFF_CSP + 14 * 112 * 32 * 4;
constexpr int WS_BYTES = OFF_ST0 + S * 8;

constexpr int SC1 = 16;
DI rsrc_t mk_rsrc(const void* p, int bytes) { return __builtin_amdgcn_make_buffer_rsrc((void*)p, 0, bytes, 0x00020000); }
template <int AUX = 0> DI v4u ld128(rsrc_t r, int voff, int soff) { return __builtin_amdgcn_raw_buffer_load_b128(r, voff, soff, AUX); }
template <int AUX = 0> DI v2u ld64(rsrc_t r, int voff, int soff) { return __builtin_amdgcn_raw_buffer_load_b64(r, voff, soff, AUX); }
template <int AUX = 0> DI unsigned ld32(rsrc_t r, int voff, int soff) { return __builtin_amdgcn_raw_buffer_load_b32(r, voff, soff, AUX); }
template <int AUX = 0> DI unsigned ld16(rsrc_t r, int voff, int soff) { return __builtin_amdgcn_raw_buffer_load_b16(r, voff, soff, AUX); }
template <int AUX = 0> DI void st128(v4u v, rsrc_t r, int voff, int soff) {
  __builtin_amdgcn_raw_buffer_store_b128(v, r, voff, soff, AUX);
  asm volatile("s_nop 1" ::"v"(v));
}
template <int AUX = 0> DI void st64(v2u v, rsrc_t r, int voff, int soff) { __builtin_amdgcn_raw_buffer_store_b64(v, r, voff, soff, AUX); }
template <int AUX = 0> DI void st32(unsigned v, rsrc_t r, int voff, int soff) { __builtin_amdgcn_raw_buffer_store_b32(v, r, voff, soff, AUX); }
DI float asf(unsigned u) { return __builtin_bit_cast(float, u); }
DI unsigned asu(float f) { return __builtin_bit_cast(unsigned, f); }
DI float bflo(unsigned u) { return asf(u << 16); }
DI float bfhi(unsigned u) { return asf(u & 0xffff0000u); }
DI unsigned pk2(float a, float b) {
  f32x2 v = {a, b};
  return __builtin_bit_cast(unsigned, __builtin_convertvector(v, bf2));
}
DI float rbf(float a) { return bflo(pk2(a, a)); }
DI float dot2(unsigned a, unsigned b, float c) {
  return __builtin_amdgcn_fdot2_f32_bf16(__builtin_bit_cast(bf2, a), __builtin_bit_cast(bf2, b), c, false);
}
DI f32x16 mfma32(v4u a, v4u b, f32x16 c) {
  return __builtin_amdgcn_mfma_f32_32x32x16_bf16(__builtin_bit_cast(bf16x8, a), __builtin_bit_cast(bf16x8, b), c, 0, 0, 0);
}
DI f32x4 mfma16(v4u a, v4u b, f32x4 c) {
  return __builtin_amdgcn_mfma_f32_16x16x32_bf16(__builtin_bit_cast(bf16x8, a), __builtin_bit_cast(bf16x8, b), c, 0, 0, 0);
}
DI v4u tr_frag(LAS unsigned char* p, int second) {
  const v2u l2 = __builtin_bit_cast(v2u, __builtin_amdgcn_ds_read_tr16_b64_v4i16((LAS s16x4*)p));
  const v2u h2 = __builtin_bit_cast(v2u, __builtin_amdgcn_ds_read_tr16_b64_v4i16((LAS s16x4*)(p + second)));
  return v4u{l2[0], l2[1], h2[0], h2[1]};
}
template <int CTRL> DI float dpp_add(float v) {
  return v + asf(__builtin_amdgcn_update_dpp(0, asu(v), CTRL, 0xf, 0xf, true));
}
DI float row16_sum(float v) {
  v = dpp_add<0x128>(v); v = dpp_add<0x124>(v); v = dpp_add<0x122>(v); v = dpp_add<0x121>(v);
  return v;
}
#define SBAR() __builtin_amdgcn_sched_barrier(0)
#define DRAIN() asm volatile("s_waitcnt vmcnt(0)" ::: "memory")
#define ORDER() __builtin_amdgcn_fence(__ATOMIC_ACQUIRE, "wavefront")
#define STAMPC(kid, i)
#define STAMP(kid, i)
constexpr unsigned ONES = 0x3f803f80u;
constexpr unsigned EMPTY = 0xffffffffu;
#define POLL_SLEEP 2
constexpr unsigned SPIN_CAP = 1u << 16;

DI void row_stats(float s, float ss, float& m, float& r) {
  m = s * (1.0f / D);
  const float var = fmaxf(fmaf(ss, 1.0f / D, -m * m), 0.0f);
  r = __builtin_amdgcn_rsqf(var + EPS);
}
#define PAUX SC1
constexpr int PREP_ACT = 224, PREP_W1 = 196, PREP_W2 = 98, PREP_ST = 64;
static_assert((PREP_ACT + PREP_W1 + PREP_W2 + PREP_ST) * 512 * 32 >= FILL_END, "fill coverage");
__global__ void __launch_bounds__(512) k_prep(const uint16_t* __restrict__ q, const uint16_t* __restrict__ k,
                                              const uint16_t* __restrict__ v, const uint16_t* __restrict__ shk,
                                              const uint16_t* __restrict__ wg, const uint16_t* __restrict__ wu,
                                              const uint16_t* __restrict__ wd, unsigned char* __restrict__ ws) {
  __shared__ __attribute__((aligned(16))) unsigned char sm[2 * 64 * 144];
  const int tid = threadIdx.x, lane = tid & 63, w = tid >> 6;
  int b = blockIdx.x;
  STAMP(0, 0);
  const int i16 = lane & 15, tq = i16 >> 2, tp = i16 & 3, grp = (lane >> 4) & 1, kh = lane >> 5;
  const rsrc_t rs = mk_rsrc(ws, WS_BYTES);
  st128<PAUX>(v4u{EMPTY, EMPTY, EMPTY, EMPTY}, mk_rsrc(ws, FILL_END), (b * 512 + tid) * 16, 0);
  st128<PAUX>(v4u{EMPTY, EMPTY, EMPTY, EMPTY}, mk_rsrc(ws, FILL_END), ((2 * gridDim.x - 1 - b) * 512 + tid) * 16, 0);
  if (b < PREP_ACT) {
    const int tt = b / 7, cg = b % 7;
    const int r = tid >> 3, qq = tid & 7;
    const int tok = tt * 64 + r, c0 = cg * 64 + qq * 8;
    const rsrc_t rk = mk_rsrc(k, ACT_BYTES);
    v4u kk[4];
#pragma unroll
    for (int j = 0; j < 4; ++j) kk[j] = ld128(rk, ((tok - j) * D + c0) * 2, 0);
    const v4u vv = *(const v4u*)(v + (size_t)tok * D + c0);
    const v4u xx = *(const v4u*)(q + (size_t)tok * D + c0);
    v4u sw[4];
#pragma unroll
    for (int j = 0; j < 4; ++j) sw[j] = *(const v4u*)(shk + (size_t)c0 * 4 + j * 8);
    v4u kv;
#pragma unroll
    for (int e2 = 0; e2 < 4; ++e2) {
      const unsigned wa01 = sw[e2][0], wa23 = sw[e2][1], wb01 = sw[e2][2], wb23 = sw[e2][3];
      unsigned p = pk2(bflo(kk[3][e2]) * bfhi(wa23), bfhi(kk[3][e2]) * bfhi(wb23));
      unsigned acc = p;
      p = pk2(bflo(kk[2][e2]) * bflo(wa23), bfhi(kk[2][e2]) * bflo(wb23));
      acc = pk2(bflo(acc) + bflo(p), bfhi(acc) + bfhi(p));
      p = pk2(bflo(kk[1][e2]) * bfhi(wa01), bfhi(kk[1][e2]) * bfhi(wb01));
      acc = pk2(bflo(acc) + bflo(p), bfhi(acc) + bfhi(p));
      p = pk2(bflo(kk[0][e2]) * bflo(wa01), bfhi(kk[0][e2]) * bflo(wb01));
      acc = pk2(bflo(acc) + bflo(p), bfhi(acc) + bfhi(p));
      kv[e2] = pk2(bflo(acc) * bflo(vv[e2]), bfhi(acc) * bfhi(vv[e2]));
    }
    *(v4u*)(sm + (63 - r) * 144 + qq * 16) = kv;
    *(v4u*)(sm + 64 * 144 + r * 144 + qq * 16) = xx;
    __syncthreads();
    {
      const int oct = (w >> 2) * 4 + (lane >> 4), ch = (w & 3) * 16 + i16;
      LAS unsigned char* base = (LAS unsigned char*)sm + (oct * 8 + tq) * 144 + ((w & 3) * 16 + tp * 4) * 2;
      const v4u o0 = tr_frag(base, 4 * 144), o1 = tr_frag(base + 64 * 144, 4 * 144);
      st128(o0, rs, ((cg * 64 + ch) * S + (S - 64 - tt * 64) + oct * 8) * 2, OFF_KVT);
      st128(o1, rs, ((cg * 64 + ch) * S + tt * 64 + oct * 8) * 2, OFF_X0T);
    }
    STAMP(0, 1);
    return;
  }
  b -= PREP_ACT;
  const int chunk = 8 * (tp & 1) + 4 * (tp >> 1);
  if (b < PREP_W1) {
    const int kb = b / 28, fb = b % 28;
    const int r = tid >> 3, qq = tid & 7;
    const size_t src = (size_t)(kb * 64 + r) * F + fb * 64 + qq * 8;
    const v4u a0 = *(const v4u*)(wg + src), a1 = *(const v4u*)(wu + src);
    *(v4u*)(sm + r * 144 + qq * 16) = a0;
    *(v4u*)(sm + 64 * 144 + r * 144 + qq * 16) = a1;
    __syncthreads();
    const int fgl = w & 3, kp = w >> 2;
    float cs = 0.f;
#pragma unroll
    for (int i = 0; i < 2; ++i) {
      const int ksl = kp * 2 + i;
      const v4u o = tr_frag((LAS unsigned char*)sm + grp * (64 * 144) + (ksl * 16 + kh * 8 + tq) * 144 + (fgl * 16 + chunk) * 2, 4 * 144);
      st128<PAUX>(o, rs, (((fb * 4 + fgl) * KS1 + kb * 4 + ksl) * 64 + lane) * 16, OFF_W1);
#pragma unroll
      for (int e = 0; e < 4; ++e) cs = dot2(o[e], ONES, cs);
    }
    cs += __shfl_xor(cs, 32);
    if (lane < 32) *(float*)(ws + OFF_CSP + (((kb * 2 + kp) * 112 + fb * 4 + fgl) * 32 + lane) * 4) = cs;
    STAMP(0, 2);
    return;
  }
  b -= PREP_W1;
  if (b < PREP_W2) {
    const int kb = b / 7, cb = b % 7;
    const int r = tid >> 3, qq = tid & 7;
    const size_t src = (size_t)(kb * 128 + r) * D + cb * 64 + qq * 8;
    const v4u a0 = *(const v4u*)(wd + src), a1 = *(const v4u*)(wd + src + 64 * D);
    *(v4u*)(sm + r * 144 + qq * 16) = a0;
    *(v4u*)(sm + 64 * 144 + r * 144 + qq * 16) = a1;
    __syncthreads();
#pragma unroll
    for (int ct = 0; ct < 2; ++ct) {
      const v4u o = tr_frag((LAS unsigned char*)sm + (w * 16 + kh * 8 + tq) * 144 + (ct * 32 + grp * 16 + chunk) * 2, 4 * 144);
      st128<PAUX>(o, rs, (((cb * 2 + ct) * KS2 + kb * 8 + w) * 64 + lane) * 16, OFF_W2);
    }
    STAMP(0, 3);
    return;
  }
  b -= PREP_W2;
  {
    const int tok = b * 32 + (tid >> 4);
    float s = 0.f, ss = 0.f;
    const rsrc_t rq = mk_rsrc(q, ACT_BYTES);
    v4u xx[4];
#pragma unroll
    for (int i = 0; i < 4; ++i) {
      const int c = (i * 16 + i16) * 8;
      xx[i] = ld128(rq, c < D ? (tok * D + c) * 2 : -16, 0);
    }
#pragma unroll
    for (int i = 0; i < 4; ++i)
#pragma unroll
      for (int e = 0; e < 4; ++e) { s = dot2(xx[i][e], ONES, s); ss = dot2(xx[i][e], xx[i][e], ss); }
    s = row16_sum(s); ss = row16_sum(ss);
    float m, r;
    row_stats(s, ss, m, r);
    if (i16 == 0) *(f32x2*)(ws + OFF_ST0 + (((tok >> 8) * 4 + ((tok >> 1) & 3)) * 32 + ((tok >> 3) & 31)) * 16 + (tok & 1) * 8) = f32x2{r, -m * r};
    STAMP(0, 4);
  }
}

constexpr int NWL = 12;
constexpr int NWR = 28 - NWL;
constexpr int NDROP = 14;
constexpr int XROW = 912;
constexpr int XBUF = 32 * XROW;
constexpr int L_XB = 0, L_ST = 2 * XBUF, L_CS = L_ST + 3 * 32 * 64, L_RED = 0, L_WD = 65536, L_BYTES = L_WD + NWL * 8 * 1024;
static_assert(L_CS + 8 * 128 <= L_WD && L_BYTES <= 163840, "LDS map");
constexpr int CV_HSTR = 4160, CV_USTR = 2304 * 2;
constexpr int CV_H = L_WD, CV_U = CV_H + 2 * 4 * CV_HSTR;
static_assert(CV_U + 2 * CV_USTR <= L_BYTES, "conv LDS map");

#define ABL 0
#define PDV 2
template <int V> struct IC { static constexpr int value = V; };
template <int I, int N, class Fn> DI void static_for(Fn&& fn) {
  if constexpr (I < N) { fn(IC<I>{}); static_for<I + 1, N>(fn); }
}
template <int N> DI void wait_vm() { asm volatile("s_waitcnt vmcnt(%0)" ::"n"(N) : "memory"); }
DI float half_sum(float v) {
  const auto r = __builtin_amdgcn_permlane32_swap(asu(v), asu(v), false, false);
  return asf(r[0]) + asf(r[1]);
}
DI unsigned umax3(unsigned a, unsigned b, unsigned c) { return max(max(a, b), c); }
constexpr int OOB = (int)0x80000000;
DI void raw_barrier() { asm volatile("" ::: "memory"); __builtin_amdgcn_s_barrier(); asm volatile("" ::: "memory"); }
DI int opaque(int v) { asm volatile("" : "+v"(v)); return v; }
DI unsigned nz(unsigned v) { return min(v, EMPTY - 1u); }

constexpr int NWS = 10;
constexpr int wd_slot(int ly, int t, int i) {
  if (ly != 0 || t == 3) return -1;
  constexpr int sl[9] = {0, 2, 9, 16, 18, 20, 22, 24, 26};
  for (int n = 0; n < 9; ++n) if (sl[n] == i && t * 9 + n < NWL + NWS) return t * 9 + n;
  return -1;
}
constexpr int x_slot(int ly, int t, int i) {
  if (t == 3) return -1;
  const int b = ly == 0 ? (t == 0 ? 11 : 4) : (t == 0 ? 23 : 13);
  return (i >= b && i < b + 4) ? i - b : -1;
}
constexpr int BAR = 15 - PDV;
constexpr int H_ST = 8;
constexpr int vm_behind(int ly, int t) {
  int n = 0;
  for (int i = H_ST; i < KS1; ++i) n += (wd_slot(ly, t, i) >= 0) + (x_slot(ly, t, i) >= 0) + (ly == 1 && x_slot(ly, t, i) == 3);
  return n;
}

__global__ void __launch_bounds__(512, 2) k_main(const uint16_t* __restrict__ ssk, const uint16_t* __restrict__ ssd,
                                                 unsigned char* __restrict__ ws, uint16_t* __restrict__ out) {
  extern __shared__ __attribute__((aligned(16))) unsigned char sm[];
  LAS unsigned char* L = (LAS unsigned char*)sm;
  const int tid = threadIdx.x, lane = tid & 63, w = __builtin_amdgcn_readfirstlane(tid >> 6);
  const int xcd = blockIdx.x & 7, li = blockIdx.x >> 3;
  const int g = (xcd >> 1) * 4 + li / 7, hs = xcd & 1, js = li % 7, j = hs * 7 + js;
  const rsrc_t rs = mk_rsrc(ws, WS_BYTES);
  const int l16 = lane * 16, tokl = lane & 31, hh = lane >> 5, i16 = lane & 15;
  const int fg = j * 8 + w;
  const int sj = w < 7 ? w : 6;
  const int w1o = OFF_W1 + fg * KS1 * 1024;
  STAMP(1, 0);

  v4u W1[KS1];
  float csum;
  {
    const int pair = blockIdx.x, c0 = pair * 2;
    const int ch = tid >> 8, i8 = tid & 255;
    const rsrc_t rh = mk_rsrc(ssk, ACT_BYTES);
    const v4u d = ld128(rh, tid * 16, c0 * S * 2);
    const v2u e = ld64(rh, tid * 16 + 16 + c0 * S * 2, 0);
    const v4u u = ld128(rs, tid * 16, OFF_KVT + c0 * S * 2);
    const int fch = (lane >> 2) & 1, oct = (lane >> 3) * 4 + (lane & 3), tok0 = 256 * w + oct * 8;
    const v4u x0 = ld128(rs, (fch * S + tok0) * 2, OFF_X0T + c0 * S * 2);
    v4u s0[4];
#pragma unroll
    for (int i = 0; i < 4; ++i) s0[i] = ld128(rs, oct * 16, OFF_ST0 + (w * 4 + i) * 512);
    const float dcoef = bflo(ssd[c0 + fch]);
    unsigned csp[7];
#pragma unroll
    for (int i = 0; i < 7; ++i) csp[i] = ld32(rs, (hh * 3584 + tokl) * 4, OFF_CSP + (i * 2 * 3584 + fg * 32) * 4);
    SBAR();
#pragma unroll
    for (int i = 0; i < 6; ++i) W1[i] = ld128(rs, l16, w1o + i * 1024);
    SBAR();
    const unsigned a0 = __builtin_amdgcn_alignbit(d[1], d[0], 16), a1 = __builtin_amdgcn_alignbit(d[2], d[1], 16),
                   a2 = __builtin_amdgcn_alignbit(d[3], d[2], 16), a3 = __builtin_amdgcn_alignbit(e[0], d[3], 16),
                   a4 = __builtin_amdgcn_alignbit(e[1], e[0], 16);
    unsigned char* hb = sm + CV_H + ch * 4 * CV_HSTR + 32 + i8 * 16;
    *(v4u*)(hb) = d;
    *(v4u*)(hb + CV_HSTR) = v4u{a0, a1, a2, a3};
    *(v4u*)(hb + 2 * CV_HSTR) = v4u{d[1], d[2], d[3], e[0]};
    *(v4u*)(hb + 3 * CV_HSTR) = v4u{a1, a2, a3, a4};
    *(v4u*)(sm + CV_U + ch * CV_USTR + i8 * 16) = u;
    const unsigned d0 = __builtin_amdgcn_readfirstlane(d[0]), d1 = __builtin_amdgcn_readfirstlane(d[1]);
    if (i8 < 40) {
      const int q8 = i8 - 32, sg = q8 >> 1;
      v4u pv = {0u, 0u, 0u, 0u};
      if (q8 == 3) pv[3] = d0 << 16;
      if (q8 == 5) pv[3] = d0;
      if (q8 == 7) { pv[2] = d0 << 16; pv[3] = __builtin_amdgcn_alignbit(d1, d0, 16); }
      *(v4u*)(sm + (q8 < 0 ? CV_U + ch * CV_USTR + 4096 + i8 * 16 : CV_H + (ch * 4 + sg) * CV_HSTR + (q8 & 1) * 16)) = pv;
    }
    STAMP(1, 1);
    __syncthreads();
    STAMP(1, 2);
    {
      const int cw = w >> 2, sp = w & 3;
      const int gq = lane >> 4;
      const int sg = (1 + i16) & 3;
      const unsigned char* fb = sm + CV_H + (cw * 4 + sg) * CV_HSTR + (1 + i16 + 8 * gq - sg) * 2 + sp * 128;
      const unsigned char* gb = sm + CV_U + cw * CV_USTR + (2032 - 16 * i16 + 8 * gq) * 2 + sp * 128 - 3584;
      auto rdF = [&](int n) -> v4u {
        const v2u lo = *(const v2u*)(fb + (n >> 3) * 64 + (n & 7) * 512), hi = *(const v2u*)(fb + (n >> 3) * 64 + (n & 7) * 512 + 8);
        return v4u{lo[0], lo[1], hi[0], hi[1]};
      };
      f32x4 acc[8];
#pragma unroll
      for (int i = 0; i < 8; ++i) acc[i] = f32x4{0.f, 0.f, 0.f, 0.f};
      v4u Gf[8], Ff[3];
      Ff[0] = rdF(0);
#pragma unroll
      for (int dl = 7; dl >= 0; --dl) Gf[dl] = *(const v4u*)(gb + (7 - dl) * 512);
      Ff[1] = rdF(1);
      SBAR();
      static_for<0, 16>([&](auto N) {
        constexpr int n = N.value, jj = n & 7;
        if constexpr (n + 2 < 16) Ff[(n + 2) % 3] = rdF(n + 2);
#pragma unroll
        for (int dl = 7 - jj; dl >= 0; --dl) acc[jj + dl] = mfma16(Ff[n % 3], Gf[dl], acc[jj + dl]);
        if constexpr (n < 8) Gf[7 - jj] = *(const v4u*)(gb + 64 + jj * 512);
        if constexpr (jj < 7) W1[6 + (n >> 3) * 7 + jj] = ld128(rs, l16, w1o + (6 + (n >> 3) * 7 + jj) * 1024);
        SBAR();
      });
      STAMP(1, 3);
      const int slot = (4 * i16 + gq) ^ ((i16 >> 1) & 3);
#pragma unroll
      for (int i = 0; i < 8; ++i) *(f32x4*)(sm + L_RED + ((w * 8 + i) * 64 + slot) * 16) = acc[i];
    }
    __syncthreads();
    STAMP(1, 4);
    {
      const int m = oct >> 1;
      const int sl0 = (2 * oct) ^ ((m >> 1) & 3), sl1 = (2 * oct + 1) ^ ((m >> 1) & 3);
      f32x4 y0 = {0.f, 0.f, 0.f, 0.f}, y1 = y0;
#pragma unroll
      for (int sp = 0; sp < 4; ++sp) {
        y0 += *(const f32x4*)(sm + L_RED + (((fch * 4 + sp) * 8 + w) * 64 + sl0) * 16);
        y1 += *(const f32x4*)(sm + L_RED + (((fch * 4 + sp) * 8 + w) * 64 + sl1) * 16);
      }
      const v4u ur = *(const v4u*)(sm + CV_U + fch * CV_USTR + (2040 - tok0) * 2);
      v4u yo, xo;
#pragma unroll
      for (int e2 = 0; e2 < 4; ++e2) {
        const float c0v = e2 < 2 ? y0[2 * e2] : y1[2 * e2 - 4], c1v = e2 < 2 ? y0[2 * e2 + 1] : y1[2 * e2 - 3];
        const unsigned cb = pk2(c0v, c1v);
        const unsigned ud = pk2(bfhi(ur[3 - e2]) * dcoef, bflo(ur[3 - e2]) * dcoef);
        const unsigned yy = pk2(bflo(cb) + bflo(ud), bfhi(cb) + bfhi(ud));
        yo[e2] = nz(yy);
        const float xl = bflo(x0[e2]), xh = bfhi(x0[e2]);
        const float tl = fmaf(xl, asf(s0[e2][0]), asf(s0[e2][1])), th = fmaf(xh, asf(s0[e2][2]), asf(s0[e2][3]));
        xo[e2] = nz(pk2(fmaf(tl, bflo(yy), xl), fmaf(th, bfhi(yy), xh)));
      }
      const int vo = (lane >> 3) * (D * 64) + (lane & 7) * 16, so = w * 8 * (D * 64) + c0 * 64;
      STAMP(1, 24);
      st128<SC1>(xo, rs, vo, OFF_X1B + so);
      st128<SC1>(yo, rs, vo, OFF_YB + so);
      float cs = 0.f;
#pragma unroll
      for (int i = 0; i < 7; ++i) cs += asf(csp[i]);
      csum = half_sum(cs);
      SBAR();
    }
    STAMP(1, 5);
  }

  v2u xres[2], yres[2];
  v4u Wd[NWR];
  const int kse = hs * 56 + w * 7, ksl = (hs ^ 1) * 56 + w * 7 - 7;
  auto ks_of = [&](int i) { return (i < 7 ? kse : ksl) + i; };
  auto ld_wd = [&](int f) { return ld128(rs, l16, OFF_W2 + ((js * 2 + (f & 1)) * KS2 + ks_of(f >> 1)) * 1024); };
  const int Ta = g * 4 + hs;

  auto sp_publish = [&](int which, int u) {
    const int ln = opaque(lane);
    const v2u xn = xres[u];
    const float s = row16_sum(dot2(xn[0], ONES, dot2(xn[1], ONES, 0.f))), ss = row16_sum(dot2(xn[0], xn[0], dot2(xn[1], xn[1], 0.f)));
    st64<SC1>(v2u{nz(asu(s)), nz(asu(ss))}, rs, (ln & 15) == 0 ? (ln >> 4) * 8 : OOB, OFF_SP + which * SP_BYTES + ((Ta + 2 * u) * 7 + js) * 256 + w * 32);
  };
  auto sp_load = [&](int which, int u) {
    const int ln = opaque(lane);
    return ld64<SC1>(rs, ((ln & 15) < 7 ? (ln & 15) * 256 : 0) + (ln >> 4) * 8, OFF_SP + which * SP_BYTES + (Ta + 2 * u) * 7 * 256 + w * 32);
  };
  auto sp_stats = [&](int which, int u, v2u sp, float& m, float& r) {
    if (__builtin_expect(!__all(max(sp[0], sp[1]) != EMPTY), 0)) {
      unsigned spins = 0;
#pragma unroll 1
      do { asm volatile("" ::: "memory"); sp = sp_load(which, u); } while (!__all(max(sp[0], sp[1]) != EMPTY) && ++spins < SPIN_CAP);
    }
    row_stats(row16_sum(i16 < 7 ? asf(sp[0]) : 0.f), row16_sum(i16 < 7 ? asf(sp[1]) : 0.f), m, r);
  };
  auto x3_publish = [&](int u, v2u sp) {
    float m, r;
    sp_stats(2, u, sp, m, r);
    const float c = -m * r;
    v2u xn;
#pragma unroll
    for (int e = 0; e < 2; ++e) {
      const float xl = bflo(xres[u][e]), xh = bfhi(xres[u][e]);
      xn[e] = nz(pk2(fmaf(fmaf(xl, r, c), bflo(yres[u][e]), xl), fmaf(fmaf(xh, r, c), bfhi(yres[u][e]), xh)));
    }
    xres[u] = xn;
    st64<SC1>(xn, rs, lane * 8, OFF_X3 + ((Ta + 2 * u) * 7 + js) * 4096 + w * 512);
    sp_publish(1, u);
  };

  v4u xs[4];
  unsigned sst = 0;
  auto x_load = [&](int ly, int t, int i, int mask = 0) {
    const int T = g * 4 + (t ^ hs);
    if (ly == 0) xs[i] = ld128<SC1>(rs, l16 | mask, OFF_X1B + T * (D * 64) + sj * 4096 + i * 1024);
    else {
      xs[i] = ld128<SC1>(rs, l16 | mask, OFF_X3 + (T * 7 + sj) * 4096 + i * 1024);
      if (i == 3) sst = ld32<SC1>(rs, (tokl * 8) | mask, OFF_SP + SP_BYTES + (T * 7 + sj) * 256);
    }
  };

  auto layer = [&](auto LY) {
    constexpr int ly = decltype(LY)::value;
    auto x_ok = [&]() {
      unsigned mx = ly == 0 ? 0u : sst;
#pragma unroll
      for (int i = 0; i < 4; ++i) mx = umax3(umax3(mx, xs[i][0], xs[i][1]), xs[i][2], xs[i][3]);
      return __all(mx != EMPTY);
    };
    auto x_check = [&](int t) {
      if (__builtin_expect(!x_ok(), 0)) {
        unsigned spins = 0;
#pragma unroll 1
        do {
          asm volatile("" ::: "memory");
#pragma unroll
          for (int i = 0; i < 4; ++i) x_load(ly, t, i);
        } while (!x_ok() && ++spins < SPIN_CAP);
      }
    };
    auto st_write = [&](int t, unsigned v) { *(unsigned*)(sm + L_ST + (t % 3) * 1024 + (sj >> 2) * 512 + tokl * 16 + (sj & 3) * 4) = v; };
    auto x_write = [&](int t, int i) {
      if (ly == 0) *(v4u*)(sm + L_XB + (t & 1) * XBUF + sj * 4096 + i * 1024 + l16) = xs[i];
      else {
        *(v4u*)(sm + L_XB + (t & 1) * XBUF + (i * 8 + (lane >> 3)) * XROW + sj * 128 + (lane & 7) * 16) = xs[i];
        if (i == 3) st_write(t, sst);
      }
    };
    const int xrd = ly == 0 ? (hh * 8 + (i16 >> 2)) * 64 + ((lane >> 4) & 1) * 32 + (lane & 3) * 8 : tokl * XROW + hh * 16;
    auto xfrag = [&](int t, int ks) -> v4u {
      if (ly == 0) return tr_frag(L + L_XB + (t & 1) * XBUF + xrd + ks * 1024, 256);
      return *(const v4u*)(sm + L_XB + (t & 1) * XBUF + xrd + ks * 32);
    };
    const int hbase = OFF_H + ly * H_BYTES;
    v4u hf[14];
    auto h_load = [&](int u, int i) { hf[i] = ld128<SC1>(rs, l16, hbase + ((Ta + 2 * u) * KS2 + ks_of(i)) * 1024); };
    auto h_flag = [&](int t) { st32<SC1>(1u, rs, lane == 0 ? 0 : OOB, OFF_FH + ((ly * 64 + g * 4 + (t ^ hs)) * KS2 + fg) * FSTR); };
    auto hf_poll = [&](int u, int half) { return ld32<SC1>(rs, (lane < 7 ? lane : 0) * FSTR, OFF_FH + ((ly * 64 + Ta + 2 * u) * KS2 + ks_of(half * 7)) * FSTR); };
    auto hf_check = [&](int u, int half, unsigned v) {
      if (__builtin_expect(!__all(v == 1u), 0)) {
        unsigned spins = 0;
#pragma unroll 1
        do { asm volatile("" ::: "memory"); v = hf_poll(u, half); } while (!__all(v == 1u) && ++spins < SPIN_CAP);
      }
      ORDER();
    };
    unsigned hpl = 0;

    const int late = sj < 4 ? 0 : 1;
    v2u gat[4], sgr;
    auto g_load = [&](int q) { gat[q] = ld64<SC1>(rs, lane * 64 + w * 8, ((q & 1) ? OFF_YB : OFF_X1B) + (Ta + (q & 2)) * (D * 64) + js * 4096); };
    if (ly == 0) {
#pragma unroll
      for (int i = 0; i < 4; ++i) x_load(0, 0, i);
    }
    x_check(0);
    STAMP(1, 6 + ly * 24);
    __syncthreads();
    {
      const int ln = opaque(lane), csrow = (ln & 16) + 8 * ((ln >> 2) & 1) + (ln & 3) + 4 * ((ln >> 3) & 1);
      *(float*)(sm + L_CS + w * 128 + csrow * 4) = csum;
    }
#pragma unroll
    for (int i = 0; i < 4; ++i) x_write(0, i);
#pragma unroll
    for (int i = 0; i < 4; ++i) x_load(ly, 1, i);
    if (ly == 0) {
#pragma unroll
      for (int i = 20; i < KS1; ++i) W1[i] = ld128(rs, l16, w1o + i * 1024);
    }
    if (ly == 1) sgr = sp_load(2, 1);
    __syncthreads();
    STAMP(1, 7 + ly * 24);
    if (ly == 1) STAMPC(1, 62);

    f32x16 acc[2];
    v4u wtmp[NWL];
    constexpr int PD = PDV;
    v4u xf[PD + 1], ho;
    f32x4 p0, p1;
    float mean = 0.f;
#pragma unroll
    for (int i = 0; i < PD; ++i) xf[i] = xfrag(0, i);
    auto ep_cs = [&](int hq) {
      p0 = *(const f32x4*)(sm + L_CS + w * 128 + hh * 32 + hq * 16);
      p1 = *(const f32x4*)(sm + L_CS + w * 128 + 64 + hh * 32 + hq * 16);
    };
    auto ep_pair = [&](const f32x16& a, int q) {
      float hv[2];
#pragma unroll
      for (int e = 0; e < 2; ++e) {
        const int i = 2 * q + e;
        hv[e] = fmaxf(fmaf(mean, p0[i & 3], a[i]), 0.f) * fmaf(mean, p1[i & 3], a[8 + i]);
        asm volatile("" : "+v"(hv[e]));
      }
      ho[q] = pk2(hv[0], hv[1]);
    };
    auto h_store = [&](int t) { st128<SC1>(ho, rs, l16, hbase + ((g * 4 + (t ^ hs)) * KS2 + fg) * 1024); };

    static_for<0, 4>([&](auto TT) {
      constexpr int t = TT.value;
      v4u sf[2];
      float s = 0.f;
      static_for<0, KS1>([&](auto II) {
        constexpr int i = II.value, n = t * KS1 + i;
        if constexpr (i + PD < KS1) xf[(n + PD) % (PD + 1)] = xfrag(t, i + PD);
        else if constexpr (t < 3) xf[(n + PD) % (PD + 1)] = xfrag(t + 1, i + PD - KS1);
        if constexpr (i == 0) {
          f32x16 z;
#pragma unroll
          for (int e = 0; e < 16; ++e) z[e] = 0.f;
          acc[t & 1] = mfma32(W1[0], xf[n % (PD + 1)], z);
        } else acc[t & 1] = mfma32(W1[i], xf[n % (PD + 1)], acc[t & 1]);
        asm volatile("" : "+v"(acc[t & 1]));
        if constexpr (t > 1 && i == 0) { wait_vm<vm_behind(ly, t - 1)>(); h_flag(t - 2); }
        if constexpr (t > 0) {
          if constexpr (!(ABL & 4)) {
          if constexpr (i == 1) ep_pair(acc[(t - 1) & 1], 0);
          if constexpr (i == 3) { ep_pair(acc[(t - 1) & 1], 1); ep_cs(1); }
          if constexpr (i == 5) ep_pair(acc[(t - 1) & 1], 2);
          if constexpr (i == 7) ep_pair(acc[(t - 1) & 1], 3);
          } else if constexpr (i == 7) ho = xf[0];
          if constexpr (i == H_ST && !(ABL & 8)) h_store(t - 1);
        }
        if constexpr (ly == 0) {
          if constexpr (i == (t == 0 ? 7 : 0)) x_check(t == 0 ? 1 : t + 1 - late);
          if constexpr (t == 0 && i >= 7 && i < 11) x_write(1, i - 7);
          if constexpr (t > 0 && i < 4 && !(ABL & 64)) x_write(t + 1 - late, i);
          if constexpr (i == 4) { sf[0] = xfrag(t, sj * 4); sf[1] = xfrag(t, sj * 4 + 1); }
          if constexpr (i == 8) {
#pragma unroll
            for (int q = 0; q < 8; ++q) s = dot2(sf[q >> 2][q & 3], ONES, s);
            asm volatile("" : "+v"(s));
            sf[0] = xfrag(t, sj * 4 + 2); sf[1] = xfrag(t, sj * 4 + 3);
          }
          if constexpr (i == 11) {
#pragma unroll
            for (int q = 0; q < 8; ++q) s = dot2(sf[q >> 2][q & 3], ONES, s);
            st_write(t, asu(half_sum(s)));
          }
          if constexpr (t == 0 && i >= 25) *(v4u*)(sm + L_WD + (w * NWL + i - 25) * 1024 + l16) = wtmp[i - 25];
          if constexpr (t == 1 && i >= 15 && (i & 1)) *(v4u*)(sm + L_WD + (w * NWL + 3 + (i - 15) / 2) * 1024 + l16) = wtmp[3 + (i - 15) / 2];
          if constexpr (t == 2 && (i == 15 || i == 17)) *(v4u*)(sm + L_WD + (w * NWL + 10 + (i - 15) / 2) * 1024 + l16) = wtmp[10 + (i - 15) / 2];
          if constexpr (t == 3 && i >= 9 && i < 13) g_load(i - 9);
        } else {
          if constexpr (t == 0 && i == 5) x3_publish(1, sgr);
          if constexpr (i == 8 && !(ABL & 2)) x_check(t == 0 ? 1 : t + 1 - late);
          if constexpr ((i == 9 || i == 10) && !(ABL & 2)) { x_write(t == 0 ? 1 : t + 1 - late, 2 * (i - 9)); x_write(t == 0 ? 1 : t + 1 - late, 2 * (i - 9) + 1); }
        }
        constexpr int wf = wd_slot(ly, t, i);
        if constexpr (wf >= NWL) Wd[wf - NWL] = (ABL & 32) ? xf[0] : ld_wd(wf - NWL);
        else if constexpr (wf >= 0 && !(ABL & 32)) wtmp[wf] = ld_wd(NWS + wf);
        if constexpr (x_slot(ly, t, i) >= 0 && !((ABL & 2) && ly == 1) && !((ABL & 64) && ly == 0)) x_load(ly, t + 2 - late, x_slot(ly, t, i), (t == 2 && !late) ? OOB : 0);
        if constexpr (t == 3) {
          if constexpr (i == 1) hpl = hf_poll(0, 0);
          if constexpr (i == 13) { hf_check(0, 0, hpl); hpl = hf_poll(0, 1); }
          if constexpr (i >= 14 && i < 21) h_load(0, i - 14);
          if constexpr (i == 24) hf_check(0, 1, hpl);
          if constexpr (i >= 24) h_load(0, i - 17);
        }
        if constexpr (i == 14) {
          const unsigned char* sp = sm + L_ST + (t % 3) * 1024 + tokl * 16;
          p0 = *(const f32x4*)sp; p1 = *(const f32x4*)(sp + 512);
        }
        if constexpr (i == 17) {
          float a = p0[0] + p0[1], b = p0[2] + p0[3], c = p1[0] + p1[1];
          asm volatile("" : "+v"(a), "+v"(b), "+v"(c));
          mean = ((a + b) + (c + p1[2])) * (-1.0f / D);
          asm volatile("" : "+v"(mean));
        }
        if constexpr (i == 26) ep_cs(0);
        if constexpr (i == BAR && !(ABL & 1)) {
          asm volatile("s_waitcnt lgkmcnt(%0)" ::"n"(ly == 0 ? 4 : 2) : "memory");
          raw_barrier();
        }
        SBAR();
      });
      STAMP(1, 8 + t + ly * 24);
      if (ly == 1 && t == 2) STAMPC(1, 63);
    });
    ep_pair(acc[1], 0); ep_pair(acc[1], 1); ep_cs(1); ep_pair(acc[1], 2); ep_pair(acc[1], 3);
    wait_vm<12>(); h_flag(2);
    h_store(3);
    h_load(0, 11); h_load(0, 12); h_load(0, 13);
#pragma unroll
    for (int i = NWS; i < NWR; ++i) Wd[i] = ld_wd(NWL + i);
    SBAR();
    if constexpr (ly == 0) {
      const int ln = opaque(lane);
      unsigned char* gb = sm + L_XB + w * 2048;
      auto g_ok = [&]() { return __all(umax3(umax3(gat[0][0], gat[0][1], gat[1][0]), umax3(gat[1][1], gat[2][0], gat[2][1]), max(gat[3][0], gat[3][1])) != EMPTY); };
      if (__builtin_expect(!g_ok(), 0)) {
        unsigned spins = 0;
#pragma unroll 1
        do {
          asm volatile("" ::: "memory");
#pragma unroll
          for (int q = 0; q < 4; ++q) g_load(q);
        } while (!g_ok() && ++spins < SPIN_CAP);
      }
#pragma unroll
      for (int q = 0; q < 4; ++q) *(v2u*)(gb + q * 512 + ln * 8) = gat[q];
#pragma unroll
      for (int q = 0; q < 4; ++q) {
        const uint16_t* gp = (const uint16_t*)(gb + q * 512 + (ln & 15) * 32 + (ln >> 4) * 2);
        v2u r = {(unsigned)gp[0] | ((unsigned)gp[4] << 16), (unsigned)gp[8] | ((unsigned)gp[12] << 16)};
        asm volatile("" : "+v"(r));
        if (q & 1) yres[q >> 1] = r; else xres[q >> 1] = r;
      }
      sp_publish(0, 0);
      sp_publish(0, 1);
      SBAR();
    }
    STAMP(1, 12 + ly * 24);

    auto finalize = [&](auto UU, float r2) {
      constexpr int u = UU.value;
      const int T = Ta + 2 * u;
      const int ln = opaque(lane), i16 = ln & 15, ftok = 4 * w + (ln >> 4);
      const int rj = 2 * (i16 >> 2) + (i16 & 1), rslot = ((i16 >> 1) & 1) * 32 + (ftok & 16) + ((ftok + i16) & 15);
      f32x4 a = *(const f32x4*)(sm + L_RED + (rj * 64 + rslot) * 16);
#pragma unroll
      for (int ww = 1; ww < 8; ++ww) a += *(const f32x4*)(sm + L_RED + ((ww * 8 + rj) * 64 + rslot) * 16);
      const unsigned f01 = pk2(a[0] * r2, a[1] * r2), f23 = pk2(a[2] * r2, a[3] * r2);
      const v2u xn = {pk2(bflo(xres[u][0]) + bflo(f01), bfhi(xres[u][0]) + bfhi(f01)), pk2(bflo(xres[u][1]) + bflo(f23), bfhi(xres[u][1]) + bfhi(f23))};
      if constexpr (ly == 1) {
        *(v2u*)(out + (size_t)(T * 32 + ftok) * D + js * 64 + i16 * 4) = xn;
      } else {
        xres[u] = xn;
        sp_publish(2, u);
      }
    };
    v2u sga;
    auto unit = [&](auto UU) {
      constexpr int u = UU.value;
      v4u wl[3];
      v2u sp;
      float r2 = 0.f;
      static_for<0, 28>([&](auto FF) {
        constexpr int f = FF.value;
        if constexpr (f + 2 >= NWS && f + 2 < NWS + NWL) wl[(f + 2) % 3] = *(const v4u*)(sm + L_WD + (w * NWL + (f + 2 - NWS)) * 1024 + l16);
        v4u wf;
        if constexpr (f < NWS) wf = Wd[f]; else if constexpr (f < NWS + NWL) wf = wl[f % 3]; else wf = Wd[f - NWL];
        if constexpr (f < 2) {
          f32x16 z;
#pragma unroll
          for (int e = 0; e < 16; ++e) z[e] = 0.f;
          acc[f] = mfma32(wf, hf[0], z);
        } else acc[f & 1] = mfma32(wf, hf[f >> 1], acc[f & 1]);
        asm volatile("" : "+v"(acc[f & 1]));
        if constexpr (f == 12) sp = sp_load(ly, u);
        if constexpr (u == 0) {
          if constexpr (f == 9) hpl = hf_poll(1, 0);
          if constexpr (f == 11) { wait_vm<3 + NWR - NWS + 1>(); h_flag(3); }
          if constexpr (f == 20) hf_check(1, 0, hpl);
          if constexpr (f >= 20 && f < 27) h_load(1, f - 20);
          if constexpr (f == 27) hpl = hf_poll(1, 1);
        }
        if constexpr (u == 1 && ly == 0) {
          if constexpr (f & 1) W1[KS1 - NDROP + (f >> 1)] = ld128(rs, l16, w1o + (KS1 - NDROP + (f >> 1)) * 1024);
          if constexpr (f == 10) sga = sp_load(2, 0);
          if constexpr (f == 22) x3_publish(0, sga);
        }
        if constexpr (f == 25) {
          float m, r;
          sp_stats(ly, u, sp, m, r);
          r2 = r * r;
          asm volatile("" : "+v"(r2));
        }
        if constexpr (f == 8) __syncthreads();
        SBAR();
      });
      STAMP(1, 13 + u * 5 + ly * 24);
      const int ln = opaque(lane);
#pragma unroll
      for (int c = 0; c < 2; ++c)
#pragma unroll
        for (int qd = 0; qd < 4; ++qd) {
          const int jq = c * 4 + qd;
          const int slot = (ln & 48) + ((ln + 4 * (jq >> 1) + 2 * (ln >> 5) + (jq & 1)) & 15);
          *(f32x4*)(sm + L_RED + ((w * 8 + jq) * 64 + slot) * 16) = f32x4{acc[c][4 * qd], acc[c][4 * qd + 1], acc[c][4 * qd + 2], acc[c][4 * qd + 3]};
        }
      SBAR();
      if constexpr (u == 1 && ly == 0) {
#pragma unroll
        for (int i = 0; i < 4; ++i) x_load(1, 0, i);
      }
      SBAR();
      STAMP(1, 14 + u * 5 + ly * 24);
      __syncthreads();
      STAMP(1, 15 + u * 5 + ly * 24);
      finalize(UU, r2);
      SBAR();
      if constexpr (u == 0) {
        hf_check(1, 1, hpl);
#pragma unroll
        for (int i = 7; i < 14; ++i) h_load(1, i);
        SBAR();
      }
      STAMP(1, 16 + u * 5 + ly * 24);
    };
    unit(IC<0>{});
    unit(IC<1>{});
  };
  layer(IC<0>{});
  layer(IC<1>{});
  STAMP(1, 60);
}

extern "C" void kernel_launch(void* const* d_in, const int* in_sizes, int n_in, void* d_out, int out_size, void* d_ws,
                              size_t ws_size, hipStream_t stream) {
  const uint16_t* q = (const uint16_t*)d_in[0];
  const uint16_t* k = (const uint16_t*)d_in[1];
  const uint16_t* v = (const uint16_t*)d_in[2];
  const uint16_t* shk = (const uint16_t*)d_in[3];
  const uint16_t* ssk = (const uint16_t*)d_in[4];
  const uint16_t* ssd = (const uint16_t*)d_in[5];
  const uint16_t* wg = (const uint16_t*)d_in[7];
  const uint16_t* wu = (const uint16_t*)d_in[8];
  const uint16_t* wd = (const uint16_t*)d_in[9];
  unsigned char* ws = (unsigned char*)d_ws;
  uint16_t* out = (uint16_t*)d_out;
  (void)hipFuncSetAttribute((const void*)k_main, hipFuncAttributeMaxDynamicSharedMemorySize, L_BYTES);
  k_prep<<<PREP_ACT + PREP_W1 + PREP_W2 + PREP_ST, 512, 0, stream>>>(q, k, v, shk, wg, wu, wd, ws);
  k_main<<<224, 512, L_BYTES, stream>>>(ssk, ssd, ws, out);
}
```

```cpp
#include <hip/hip_runtime.h>
#include <hip/hip_bf16.h>
#include <stdint.h>

#define DI __device__ __forceinline__
#define AI __attribute__((always_inline))
#define LAS __attribute__((address_space(3)))
typedef unsigned v4u __attribute__((ext_vector_type(4)));
typedef unsigned v2u __attribute__((ext_vector_type(2)));
typedef float f32x4 __attribute__((ext_vector_type(4)));
typedef float f32x2 __attribute__((ext_vector_type(2)));
typedef float f32x16 __attribute__((ext_vector_type(16)));
typedef short bf16x8 __attribute__((ext_vector_type(8)));
typedef short s16x4 __attribute__((ext_vector_type(4)));
typedef __bf16 bf2 __attribute__((ext_vector_type(2)));
typedef __amdgpu_buffer_rsrc_t rsrc_t;

constexpr int S = 2048, D = 448, F = 1792;
constexpr int KS1 = D / 16;
constexpr int KS2 = F / 16;
constexpr float EPS = 1e-5f;

constexpr int ACT_BYTES = S * D * 2;
constexpr int FSTR = 32;
constexpr int OFF_EP = 0;
constexpr int OFF_FW1 = 256;
constexpr int OFF_FW2 = OFF_FW1 + 112 * 16 * FSTR;
constexpr int OFF_FH = OFF_FW2 + 7 * KS2 * FSTR;
constexpr int OFF_SP = OFF_FH + 2 * 64 * KS2 * FSTR;
constexpr int SP_BYTES = 64 * 7 * 32 * 16;
constexpr int OFF_KVG = OFF_SP + 4 * SP_BYTES;
constexpr int OFF_YG = OFF_KVG + 2 * ACT_BYTES;
constexpr int OFF_X = OFF_YG + 2 * ACT_BYTES;
constexpr int OFF_H = OFF_X + 2 * ACT_BYTES;
constexpr int H_BYTES = 64 * KS2 * 1024;
constexpr int OFF_W1 = OFF_H + 2 * H_BYTES;
constexpr int OFF_W2 = OFF_W1 + 112 * KS1 * 1024;
constexpr int OFF_CSP = OFF_W2 + 14 * KS2 * 1024;
constexpr int WS_BYTES = OFF_CSP + 14 * 112 * 32 * 4;
static_assert(OFF_SP % 128 == 0 && OFF_KVG % 128 == 0, "alignment");

constexpr int SC1 = 16;
DI rsrc_t mk_rsrc(const void* p, int bytes) { return __builtin_amdgcn_make_buffer_rsrc((void*)p, 0, bytes, 0x00020000); }
template <int AUX = 0> DI v4u ld128(rsrc_t r, int voff, int soff) { return __builtin_amdgcn_raw_buffer_load_b128(r, voff, soff, AUX); }
template <int AUX = 0> DI v2u ld64(rsrc_t r, int voff, int soff) { return __builtin_amdgcn_raw_buffer_load_b64(r, voff, soff, AUX); }
template <int AUX = 0> DI unsigned ld32(rsrc_t r, int voff, int soff) { return __builtin_amdgcn_raw_buffer_load_b32(r, voff, soff, AUX); }
template <int AUX = 0> DI unsigned ld16(rsrc_t r, int voff, int soff) { return __builtin_amdgcn_raw_buffer_load_b16(r, voff, soff, AUX); }
template <int AUX = 0> DI void st128(v4u v, rsrc_t r, int voff, int soff) {
  __builtin_amdgcn_raw_buffer_store_b128(v, r, voff, soff, AUX);
  asm volatile("s_nop 1" ::"v"(v));
}
template <int AUX = 0> DI void st64(v2u v, rsrc_t r, int voff, int soff) { __builtin_amdgcn_raw_buffer_store_b64(v, r, voff, soff, AUX); }
template <int AUX = 0> DI void st32(unsigned v, rsrc_t r, int voff, int soff) { __builtin_amdgcn_raw_buffer_store_b32(v, r, voff, soff, AUX); }
DI float asf(unsigned u) { return __builtin_bit_cast(float, u); }
DI unsigned asu(float f) { return __builtin_bit_cast(unsigned, f); }
DI float bflo(unsigned u) { return asf(u << 16); }
DI float bfhi(unsigned u) { return asf(u & 0xffff0000u); }
DI unsigned pk2(float a, float b) {
  f32x2 v = {a, b};
  return __builtin_bit_cast(unsigned, __builtin_convertvector(v, bf2));
}
DI float rbf(float a) { return bflo(pk2(a, a)); }
DI float dot2(unsigned a, unsigned b, float c) {
  return __builtin_amdgcn_fdot2_f32_bf16(__builtin_bit_cast(bf2, a), __builtin_bit_cast(bf2, b), c, false);
}
DI f32x16 mfma32(v4u a, v4u b, f32x16 c) {
  return __builtin_amdgcn_mfma_f32_32x32x16_bf16(__builtin_bit_cast(bf16x8, a), __builtin_bit_cast(bf16x8, b), c, 0, 0, 0);
}
DI f32x4 mfma16(v4u a, v4u b, f32x4 c) {
  return __builtin_amdgcn_mfma_f32_16x16x32_bf16(__builtin_bit_cast(bf16x8, a), __builtin_bit_cast(bf16x8, b), c, 0, 0, 0);
}
DI v4u tr_frag(LAS unsigned char* p, int second) {
  const v2u l2 = __builtin_bit_cast(v2u, __builtin_amdgcn_ds_read_tr16_b64_v4i16((LAS s16x4*)p));
  const v2u h2 = __builtin_bit_cast(v2u, __builtin_amdgcn_ds_read_tr16_b64_v4i16((LAS s16x4*)(p + second)));
  return v4u{l2[0], l2[1], h2[0], h2[1]};
}
template <int CTRL> DI float dpp_add(float v) {
  return v + asf(__builtin_amdgcn_update_dpp(0, asu(v), CTRL, 0xf, 0xf, true));
}
DI float row16_sum(float v) {
  v = dpp_add<0x128>(v); v = dpp_add<0x124>(v); v = dpp_add<0x122>(v); v = dpp_add<0x121>(v);
  return v;
}
#define SBAR() __builtin_amdgcn_sched_barrier(0)
#define DRAIN() asm volatile("s_waitcnt vmcnt(0)" ::: "memory")
#define ORDER() __builtin_amdgcn_fence(__ATOMIC_ACQUIRE, "wavefront")
#define STAMPC(kid, i)
#define STAMP(kid, i)
constexpr unsigned ONES = 0x3f803f80u;
constexpr unsigned EMPTY = 0xffffffffu;
#define POLL_SLEEP 2
constexpr unsigned SPIN_CAP = 1u << 16;

DI void row_stats(float s, float ss, float& m, float& r) {
  m = s * (1.0f / D);
  const float var = fmaxf(fmaf(ss, 1.0f / D, -m * m), 0.0f);
  r = __builtin_amdgcn_rsqf(var + EPS);
}
constexpr int NWL = 12;
constexpr int NWR = 28 - NWL;
constexpr int NDROP = 14;
constexpr int XBUF = D * 64;
constexpr int L_XB = 0, L_ST = 2 * XBUF, L_CS = L_ST + 3 * 1024, L_RED = 0, L_WD = 65536, L_BYTES = L_WD + NWL * 8 * 1024;
static_assert(L_CS + 8 * 128 <= L_WD && L_BYTES <= 163840, "LDS map");
constexpr int CV_HSTR = 4160, CV_USTR = 2304 * 2;
constexpr int CV_H = 0, CV_U = CV_H + 2 * 4 * CV_HSTR, CV_RED = L_WD;
static_assert(CV_U + 2 * CV_USTR <= L_WD && CV_RED + 65536 <= L_BYTES, "conv LDS map");

#define ABL 0
#define PDV 4
template <int V> struct IC { static constexpr int value = V; };
template <int I, int N, class Fn> DI void static_for(Fn&& fn) {
  if constexpr (I < N) { fn(IC<I>{}); static_for<I + 1, N>(fn); }
}
template <int N> DI void wait_vm() { asm volatile("s_waitcnt vmcnt(%0)" ::"n"(N) : "memory"); }
DI float half_sum(float v) {
  const auto r = __builtin_amdgcn_permlane32_swap(asu(v), asu(v), false, false);
  return asf(r[0]) + asf(r[1]);
}
DI unsigned umax3(unsigned a, unsigned b, unsigned c) { return max(max(a, b), c); }
constexpr int OOB = (int)0x80000000;
DI void raw_barrier() { asm volatile("" ::: "memory"); __builtin_amdgcn_s_barrier(); asm volatile("" ::: "memory"); }
template <int AUX = 0> DI v4u ldfrag(rsrc_t r, int l16, int base, int i) { return ld128<AUX>(r, l16, base + i * 1024); }
template <bool DEV, int O0> DI void dma2(rsrc_t r, int l16, int soff, unsigned lds) {
  if constexpr (DEV)
    asm volatile("s_mov_b32 m0, %0\n\ts_nop 0\n\tbuffer_load_dwordx4 %1, %2, %3 offen offset:%4 sc1 lds\n\tbuffer_load_dwordx4 %1, %2, %3 offen offset:%5 sc1 lds"
                 ::"s"(lds), "v"(l16), "s"(r), "s"(soff), "n"(O0), "n"(O0 + 1024) : "m0", "memory");
  else
    asm volatile("s_mov_b32 m0, %0\n\ts_nop 0\n\tbuffer_load_dwordx4 %1, %2, %3 offen offset:%4 lds\n\tbuffer_load_dwordx4 %1, %2, %3 offen offset:%5 lds"
                 ::"s"(lds), "v"(l16), "s"(r), "s"(soff), "n"(O0), "n"(O0 + 1024) : "m0", "memory");
}
DI void dma1(rsrc_t r, int l16, int soff, unsigned lds) {
  asm volatile("s_mov_b32 m0, %0\n\ts_nop 0\n\tbuffer_load_dwordx4 %1, %2, %3 offen sc1 lds" ::"s"(lds), "v"(l16), "s"(r), "s"(soff) : "m0", "memory");
}
DI int opaque(int v) { asm volatile("" : "+v"(v)); return v; }
DI unsigned sopq(unsigned v) { asm volatile("" : "+s"(v)); return v; }
DI unsigned nz(unsigned v) { return min(v, EMPTY - 1u); }

#define NWSV 11
constexpr int NWS = NWSV;
constexpr int BAR = KS1 - 1 - PDV;
constexpr int H_ST = 11;
constexpr int XC = 15;
constexpr int wl_slot(int ly, int t, int i) {
  return (ly == 0 && t < 3 && i >= 16 && i < 24 && !(i & 1)) ? t * 4 + (i - 16) / 2 : -1;
}

#define SPIN(cond_ok, reload) do { if (__builtin_expect(!(cond_ok), 0)) { unsigned spins_ = 0; \
  _Pragma("unroll 1") do { asm volatile("" ::: "memory"); reload; } while (!(cond_ok) && ++spins_ < SPIN_CAP); } } while (0)
constexpr int NAUX = 32;

__global__ void __launch_bounds__(512, 2) k_main(const uint16_t* __restrict__ q, const uint16_t* __restrict__ kin,
                                                 const uint16_t* __restrict__ vin, const uint16_t* __restrict__ shk,
                                                 const uint16_t* __restrict__ ssk, const uint16_t* __restrict__ ssd,
                                                 const uint16_t* __restrict__ wg, const uint16_t* __restrict__ wu,
                                                 const uint16_t* __restrict__ wdn, unsigned char* __restrict__ ws,
                                                 uint16_t* __restrict__ out) {
  extern __shared__ __attribute__((aligned(16))) unsigned char sm[];
  LAS unsigned char* L = (LAS unsigned char*)sm;
  const int tid = threadIdx.x, lane = tid & 63;
  int w_ = __builtin_amdgcn_readfirstlane(tid >> 6);
  asm volatile("" : "+s"(w_));
  const int w = w_;
  const rsrc_t rs = mk_rsrc(ws, WS_BYTES);
  const int l16 = lane * 16, tokl = lane & 31, hh = lane >> 5, i16 = lane & 15;
  STAMP(1, 0);
  const v2u ep = ld64<SC1>(rs, 0, OFF_EP);
  const int tr_off = (hh * 8 + (i16 >> 2)) * 144 + (8 * (i16 & 1) + 4 * ((i16 >> 1) & 1)) * 2;

  if (blockIdx.x < NAUX) {
    const int r = tid >> 3, qq = tid & 7;
    v4u a[4][2];
#pragma unroll
    for (int n = 0; n < 4; ++n) {
      const int b = min((int)blockIdx.x + 32 * n, 97);
      const size_t src = (size_t)((b / 7) * 128 + r) * D + (b % 7) * 64 + qq * 8;
      a[n][0] = *(const v4u*)(wdn + src); a[n][1] = *(const v4u*)(wdn + src + 64 * D);
    }
#pragma unroll
    for (int n = 0; n < 4; ++n) {
      *(v4u*)(sm + n * 18432 + r * 144 + qq * 16) = a[n][0];
      *(v4u*)(sm + n * 18432 + (64 + r) * 144 + qq * 16) = a[n][1];
    }
    __syncthreads();
    const v2u tg = {__builtin_amdgcn_readfirstlane(ep[0]) + 1u, __builtin_amdgcn_readfirstlane(ep[1]) + 0x9e3779b9u};
#pragma unroll
    for (int n = 0; n < 4; ++n) {
      const int b = blockIdx.x + 32 * n, kb = b / 7, cb = b % 7;
#pragma unroll
      for (int ct = 0; ct < 2; ++ct) {
        const v4u o = tr_frag(L + n * 18432 + w * 16 * 144 + tr_off + (ct * 32 + ((lane >> 4) & 1) * 16) * 2, 4 * 144);
        st128<SC1>(o, rs, b < 98 ? l16 : OOB, OFF_W2 + ((cb * 2 + ct) * KS2 + kb * 8 + w) * 1024);
      }
    }
    DRAIN();
    {
      const int b = blockIdx.x + 32 * lane;
      st64<SC1>(tg, rs, (lane < 4 && b < 98) ? ((b % 7) * KS2 + (b / 7) * 8 + w) * FSTR : OOB, OFF_FW2);
    }
    return;
  }

  const int bid = blockIdx.x - NAUX;
  const int xcd = bid & 7, li = bid >> 3;
  const int g = (xcd >> 1) * 4 + li / 7, hs = xcd & 1, js = li % 7, j = hs * 7 + js;
  const int fg = j * 8 + w;
  const int sj = w < 7 ? w : 6;
  const int w1o = OFF_W1 + fg * KS1 * 1024;
  const int Ta = g * 4 + hs * 2;
  const unsigned lds0 = (unsigned)(size_t)L;

  v4u W1[KS1], Wd[NWR];
  float csum;
  v2u xres[2], yres[2];
  unsigned tag0, tag1;
  const int kse = hs * 56 + w * 7, ksl = (hs ^ 1) * 56 + w * 7;
  auto wd_base = [&](int f) AI { return OFF_W2 + ((js * 2 + (f & 1)) * KS2 + (f < 14 ? kse : ksl)) * 1024; };
  auto ld_wd = [&](int f) AI { return ldfrag<SC1>(rs, l16, wd_base(f), (f >> 1) % 7); };
  auto ld_wgt = [&](int n) AI {
    if (n < KS1) W1[n] = ldfrag<SC1>(rs, l16, w1o, n);
    else if (n < KS1 + NWS) Wd[n - KS1] = ld_wd(n - KS1);
  };
  auto tag_ok = [&](v4u v) AI { return __all(((v[1] ^ sopq(tag0)) | (v[3] ^ sopq(tag1))) == 0u); };
  auto flag_ok = [&](v2u v) AI { return __all(((v[0] ^ sopq(tag0)) | (v[1] ^ sopq(tag1))) == 0u); };
  auto sp_publish = [&](int which, int u) AI {
    const int ln = opaque(lane);
    const v2u xn = xres[u];
    const float s = row16_sum(dot2(xn[0], ONES, dot2(xn[1], ONES, 0.f))), ss = row16_sum(dot2(xn[0], xn[0], dot2(xn[1], xn[1], 0.f)));
    st128<SC1>(v4u{asu(s), sopq(tag0), asu(ss), sopq(tag1)}, rs, (ln & 15) == 0 ? ln : OOB, OFF_SP + which * SP_BYTES + ((Ta + u) * 7 + js) * 512 + w * 64);
  };
  auto sp_load = [&](int which, int T) AI {
    const int ln = opaque(lane);
    return ld128<SC1>(rs, ((ln & 15) < 7 ? (ln & 15) * 512 : 0) + (ln & 48), OFF_SP + which * SP_BYTES + T * 7 * 512 + w * 64);
  };
  auto sp_stats = [&](int which, int u, v4u sp, float& m, float& r) AI {
    SPIN(tag_ok(sp), sp = sp_load(which, Ta + u));
    row_stats(row16_sum(i16 < 7 ? asf(sp[0]) : 0.f), row16_sum(i16 < 7 ? asf(sp[2]) : 0.f), m, r);
  };
  auto gate_publish = [&](int ly, int u, float r, float c) AI {
    v2u xn;
#pragma unroll
    for (int e = 0; e < 2; ++e) {
      const float xl = bflo(xres[u][e]), xh = bfhi(xres[u][e]);
      xn[e] = nz(pk2(fmaf(fmaf(xl, r, c), bflo(yres[u][e]), xl), fmaf(fmaf(xh, r, c), bfhi(yres[u][e]), xh)));
    }
    xres[u] = xn;
    const int ln = opaque(lane);
    st64<SC1>(xn, rs, (ln * 8) ^ (((2 * w + (ln >> 5)) & 7) << 4), OFF_X + ly * ACT_BYTES + ((Ta + u) * 7 + js) * 4096 + w * 512);
    sp_publish(2 * ly + 1, u);
  };
  auto x3_publish = [&](int u, v4u sp) AI {
    float m, r;
    sp_stats(2, u, sp, m, r);
    gate_publish(1, u, r, -m * r);
  };

  {
    const int c0 = bid * 2;
    const int r = tid >> 3, qq = tid & 7;
    const int tok = Ta * 32 + r, ca = js * 64 + qq * 8;
    const rsrc_t rk = mk_rsrc(kin, ACT_BYTES), rh = mk_rsrc(ssk, ACT_BYTES);
    v4u kk[4], sw[4];
#pragma unroll
    for (int i = 0; i < 4; ++i) kk[i] = ld128(rk, ((tok - i) * D + ca) * 2, 0);
    const v4u vv = *(const v4u*)(vin + (size_t)tok * D + ca);
#pragma unroll
    for (int i = 0; i < 4; ++i) sw[i] = *(const v4u*)(shk + (size_t)ca * 4 + i * 8);
#pragma unroll
    for (int u = 0; u < 2; ++u) xres[u] = *(const v2u*)(q + (size_t)((Ta + u) * 32 + 4 * w + (lane >> 4)) * D + js * 64 + i16 * 4);
    const int ch = tid >> 8, i8 = tid & 255;
    const v4u d = ld128(rh, tid * 16, c0 * S * 2);
    const v2u e = ld64(rh, tid * 16 + 16 + c0 * S * 2, 0);
    const int wt = min(bid, 195), kb = wt / 28, fb = wt % 28;
    const size_t wsrc = (size_t)(kb * 64 + r) * F + fb * 64 + qq * 8;
    const v4u wa0 = *(const v4u*)(wg + wsrc), wa1 = *(const v4u*)(wu + wsrc);
    const float dcoef = bflo(ssd[c0 + ((lane >> 4) & 1)]);
    SBAR();
#pragma unroll
    for (int ly = 0; ly < 2; ++ly)
      st128<SC1>(v4u{EMPTY, EMPTY, EMPTY, EMPTY}, rs, (tid >> 8) * XBUF + (tid & 255) * 16, OFF_X + ly * ACT_BYTES + (Ta * 7 + js) * 4096);
    SBAR();
    tag0 = __builtin_amdgcn_readfirstlane(ep[0]) + 1u;
    tag1 = __builtin_amdgcn_readfirstlane(ep[1]) + 0x9e3779b9u;
    {
      uint16_t* tl = (uint16_t*)(sm + CV_RED) + qq * 8 * 64 + ((63 - r) ^ (qq << 3));
#pragma unroll
      for (int e2 = 0; e2 < 4; ++e2) {
        const unsigned wa01 = sw[e2][0], wa23 = sw[e2][1], wb01 = sw[e2][2], wb23 = sw[e2][3];
        unsigned p = pk2(bflo(kk[3][e2]) * bfhi(wa23), bfhi(kk[3][e2]) * bfhi(wb23));
        unsigned acc = p;
        p = pk2(bflo(kk[2][e2]) * bflo(wa23), bfhi(kk[2][e2]) * bflo(wb23));
        acc = pk2(bflo(acc) + bflo(p), bfhi(acc) + bfhi(p));
        p = pk2(bflo(kk[1][e2]) * bfhi(wa01), bfhi(kk[1][e2]) * bfhi(wb01));
        acc = pk2(bflo(acc) + bflo(p), bfhi(acc) + bfhi(p));
        p = pk2(bflo(kk[0][e2]) * bflo(wa01), bfhi(kk[0][e2]) * bflo(wb01));
        acc = pk2(bflo(acc) + bflo(p), bfhi(acc) + bfhi(p));
        const unsigned kv = pk2(bflo(acc) * bflo(vv[e2]), bfhi(acc) * bfhi(vv[e2]));
        tl[(2 * e2) * 64] = (uint16_t)kv;
        tl[(2 * e2 + 1) * 64] = (uint16_t)(kv >> 16);
      }
    }
    __syncthreads();
    STAMP(1, 1);
#pragma unroll
    for (int i = 0; i < 2; ++i) {
      const int cl = i * 32 + (tid >> 4);
      const v2u o = *(const v2u*)(sm + CV_RED + cl * 128 + ((i16 ^ ((cl >> 3) << 1)) * 8));
      st128<SC1>(v4u{o[0], sopq(tag0), o[1], sopq(tag1)}, rs, (tid >> 4) * 8192 + i16 * 16, OFF_KVG + (js * 64 + i * 32) * 8192 + (S - 64 - Ta * 32) * 4);
    }
    SBAR();
    {
      const unsigned a0 = __builtin_amdgcn_alignbit(d[1], d[0], 16), a1 = __builtin_amdgcn_alignbit(d[2], d[1], 16),
                     a2 = __builtin_amdgcn_alignbit(d[3], d[2], 16), a3 = __builtin_amdgcn_alignbit(e[0], d[3], 16),
                     a4 = __builtin_amdgcn_alignbit(e[1], e[0], 16);
      unsigned char* hb = sm + CV_H + ch * 4 * CV_HSTR + 32 + i8 * 16;
      *(v4u*)(hb) = d;
      *(v4u*)(hb + CV_HSTR) = v4u{a0, a1, a2, a3};
      *(v4u*)(hb + 2 * CV_HSTR) = v4u{d[1], d[2], d[3], e[0]};
      *(v4u*)(hb + 3 * CV_HSTR) = v4u{a1, a2, a3, a4};
      const unsigned d0 = __builtin_amdgcn_readfirstlane(d[0]), d1 = __builtin_amdgcn_readfirstlane(d[1]);
      if (i8 < 40) {
        const int q8 = i8 - 32, sg = q8 >> 1;
        v4u pv = {0u, 0u, 0u, 0u};
        if (q8 == 3) pv[3] = d0 << 16;
        if (q8 == 5) pv[3] = d0;
        if (q8 == 7) { pv[2] = d0 << 16; pv[3] = __builtin_amdgcn_alignbit(d1, d0, 16); }
        *(v4u*)(sm + (q8 < 0 ? CV_U + ch * CV_USTR + 4096 + i8 * 16 : CV_H + (ch * 4 + sg) * CV_HSTR + (q8 & 1) * 16)) = pv;
      }
      *(v4u*)(sm + CV_RED + 16384 + r * 144 + qq * 16) = wa0;
      *(v4u*)(sm + CV_RED + 16384 + (64 + r) * 144 + qq * 16) = wa1;
    }
    wait_vm<2>();
    __syncthreads();
    sp_publish(0, 0);
    sp_publish(0, 1);
    {
      const int fgl = w & 3, kp = w >> 2, vo = bid < 196 ? l16 : OOB;
      float cs = 0.f;
#pragma unroll
      for (int i = 0; i < 2; ++i) {
        const v4u o = tr_frag(L + CV_RED + 16384 + ((lane >> 4) & 1) * (64 * 144) + (kp * 2 + i) * 16 * 144 + tr_off + fgl * 32, 4 * 144);
        st128<SC1>(o, rs, vo, OFF_W1 + ((fb * 4 + fgl) * KS1 + kb * 4 + kp * 2 + i) * 1024);
#pragma unroll
        for (int e2 = 0; e2 < 4; ++e2) cs = dot2(o[e2], ONES, cs);
      }
      st32<SC1>(asu(half_sum(cs)), rs, (bid < 196 && lane < 32) ? lane * 4 : OOB, OFF_CSP + ((kb * 2 + kp) * 112 + fb * 4 + fgl) * 128);
      DRAIN();
      st64<SC1>(v2u{sopq(tag0), sopq(tag1)}, rs, (bid < 196 && lane == 0) ? 0 : OOB, OFF_FW1 + ((fb * 4 + fgl) * 16 + kb * 2 + kp) * FSTR);
    }
    STAMP(1, 2);
    {
      v4u ug[2];
      auto u_load = [&]() AI { ug[0] = ld128<SC1>(rs, tid * 16, OFF_KVG + c0 * 8192); ug[1] = ld128<SC1>(rs, tid * 16, OFF_KVG + c0 * 8192 + 8192); };
      u_load();
      SPIN(tag_ok(ug[0]) && tag_ok(ug[1]), u_load());
      *(v2u*)(sm + CV_U + tid * 8) = v2u{ug[0][0], ug[0][2]};
      *(v2u*)(sm + CV_U + CV_USTR + tid * 8) = v2u{ug[1][0], ug[1][2]};
    }
    auto fw_poll = [&]() AI {
      const int ln = opaque(lane);
      const int o1 = OFF_FW1 + (fg * 16 + (ln < 14 ? ln : 0)) * FSTR, o2 = OFF_FW2 + (js * KS2 + ((ln & 8) ? ksl : kse) + ((ln & 7) < 7 ? (ln & 7) : 0)) * FSTR;
      return ld64<SC1>(rs, (ln & 16) ? o2 : o1, 0);
    };
    v2u fw = fw_poll();
    v4u sp0 = sp_load(0, Ta), sp1 = sp_load(0, Ta + 1), spo = sp_load(0, Ta ^ 2);
    __syncthreads();
    STAMP(1, 3);
    float r0[2], c0n[2];
    unsigned csp[7];
    {
      const int cw = w >> 2, sp = w & 3;
      const int gq = lane >> 4;
      const int sg = (1 + i16) & 3;
      const unsigned char* fbp = sm + CV_H + (cw * 4 + sg) * CV_HSTR + (1 + i16 + 8 * gq - sg) * 2 + sp * 128;
      const unsigned char* gb = sm + CV_U + cw * CV_USTR + (2032 - 16 * i16 + 8 * gq) * 2 + sp * 128 - 3584;
      auto rdF = [&](int n) AI -> v4u {
        const v2u lo = *(const v2u*)(fbp + (n >> 3) * 64 + (n & 7) * 512), hi = *(const v2u*)(fbp + (n >> 3) * 64 + (n & 7) * 512 + 8);
        return v4u{lo[0], lo[1], hi[0], hi[1]};
      };
      f32x4 acc[8];
#pragma unroll
      for (int i = 0; i < 8; ++i) acc[i] = f32x4{0.f, 0.f, 0.f, 0.f};
      v4u Gf[8], Ff[3];
      Ff[0] = rdF(0);
#pragma unroll
      for (int dl = 7; dl >= 0; --dl) Gf[dl] = *(const v4u*)(gb + (7 - dl) * 512);
      Ff[1] = rdF(1);
      SBAR();
      static_for<0, 16>([&](auto N) AI {
        constexpr int n = N.value, jj = n & 7;
        if constexpr (n + 2 < 16) Ff[(n + 2) % 3] = rdF(n + 2);
#pragma unroll
        for (int dl = 7 - jj; dl >= 0; --dl) { acc[jj + dl] = mfma16(Ff[n % 3], Gf[dl], acc[jj + dl]); asm volatile("" : "+v"(acc[jj + dl])); }
        if constexpr (n < 8) Gf[7 - jj] = *(const v4u*)(gb + 64 + jj * 512);
        if constexpr (n == 2) { SPIN(flag_ok(fw), fw = fw_poll()); ORDER(); }
        if constexpr (n >= 2) ld_wgt(n - 2);
        if constexpr (n >= 8 && n < 12) ld_wgt(n + 6);
        SBAR();
      });
      STAMP(1, 4);
#pragma unroll
      for (int i = 18; i < 24; ++i) ld_wgt(i);
      const int slot = (4 * i16 + gq) ^ ((i16 >> 1) & 3);
#pragma unroll
      for (int i = 0; i < 8; ++i) *(f32x4*)(sm + CV_RED + ((w * 8 + i) * 64 + slot) * 16) = acc[i];
    }
    const int fch = (lane >> 4) & 1;
    v2u ur[2];
#pragma unroll
    for (int i = 0; i < 2; ++i) ur[i] = *(const v2u*)(sm + CV_U + fch * CV_USTR + (2044 - (256 * w + (2 * i + hh) * 64 + i16 * 4)) * 2);
    __syncthreads();
    STAMP(1, 5);
#pragma unroll
    for (int i = 0; i < 2; ++i) {
      const int qi = (2 * i + hh) * 16 + i16, sl = qi ^ ((qi >> 3) & 3);
      f32x4 y = *(const f32x4*)(sm + CV_RED + ((fch * 32 + w) * 64 + sl) * 16);
#pragma unroll
      for (int sp = 1; sp < 4; ++sp) y += *(const f32x4*)(sm + CV_RED + (((fch * 4 + sp) * 8 + w) * 64 + sl) * 16);
      unsigned yy[2];
#pragma unroll
      for (int e2 = 0; e2 < 2; ++e2) {
        const unsigned cb = pk2(y[2 * e2], y[2 * e2 + 1]);
        const unsigned ud = pk2(bfhi(ur[i][1 - e2]) * dcoef, bflo(ur[i][1 - e2]) * dcoef);
        yy[e2] = pk2(bflo(cb) + bflo(ud), bfhi(cb) + bfhi(ud));
      }
      st128<SC1>(v4u{yy[0], sopq(tag0), yy[1], sopq(tag1)}, rs, hh * (D * 256) + tokl * 16, OFF_YG + ((4 * w + 2 * i) * D + c0) * 256);
    }
    SBAR();
    STAMP(1, 24);
#pragma unroll
    for (int i = 24; i < 28; ++i) ld_wgt(i);
#pragma unroll
    for (int i = 0; i < 7; ++i) csp[i] = ld32<SC1>(rs, (hh * 3584 + tokl) * 4, OFF_CSP + (i * 2 * 3584 + fg * 32) * 4);
    { float m; sp_stats(0, 0, sp0, m, r0[0]); c0n[0] = -m * r0[0]; sp_stats(0, 1, sp1, m, r0[1]); c0n[1] = -m * r0[1]; }
    SPIN(tag_ok(spo), spo = sp_load(0, Ta ^ 2));
    SBAR();
    {
      v4u yg[2];
      auto y_load = [&]() AI {
        yg[0] = ld128<SC1>(rs, tid * 16, OFF_YG + ((Ta >> 1) * D + js * 64) * 256);
        yg[1] = ld128<SC1>(rs, tid * 16, OFF_YG + ((Ta >> 1) * D + js * 64) * 256 + 8192);
      };
      y_load();
      SPIN(tag_ok(yg[0]) && tag_ok(yg[1]), y_load());
#pragma unroll
      for (int i = 0; i < 2; ++i) {
        const int cl = i * 32 + (tid >> 4);
        *(v2u*)(sm + L_XB + XBUF + cl * 128 + ((i16 ^ (cl >> 2)) * 8)) = v2u{yg[i][0], yg[i][2]};
      }
      __syncthreads();
      STAMP(1, 25);
#pragma unroll
      for (int u = 0; u < 2; ++u) {
        const uint16_t* gp = (const uint16_t*)(sm + L_XB + XBUF + i16 * 512 + (((u * 8 + w) ^ i16) * 8) + (lane >> 4) * 2);
        v2u yv = {(unsigned)gp[0] | ((unsigned)gp[64] << 16), (unsigned)gp[128] | ((unsigned)gp[192] << 16)};
        asm volatile("" : "+v"(yv));
        yres[u] = yv;
        gate_publish(0, u, r0[u], c0n[u]);
      }
    }
    float cs = 0.f;
#pragma unroll
    for (int i = 0; i < 7; ++i) cs += asf(csp[i]);
    csum = half_sum(cs);
    SBAR();
    STAMP(1, 26);
  }

  v4u xs[4];
  v2u sst;
  auto tile_of = [&](int t) AI { return g * 4 + (((t & 1) ^ hs) << 1) + (t >> 1); };
  auto x_src = [&](int ly, int t) AI { return OFF_X + ly * ACT_BYTES + tile_of(t) * XBUF + sj * 4096; };
  auto s_load = [&](int ly, int t) AI { sst = ld64<SC1>(rs, l16 & 511, OFF_SP + (2 * ly + 1) * SP_BYTES + (tile_of(t) * 7 + sj) * 512); };
  auto x0_load = [&]() AI {
#pragma unroll
    for (int i = 0; i < 4; ++i) xs[i] = ldfrag<SC1>(rs, l16, x_src(1, 0), i);
    s_load(1, 0);
  };

  auto layer = [&](auto LY) AI {
    constexpr int ly = decltype(LY)::value;
    const int lane = opaque(tid & 63), tokl = lane & 31, hh = lane >> 5;
    auto x_ok = [&]() AI {
      unsigned mx = 0u;
#pragma unroll
      for (int i = 0; i < 4; ++i) mx = umax3(umax3(mx, xs[i][0], xs[i][1]), xs[i][2], xs[i][3]);
      return __all(mx != EMPTY && sst[1] == tag0);
    };
    auto x_dma = [&](int t, int half) AI {
      const unsigned dst = lds0 + L_XB + (t & 1) * XBUF + sj * 4096;
      if (half) { dma2<true, 2048>(rs, l16, x_src(ly, t), dst); s_load(ly, t); }
      else dma2<true, 0>(rs, l16, x_src(ly, t), dst);
    };
    auto x_fetch = [&](int t) AI {
      unsigned spins = 0;
      bool ok;
#pragma unroll 1
      do {
        x_dma(t, 0); x_dma(t, 1); DRAIN();
        unsigned mx = 0u;
#pragma unroll
        for (int i = 0; i < 4; i += 2) {
          const v4u a = *(const v4u*)(sm + L_XB + (t & 1) * XBUF + sj * 4096 + i * 1024 + l16), b = *(const v4u*)(sm + L_XB + (t & 1) * XBUF + sj * 4096 + i * 1024 + 1024 + l16);
          mx = umax3(umax3(a[0], a[1], a[2]), umax3(a[3], b[0], b[1]), umax3(b[2], b[3], mx));
          asm volatile("" : "+v"(mx) :: "memory");
        }
        ok = __all(mx != EMPTY && sst[1] == tag0);
      } while (!ok && ++spins < SPIN_CAP);
    };
    auto st_write = [&](int t) AI { *(unsigned*)(sm + L_ST + (t % 3) * 1024 + (sj >> 2) * 512 + tokl * 16 + (sj & 3) * 4) = sst[0]; };
    const int xrd = tokl * 128 + ((hh ^ ((tokl >> 1) & 7)) << 4);
    auto xfrag = [&](int t, int ks) AI -> v4u { return *(const v4u*)(sm + L_XB + (t & 1) * XBUF + (xrd ^ ((ks & 3) << 5)) + (ks >> 2) * 4096); };
    const int hbase = OFF_H + ly * H_BYTES;
    v4u hf[14];
    auto h_load = [&](int u, int i) AI { hf[i] = ldfrag<SC1>(rs, l16, hbase + ((Ta + u) * KS2 + (i < 7 ? kse : ksl)) * 1024, i % 7); };
    auto h_flag = [&](int t) AI { st64<SC1>(v2u{sopq(tag0), sopq(tag1)}, rs, opaque(l16) == 0 ? 0 : OOB, OFF_FH + ((ly * 64 + tile_of(t)) * KS2 + fg) * FSTR); };
    auto hf_poll = [&](int u, int half) AI { return ld64<SC1>(rs, min(opaque(l16) * (FSTR / 16), 6 * FSTR), OFF_FH + ((ly * 64 + Ta + u) * KS2 + (half ? ksl : kse)) * FSTR); };
    auto hf_check = [&](int u, int half, v2u v) AI { SPIN(flag_ok(v), v = hf_poll(u, half)); ORDER(); };
    v2u hpl;

    v4u sgr;
    auto cs_write = [&]() AI {
      const int ln = opaque(lane), csrow = (ln & 16) + 8 * ((ln >> 2) & 1) + (ln & 3) + 4 * ((ln >> 3) & 1);
      *(float*)(sm + L_CS + w * 128 + csrow * 4) = csum;
    };
    if (ly == 0) {
      cs_write();
      x_fetch(0);
      st_write(0);
      STAMP(1, 6);
      __syncthreads();
      x_dma(1, 0); x_dma(1, 1);
    } else {
      SPIN(x_ok(), x0_load());
      STAMP(1, 30);
      __syncthreads();
      cs_write();
#pragma unroll
      for (int i = 0; i < 4; ++i) *(v4u*)(sm + L_XB + sj * 4096 + i * 1024 + l16) = xs[i];
      st_write(0);
      sgr = sp_load(2, Ta + 1);
      x_dma(1, 0); x_dma(1, 1);
      asm volatile("s_waitcnt lgkmcnt(0)" ::: "memory");
      raw_barrier();
    }
    STAMP(1, 7 + ly * 24);
    if (ly == 1) STAMPC(1, 62);

    f32x16 acc[2];
    constexpr int PD = PDV;
    v4u xf[PD], ho;
    f32x4 p0, p1;
    float mean = 0.f, r2[2];
    v4u spr;
#pragma unroll
    for (int i = 0; i < PD; ++i) xf[i] = xfrag(0, i);
    auto ep_cs = [&](int hq) AI {
      p0 = *(const f32x4*)(sm + L_CS + w * 128 + hh * 32 + hq * 16);
      p1 = *(const f32x4*)(sm + L_CS + w * 128 + 64 + hh * 32 + hq * 16);
    };
    float hv0 = 0.f;
    auto ep_one = [&](const f32x16& a, int i) AI {
      float hv = fmaxf(fmaf(mean, p0[i & 3], a[i]), 0.f) * fmaf(mean, p1[i & 3], a[8 + i]);
      asm volatile("" : "+v"(hv));
      if (i & 1) ho[i >> 1] = pk2(hv0, hv); else hv0 = hv;
    };
    auto h_store = [&](int t) AI { st128<SC1>(ho, rs, l16, hbase + (tile_of(t) * KS2 + fg) * 1024); };

    static_for<0, 4>([&](auto TT) AI {
      constexpr int t = TT.value;
      v4u cb0, cb1;
      unsigned cmx = 0u;
      static_for<0, KS1>([&](auto II) AI {
        constexpr int i = II.value, n = t * KS1 + i;
        if constexpr (i == 0) {
          f32x16 z;
#pragma unroll
          for (int e = 0; e < 16; ++e) z[e] = 0.f;
          acc[t & 1] = mfma32(W1[0], xf[n % PD], z);
        } else acc[t & 1] = mfma32(W1[i], xf[n % PD], acc[t & 1]);
        asm volatile("" : "+v"(acc[t & 1]));
        SBAR();
        if constexpr (i + PD < KS1) xf[n % PD] = xfrag(t, i + PD);
        else if constexpr (t < 3) xf[n % PD] = xfrag(t + 1, i + PD - KS1);
        if constexpr (t > 1 && i == 0) { wait_vm<(t == 2 ? 5 : 0) + (ly == 0 ? 4 : 0)>(); h_flag(t - 2); }
        if constexpr (t > 0) {
          if constexpr (i <= 3) ep_one(acc[(t - 1) & 1], i);
          if constexpr (i == 3) ep_cs(1);
          if constexpr (i >= 7 && i <= 10) ep_one(acc[(t - 1) & 1], i - 3);
          if constexpr (i == H_ST) h_store(t - 1);
        }
        if constexpr (t < 3) {
          const unsigned char* xb = sm + L_XB + ((t + 1) & 1) * XBUF + sj * 4096 + l16;
          if constexpr (i == XC) {
            wait_vm<2 * (t > 0) + (t > 1) + (t == 0 ? (ly == 1 ? 2 : NWS) : 0)>();
            asm volatile("" : "+v"(sst));
            cb0 = *(const v4u*)xb; cb1 = *(const v4u*)(xb + 1024);
          }
          if constexpr (i == XC + 3) {
            cmx = umax3(umax3(cb0[0], cb0[1], cb0[2]), umax3(cb0[3], cb1[0], cb1[1]), max(cb1[2], cb1[3]));
            asm volatile("" : "+v"(cmx));
            cb0 = *(const v4u*)(xb + 2048); cb1 = *(const v4u*)(xb + 3072);
          }
          if constexpr (i == XC + 6) {
            cmx = umax3(umax3(cb0[0], cb0[1], cb0[2]), umax3(cb0[3], cb1[0], cb1[1]), umax3(cb1[2], cb1[3], cmx));
            if (__builtin_expect(!__all(cmx != EMPTY && sst[1] == tag0), 0)) x_fetch(t + 1);
            st_write(t + 1);
          }
        }
        if constexpr (ly == 0 && t == 0 && i < NWS) ld_wgt(KS1 + i);
        if constexpr (wl_slot(ly, t, i) >= 0) {
          constexpr int qn = wl_slot(ly, t, i), f = NWS + qn;
          dma1(rs, l16, wd_base(f) + ((f >> 1) % 7) * 1024, lds0 + L_WD + (w * NWL + qn) * 1024);
        }
        if constexpr (ly == 1 && t == 0 && i == 2) x3_publish(1, sgr);
        if constexpr (t < 2 && (i == BAR + 1 || i == BAR + 3)) x_dma(t + 2, (i - BAR - 1) / 2);
        if constexpr ((t == 1 || t == 2) && i == 1) spr = sp_load(2 * ly + 1, Ta + t - 1);
        if constexpr ((t == 1 || t == 2) && i == 13) {
          float m, r;
          sp_stats(2 * ly + 1, t - 1, spr, m, r);
          r2[(t - 1) & 1] = r * r;
          asm volatile("" : "+v"(r2[(t - 1) & 1]));
        }
        if constexpr (t == 2 && i == 24) hpl = hf_poll(0, 0);
        if constexpr (t == 3) {
          if constexpr (i == 4) hf_check(0, 0, hpl);
          if constexpr (i >= 4 && i < 18 && !(i & 1)) h_load(0, (i - 4) / 2);
          if constexpr (i >= 17 && i < 17 + 2 * (NWR - NWS) && (i & 1)) Wd[NWS + (i - 17) / 2] = ld_wd(NWL + NWS + (i - 17) / 2);
          if constexpr (i == 24) hpl = hf_poll(0, 1);
        }
        if constexpr (i == 11) {
          const unsigned char* sp = sm + L_ST + (t % 3) * 1024 + tokl * 16;
          p0 = *(const f32x4*)sp; p1 = *(const f32x4*)(sp + 512);
        }
        if constexpr (i == 14) {
          float a = p0[0] + p0[1], b = p0[2] + p0[3], c = p1[0] + p1[1];
          asm volatile("" : "+v"(a), "+v"(b), "+v"(c));
          mean = ((a + b) + (c + p1[2])) * (-1.0f / D);
          asm volatile("" : "+v"(mean));
        }
        if constexpr (i == 24) ep_cs(0);
        if constexpr (i == BAR && t < 3) {
          asm volatile("s_waitcnt lgkmcnt(2)" ::: "memory");
          raw_barrier();
        }
        SBAR();
      });
      STAMP(1, 8 + t + ly * 24);
      if (ly == 1 && t == 2) STAMPC(1, 63);
    });
#pragma unroll
    for (int i = 0; i < 4; ++i) ep_one(acc[1], i);
    ep_cs(1);
#pragma unroll
    for (int i = 4; i < 8; ++i) ep_one(acc[1], i);
    wait_vm<4 + NWR - NWS>(); h_flag(2);
    h_store(3);
    hf_check(0, 1, hpl);
#pragma unroll
    for (int i = 7; i < 14; ++i) h_load(0, i);
    SBAR();
    STAMP(1, 12 + ly * 24);

    auto finalize = [&](auto UU, float r2) AI {
      constexpr int u = UU.value;
      const int T = Ta + u;
      const int ln = opaque(lane), i16 = ln & 15, ftok = 4 * w + (ln >> 4);
      const int rj = 2 * (i16 >> 2) + (i16 & 1), rslot = ((i16 >> 1) & 1) * 32 + (ftok & 16) + ((ftok + i16) & 15);
      f32x4 a = *(const f32x4*)(sm + L_RED + (rj * 64 + rslot) * 16);
#pragma unroll
      for (int ww = 1; ww < 8; ++ww) a += *(const f32x4*)(sm + L_RED + ((ww * 8 + rj) * 64 + rslot) * 16);
      const unsigned f01 = pk2(a[0] * r2, a[1] * r2), f23 = pk2(a[2] * r2, a[3] * r2);
      const v2u xn = {pk2(bflo(xres[u][0]) + bflo(f01), bfhi(xres[u][0]) + bfhi(f01)), pk2(bflo(xres[u][1]) + bflo(f23), bfhi(xres[u][1]) + bfhi(f23))};
      if constexpr (ly == 1) {
        *(v2u*)(out + (size_t)(T * 32 + ftok) * D + js * 64 + i16 * 4) = xn;
      } else {
        xres[u] = xn;
        sp_publish(2, u);
      }
    };
    v4u sga;
    auto unit = [&](auto UU) AI {
      constexpr int u = UU.value;
      v4u wl[3];
      static_for<0, 28>([&](auto FF) AI {
        constexpr int f = FF.value;
        v4u wf;
        if constexpr (f < NWS) wf = Wd[f]; else if constexpr (f < NWS + NWL) wf = wl[f % 3]; else wf = Wd[f - NWL];
        if constexpr (f < 2) {
          f32x16 z;
#pragma unroll
          for (int e = 0; e < 16; ++e) z[e] = 0.f;
          acc[f] = mfma32(wf, hf[0], z);
        } else acc[f & 1] = mfma32(wf, hf[f >> 1], acc[f & 1]);
        asm volatile("" : "+v"(acc[f & 1]));
        SBAR();
        if constexpr (f + 3 >= NWS && f + 3 < NWS + NWL) wl[f % 3] = *(const v4u*)(sm + L_WD + (w * NWL + (f + 3 - NWS)) * 1024 + l16);
        if constexpr (u == 0) {
          if constexpr (f == 9) hpl = hf_poll(1, 0);
          if constexpr (f == 11) { wait_vm<8>(); h_flag(3); }
          if constexpr (f == 20) hf_check(1, 0, hpl);
          if constexpr (f >= 20 && f < 27) h_load(1, f - 20);
          if constexpr (f == 27) hpl = hf_poll(1, 1);
        }
        if constexpr (u == 1 && ly == 0) {
          if constexpr (f & 1) W1[f >> 1] = ldfrag<SC1>(rs, l16, w1o, f >> 1);
          if constexpr (f == 10) sga = sp_load(2, Ta);
          if constexpr (f == 22) x3_publish(0, sga);
        }
        if constexpr (f == 4) __syncthreads();
        SBAR();
      });
      STAMP(1, 13 + u * 5 + ly * 24);
      const int ln = opaque(lane);
#pragma unroll
      for (int c = 0; c < 2; ++c)
#pragma unroll
        for (int qd = 0; qd < 4; ++qd) {
          const int jq = c * 4 + qd;
          const int slot = (ln & 48) + ((ln + 4 * (jq >> 1) + 2 * (ln >> 5) + (jq & 1)) & 15);
          *(f32x4*)(sm + L_RED + ((w * 8 + jq) * 64 + slot) * 16) = f32x4{acc[c][4 * qd], acc[c][4 * qd + 1], acc[c][4 * qd + 2], acc[c][4 * qd + 3]};
        }
      SBAR();
      if constexpr (u == 1 && ly == 0) x0_load();
      SBAR();
      STAMP(1, 14 + u * 5 + ly * 24);
      __syncthreads();
      STAMP(1, 15 + u * 5 + ly * 24);
      finalize(UU, r2[u]);
      SBAR();
      if constexpr (u == 0) {
        hf_check(1, 1, hpl);
#pragma unroll
        for (int i = 7; i < 14; ++i) h_load(1, i);
        SBAR();
      }
      STAMP(1, 16 + u * 5 + ly * 24);
    };
    unit(IC<0>{});
    unit(IC<1>{});
  };
  layer(IC<0>{});
  layer(IC<1>{});
  st64<SC1>(v2u{sopq(tag0), sopq(tag1)}, rs, (bid == 0 && tid == 0) ? 0 : OOB, OFF_EP);
  STAMP(1, 60);
}

extern "C" void kernel_launch(void* const* d_in, const int* in_sizes, int n_in, void* d_out, int out_size, void* d_ws,
                              size_t ws_size, hipStream_t stream) {
  const uint16_t* q = (const uint16_t*)d_in[0];
  const uint16_t* k = (const uint16_t*)d_in[1];
  const uint16_t* v = (const uint16_t*)d_in[2];
  const uint16_t* shk = (const uint16_t*)d_in[3];
  const uint16_t* ssk = (const uint16_t*)d_in[4];
  const uint16_t* ssd = (const uint16_t*)d_in[5];
  const uint16_t* wg = (const uint16_t*)d_in[7];
  const uint16_t* wu = (const uint16_t*)d_in[8];
  const uint16_t* wd = (const uint16_t*)d_in[9];
  unsigned char* ws = (unsigned char*)d_ws;
  uint16_t* out = (uint16_t*)d_out;
  (void)hipFuncSetAttribute((const void*)k_main, hipFuncAttributeMaxDynamicSharedMemorySize, L_BYTES);
  k_main<<<NAUX + 224, 512, L_BYTES, stream>>>(q, k, v, shk, ssk, ssd, wg, wu, wd, ws, out);
}
```

```cpp
#include <hip/hip_runtime.h>
#include <hip/hip_bf16.h>
#include <stdint.h>

#define DI __device__ __forceinline__
#define AI __attribute__((always_inline))
#define LAS __attribute__((address_space(3)))
typedef unsigned v4u __attribute__((ext_vector_type(4)));
typedef unsigned v2u __attribute__((ext_vector_type(2)));
typedef float f32x4 __attribute__((ext_vector_type(4)));
typedef float f32x2 __attribute__((ext_vector_type(2)));
typedef float f32x16 __attribute__((ext_vector_type(16)));
typedef short bf16x8 __attribute__((ext_vector_type(8)));
typedef short s16x4 __attribute__((ext_vector_type(4)));
typedef __bf16 bf2 __attribute__((ext_vector_type(2)));
typedef __amdgpu_buffer_rsrc_t rsrc_t;

constexpr int S = 2048, D = 448, F = 1792;
constexpr int KS1 = D / 16;
constexpr int KS2 = F / 16;
constexpr float EPS = 1e-5f;

constexpr int ACT_BYTES = S * D * 2;
constexpr int FSTR = 32;
constexpr int OFF_EP = 0;
constexpr int OFF_FW1 = 256;
constexpr int OFF_FW2 = OFF_FW1 + 28 * 32 * 128;
constexpr int OFF_FH = OFF_FW2 + 7 * KS2 * 128;
constexpr int OFF_SP = OFF_FH + 2 * 64 * KS2 * FSTR;
constexpr int SP_BYTES = 64 * 7 * 32 * 16;
constexpr int OFF_KVG = OFF_SP + 4 * SP_BYTES;
constexpr int OFF_X = OFF_KVG + 2 * ACT_BYTES;
constexpr int OFF_H = OFF_X + 2 * ACT_BYTES;
constexpr int H_BYTES = 64 * KS2 * 1024;
constexpr int OFF_W1 = OFF_H + 2 * H_BYTES;
constexpr int OFF_W2 = OFF_W1 + 112 * KS1 * 1024;
constexpr int OFF_CSP = OFF_W2 + 14 * KS2 * 1024;
constexpr int WS_BYTES = OFF_CSP + 28 * 112 * 32 * 4;
static_assert(OFF_SP % 128 == 0 && OFF_KVG % 128 == 0, "alignment");

constexpr int SC1 = 16;
DI rsrc_t mk_rsrc(const void* p, int bytes) { return __builtin_amdgcn_make_buffer_rsrc((void*)p, 0, bytes, 0x00020000); }
template <int AUX = 0> DI v4u ld128(rsrc_t r, int voff, int soff) { return __builtin_amdgcn_raw_buffer_load_b128(r, voff, soff, AUX); }
template <int AUX = 0> DI v2u ld64(rsrc_t r, int voff, int soff) { return __builtin_amdgcn_raw_buffer_load_b64(r, voff, soff, AUX); }
template <int AUX = 0> DI unsigned ld32(rsrc_t r, int voff, int soff) { return __builtin_amdgcn_raw_buffer_load_b32(r, voff, soff, AUX); }
template <int AUX = 0> DI unsigned ld16(rsrc_t r, int voff, int soff) { return __builtin_amdgcn_raw_buffer_load_b16(r, voff, soff, AUX); }
template <int AUX = 0> DI void st128(v4u v, rsrc_t r, int voff, int soff) {
  __builtin_amdgcn_raw_buffer_store_b128(v, r, voff, soff, AUX);
  asm volatile("s_nop 1" ::"v"(v));
}
template <int AUX = 0> DI void st64(v2u v, rsrc_t r, int voff, int soff) { __builtin_amdgcn_raw_buffer_store_b64(v, r, voff, soff, AUX); }
template <int AUX = 0> DI void st32(unsigned v, rsrc_t r, int voff, int soff) { __builtin_amdgcn_raw_buffer_store_b32(v, r, voff, soff, AUX); }
DI float asf(unsigned u) { return __builtin_bit_cast(float, u); }
DI unsigned asu(float f) { return __builtin_bit_cast(unsigned, f); }
DI float bflo(unsigned u) { return asf(u << 16); }
DI float bfhi(unsigned u) { return asf(u & 0xffff0000u); }
DI unsigned pk2(float a, float b) {
  f32x2 v = {a, b};
  return __builtin_bit_cast(unsigned, __builtin_convertvector(v, bf2));
}
DI float rbf(float a) { return bflo(pk2(a, a)); }
DI float dot2(unsigned a, unsigned b, float c) {
  return __builtin_amdgcn_fdot2_f32_bf16(__builtin_bit_cast(bf2, a), __builtin_bit_cast(bf2, b), c, false);
}
DI f32x16 mfma32(v4u a, v4u b, f32x16 c) {
  return __builtin_amdgcn_mfma_f32_32x32x16_bf16(__builtin_bit_cast(bf16x8, a), __builtin_bit_cast(bf16x8, b), c, 0, 0, 0);
}
DI f32x4 mfma16(v4u a, v4u b, f32x4 c) {
  return __builtin_amdgcn_mfma_f32_16x16x32_bf16(__builtin_bit_cast(bf16x8, a), __builtin_bit_cast(bf16x8, b), c, 0, 0, 0);
}
DI v4u tr_frag(LAS unsigned char* p, int second) {
  const v2u l2 = __builtin_bit_cast(v2u, __builtin_amdgcn_ds_read_tr16_b64_v4i16((LAS s16x4*)p));
  const v2u h2 = __builtin_bit_cast(v2u, __builtin_amdgcn_ds_read_tr16_b64_v4i16((LAS s16x4*)(p + second)));
  return v4u{l2[0], l2[1], h2[0], h2[1]};
}
template <int CTRL> DI float dpp_add(float v) {
  return v + asf(__builtin_amdgcn_update_dpp(0, asu(v), CTRL, 0xf, 0xf, true));
}
DI float row16_sum(float v) {
  v = dpp_add<0x128>(v); v = dpp_add<0x124>(v); v = dpp_add<0x122>(v); v = dpp_add<0x121>(v);
  return v;
}
#define SBAR() __builtin_amdgcn_sched_barrier(0)
#define DRAIN() asm volatile("s_waitcnt vmcnt(0)" ::: "memory")
#define ORDER() __builtin_amdgcn_fence(__ATOMIC_ACQUIRE, "wavefront")
#define STAMPC(kid, i)
#define STAMP(kid, i)
constexpr unsigned ONES = 0x3f803f80u;
constexpr unsigned EMPTY = 0xffffffffu;
#define POLL_SLEEP 2
constexpr unsigned SPIN_CAP = 1u << 16;

DI void row_stats(float s, float ss, float& m, float& r) {
  m = s * (1.0f / D);
  const float var = fmaxf(fmaf(ss, 1.0f / D, -m * m), 0.0f);
  r = __builtin_amdgcn_rsqf(var + EPS);
}
constexpr int NWL = 12;
constexpr int NWR = 28 - NWL;
constexpr int NDROP = 14;
constexpr int XBUF = D * 64;
constexpr int L_XB = 0, L_ST = 2 * XBUF, L_CS = L_ST + 3 * 2048, L_RED = 0, L_WD = 65536, L_BYTES = L_WD + NWL * 8 * 1024;
static_assert(L_CS + 8 * 128 <= L_WD && L_BYTES <= 163840, "LDS map");
constexpr int CV_HSTR = 4160, CV_USTR = 2304 * 2;
constexpr int CV_H = 0, CV_U = CV_H + 2 * 4 * CV_HSTR, CV_RED = L_WD, CV_W = 43008;
static_assert(CV_U + 2 * CV_USTR <= CV_W && CV_W + 18432 <= L_WD && CV_RED + 65536 <= L_BYTES, "conv LDS map");

#define ABL 0
#define PDV 4
template <int V> struct IC { static constexpr int value = V; };
template <int I, int N, class Fn> DI void static_for(Fn&& fn) {
  if constexpr (I < N) { fn(IC<I>{}); static_for<I + 1, N>(fn); }
}
template <int N> DI void wait_vm() { asm volatile("s_waitcnt vmcnt(%0)" ::"n"(N) : "memory"); }
DI float half_sum(float v) {
  const auto r = __builtin_amdgcn_permlane32_swap(asu(v), asu(v), false, false);
  return asf(r[0]) + asf(r[1]);
}
DI unsigned umax3(unsigned a, unsigned b, unsigned c) { return max(max(a, b), c); }
constexpr int OOB = (int)0x80000000;
DI void raw_barrier() { asm volatile("" ::: "memory"); __builtin_amdgcn_s_barrier(); asm volatile("" ::: "memory"); }
template <int AUX = 0> DI v4u ldfrag(rsrc_t r, int l16, int base, int i) { return ld128<AUX>(r, l16, base + i * 1024); }
template <bool DEV, int O0> DI void dma2(rsrc_t r, int l16, int soff, unsigned lds) {
  if constexpr (DEV)
    asm volatile("s_mov_b32 m0, %0\n\ts_nop 0\n\tbuffer_load_dwordx4 %1, %2, %3 offen offset:%4 sc1 lds\n\tbuffer_load_dwordx4 %1, %2, %3 offen offset:%5 sc1 lds"
                 ::"s"(lds), "v"(l16), "s"(r), "s"(soff), "n"(O0), "n"(O0 + 1024) : "m0", "memory");
  else
    asm volatile("s_mov_b32 m0, %0\n\ts_nop 0\n\tbuffer_load_dwordx4 %1, %2, %3 offen offset:%4 lds\n\tbuffer_load_dwordx4 %1, %2, %3 offen offset:%5 lds"
                 ::"s"(lds), "v"(l16), "s"(r), "s"(soff), "n"(O0), "n"(O0 + 1024) : "m0", "memory");
}
DI void dma1(rsrc_t r, int l16, int soff, unsigned lds) {
  asm volatile("s_mov_b32 m0, %0\n\ts_nop 0\n\tbuffer_load_dwordx4 %1, %2, %3 offen sc1 lds" ::"s"(lds), "v"(l16), "s"(r), "s"(soff) : "m0", "memory");
}
DI int opaque(int v) { asm volatile("" : "+v"(v)); return v; }
DI unsigned sopq(unsigned v) { asm volatile("" : "+s"(v)); return v; }
DI unsigned nz(unsigned v) { return min(v, EMPTY - 1u); }

#define NWSV 10
constexpr int NWS = NWSV;
constexpr int BAR = KS1 - 1 - PDV;
constexpr int H_ST = 10;
constexpr int XC = 14;
constexpr int pace(int i) {
  constexpr int sl[11] = {0, 3, 5, 8, 11, 13, 16, 19, 21, 24, 26};
  for (int n = 0; n < 11; ++n) if (sl[n] == i) return n;
  return -1;
}
constexpr int wl_slot(int ly, int t, int i) {
  return (ly != 0 || t == 3) ? -1 : i == 16 ? t * 4 : i == 19 ? t * 4 + 1 : i == 23 ? t * 4 + 2 : i == 27 ? t * 4 + 3 : -1;
}

#define SPIN(cond_ok, reload) do { if (__builtin_expect(!(cond_ok), 0)) { unsigned spins_ = 0; \
  _Pragma("unroll 1") do { asm volatile("" ::: "memory"); reload; } while (!(cond_ok) && ++spins_ < SPIN_CAP); } } while (0)
DI unsigned mk_tag1(v2u ep, unsigned tag0) {
  unsigned t = __builtin_amdgcn_readfirstlane(ep[1]) + 0x9e3779b9u;
  if (t - tag0 + 1u <= 2u) t += 0x10001u;
  return t;
}
constexpr int NAUX = 32;
#define X3S 13
#define SGA 17
#define DELAY2 0
#define DELAY3 0
#define FWP 0
#define FWC 5
#define PSLEEP
#define NPRE 18
#define NPRE2 24
#define DELAY1 40
#define LATE_N 0

__global__ void __launch_bounds__(512, 2) k_main(const uint16_t* __restrict__ q, const uint16_t* __restrict__ kin,
                                                 const uint16_t* __restrict__ vin, const uint16_t* __restrict__ shk,
                                                 const uint16_t* __restrict__ ssk, const uint16_t* __restrict__ ssd,
                                                 const uint16_t* __restrict__ wg, const uint16_t* __restrict__ wu,
                                                 const uint16_t* __restrict__ wdn, unsigned char* __restrict__ ws,
                                                 uint16_t* __restrict__ out) {
  extern __shared__ __attribute__((aligned(16))) unsigned char sm[];
  LAS unsigned char* L = (LAS unsigned char*)sm;
  const int tid = threadIdx.x, lane = tid & 63;
  int w_ = __builtin_amdgcn_readfirstlane(tid >> 6);
  asm volatile("" : "+s"(w_));
  const int w = w_;
  const rsrc_t rs = mk_rsrc(ws, WS_BYTES);
  const int l16 = lane * 16, tokl = lane & 31, hh = lane >> 5, i16 = lane & 15;
  STAMP(1, 0);
  const v2u ep = ld64<SC1>(rs, 0, OFF_EP);

  const int tr_off = (hh * 8 + (i16 >> 2)) * 144 + (8 * (i16 & 1) + 4 * ((i16 >> 1) & 1)) * 2;

  if (blockIdx.x < NAUX) {
    const int r = tid >> 3, qq = tid & 7;
    v4u a[4][2];
#pragma unroll
    for (int n = 0; n < 4; ++n) {
      const int b = min((int)blockIdx.x + 32 * n, 97);
      const size_t src = (size_t)((b / 7) * 128 + r) * D + (b % 7) * 64 + qq * 8;
      a[n][0] = *(const v4u*)(wdn + src); a[n][1] = *(const v4u*)(wdn + src + 64 * D);
    }
#pragma unroll
    for (int n = 0; n < 4; ++n) {
      *(v4u*)(sm + n * 18432 + r * 144 + qq * 16) = a[n][0];
      *(v4u*)(sm + n * 18432 + (64 + r) * 144 + qq * 16) = a[n][1];
    }
    __syncthreads();
    const unsigned tg0 = __builtin_amdgcn_readfirstlane(ep[0]) + 1u;
    const v2u tg = {tg0, mk_tag1(ep, tg0)};
#pragma unroll
    for (int n = 0; n < 4; ++n) {
      const int b = blockIdx.x + 32 * n, kb = b / 7, cb = b % 7;
#pragma unroll
      for (int ct = 0; ct < 2; ++ct) {
        const v4u o = tr_frag(L + n * 18432 + w * 16 * 144 + tr_off + (ct * 32 + ((lane >> 4) & 1) * 16) * 2, 4 * 144);
        st128<SC1>(o, rs, b < 98 ? l16 : OOB, OFF_W2 + ((cb * 2 + ct) * KS2 + kb * 8 + w) * 1024);
      }
    }
    DRAIN();
    {
      const int b = blockIdx.x + 32 * (lane >> 4);
      st64<SC1>(tg, rs, b < 98 ? ((b % 7) * KS2 + (b / 7) * 8 + w) * 128 + i16 * 8 : OOB, OFF_FW2);
    }
    return;
  }

  const int bid = blockIdx.x - NAUX;
  const int xcd = bid & 7, li = bid >> 3;
  const int g = (xcd >> 1) * 4 + li / 7, hs = xcd & 1, js = li % 7, j = hs * 7 + js;
  const int fg = j * 8 + w;
  const int sj = w < 7 ? w : 6;
  const int w1o = OFF_W1 + fg * KS1 * 1024;
  const int Ta = g * 4 + hs * 2;
  const unsigned lds0 = (unsigned)(size_t)L;

  v4u W1[KS1], Wd[NWR];
  float csum;
  v2u xres[2], yres[2];
  unsigned tag0, tag1;
  const int kse = hs * 56 + w * 7, ksl = (hs ^ 1) * 56 + w * 7;
  auto wd_base = [&](int f) AI { return OFF_W2 + ((js * 2 + (f & 1)) * KS2 + (f < 14 ? kse : ksl)) * 1024; };
  auto ld_wd = [&](int f) AI { return ldfrag<SC1>(rs, l16, wd_base(f), (f >> 1) % 7); };
  auto ld_wgt = [&](int n) AI {
    if (n < KS1) W1[n] = ldfrag<SC1>(rs, l16, w1o, n);
    else if (n < KS1 + NWS) Wd[n - KS1] = ld_wd(n - KS1);
  };
  auto tag_ok = [&](v4u v) AI { return __all(((v[1] ^ sopq(tag0)) | (v[3] ^ sopq(tag1))) == 0u); };
  auto flag_ok = [&](v2u v) AI { return __all(((v[0] ^ sopq(tag0)) | (v[1] ^ sopq(tag1))) == 0u); };
  auto sp_publish = [&](int which, int u) AI {
    const int ln = opaque(lane);
    const v2u xn = xres[u];
    const float s = row16_sum(dot2(xn[0], ONES, dot2(xn[1], ONES, 0.f))), ss = row16_sum(dot2(xn[0], xn[0], dot2(xn[1], xn[1], 0.f)));
    st128<SC1>(v4u{asu(s), sopq(tag0), asu(ss), sopq(tag1)}, rs, (ln & 15) == 0 ? ln : OOB, OFF_SP + which * SP_BYTES + ((Ta + u) * 7 + js) * 512 + w * 64);
  };
  auto sp_load = [&](int which, int T) AI {
    const int ln = opaque(lane);
    return ld128<SC1>(rs, ((ln & 15) < 7 ? (ln & 15) * 512 : 0) + (ln & 48), OFF_SP + which * SP_BYTES + T * 7 * 512 + w * 64);
  };
  auto sp_stats = [&](int which, int u, v4u sp, float& m, float& r) AI {
    SPIN(tag_ok(sp), sp = sp_load(which, Ta + u));
    row_stats(row16_sum(i16 < 7 ? asf(sp[0]) : 0.f), row16_sum(i16 < 7 ? asf(sp[2]) : 0.f), m, r);
  };
  auto gate_publish = [&](int ly, int u, float r, float c) AI {
    v2u xn;
#pragma unroll
    for (int e = 0; e < 2; ++e) {
      const float xl = bflo(xres[u][e]), xh = bfhi(xres[u][e]);
      xn[e] = nz(pk2(fmaf(fmaf(xl, r, c), bflo(yres[u][e]), xl), fmaf(fmaf(xh, r, c), bfhi(yres[u][e]), xh)));
    }
    xres[u] = xn;
    const int ln = opaque(lane);
    st64<SC1>(xn, rs, (ln * 8) ^ (((2 * w + (ln >> 5)) & 7) << 4), OFF_X + ly * ACT_BYTES + ((Ta + u) * 7 + js) * 4096 + w * 512);
    sp_publish(2 * ly + 1, u);
  };
  auto x3_publish = [&](int u, v4u sp) AI {
    float m, r;
    sp_stats(2, u, sp, m, r);
    gate_publish(1, u, r, -m * r);
  };

  {
    const int c0 = bid * 2;
    const int r = tid >> 3, qq = tid & 7;
    const int tok = Ta * 32 + r, ca = js * 64 + qq * 8;
    const rsrc_t rk = mk_rsrc(kin, ACT_BYTES), rh = mk_rsrc(ssk, ACT_BYTES);
    v4u kk[4], sw[4];
    const v4u swl = *(const v4u*)(shk + js * 256 + tokl * 8);
#pragma unroll
    for (int i = 0; i < 4; ++i) kk[i] = ld128(rk, ((tok - i) * D + ca) * 2, 0);
    const v4u vv = *(const v4u*)(vin + (size_t)tok * D + ca);
    const int ch = tid >> 8, i8 = tid & 255;
    const v4u d = ld128(rh, tid * 16, c0 * S * 2);
    const v2u e = ld64(rh, tid * 16 + 16 + c0 * S * 2, 0);
    STAMP(1, 28);
    SBAR();
    *(v4u*)(sm + CV_RED + 16384 + w * 1024 + l16) = swl;
#pragma unroll
    for (int i = 0; i < 4; ++i) sw[i] = *(const v4u*)(sm + CV_RED + 16384 + w * 1024 + qq * 64 + i * 16);
    tag0 = __builtin_amdgcn_readfirstlane(ep[0]) + 1u;
    tag1 = mk_tag1(ep, tag0);
    {
      uint16_t* tl = (uint16_t*)(sm + CV_RED) + qq * 8 * 64 + ((63 - r) ^ (qq << 3));
#pragma unroll
      for (int e2 = 0; e2 < 4; ++e2) {
        const unsigned wa01 = sw[e2][0], wa23 = sw[e2][1], wb01 = sw[e2][2], wb23 = sw[e2][3];
        unsigned p = pk2(bflo(kk[3][e2]) * bfhi(wa23), bfhi(kk[3][e2]) * bfhi(wb23));
        unsigned acc = p;
        p = pk2(bflo(kk[2][e2]) * bflo(wa23), bfhi(kk[2][e2]) * bflo(wb23));
        acc = pk2(bflo(acc) + bflo(p), bfhi(acc) + bfhi(p));
        p = pk2(bflo(kk[1][e2]) * bfhi(wa01), bfhi(kk[1][e2]) * bfhi(wb01));
        acc = pk2(bflo(acc) + bflo(p), bfhi(acc) + bfhi(p));
        p = pk2(bflo(kk[0][e2]) * bflo(wa01), bfhi(kk[0][e2]) * bflo(wb01));
        acc = pk2(bflo(acc) + bflo(p), bfhi(acc) + bfhi(p));
        const unsigned kv = pk2(bflo(acc) * bflo(vv[e2]), bfhi(acc) * bfhi(vv[e2]));
        tl[(2 * e2) * 64] = (uint16_t)kv;
        tl[(2 * e2 + 1) * 64] = (uint16_t)(kv >> 16);
      }
    }
    SBAR();
    const int wt = min(bid, 195), kb = wt / 28, fb = wt % 28, ks = kb * 4 + (w & 3), rl = lane >> 3;
    v4u wa[4];
    {
      const rsrc_t rg = mk_rsrc(wg, D * F * 2), ru = mk_rsrc(wu, D * F * 2);
      const int vo = w < 4 ? OOB : ((ks * 16 + rl) * F + fb * 64 + qq * 8) * 2;
      wa[0] = ld128(rg, vo, 0); wa[1] = ld128(rg, vo, 16 * F); wa[2] = ld128(ru, vo, 0); wa[3] = ld128(ru, vo, 16 * F);
    }
    SBAR();
    STAMP(1, 29);
    __syncthreads();
    STAMP(1, 1);
#pragma unroll
    for (int i = 0; i < 2; ++i) {
      const int cl = i * 32 + (tid >> 4);
      const v2u o = *(const v2u*)(sm + CV_RED + cl * 128 + ((i16 ^ ((cl >> 3) << 1)) * 8));
      st128<SC1>(v4u{o[0], sopq(tag0), o[1], sopq(tag1)}, rs, (tid >> 4) * 8192 + i16 * 16, OFF_KVG + (js * 64 + i * 32) * 8192 + (S - 64 - Ta * 32) * 4);
    }
    SBAR();
    {
      const unsigned a0 = __builtin_amdgcn_alignbit(d[1], d[0], 16), a1 = __builtin_amdgcn_alignbit(d[2], d[1], 16),
                     a2 = __builtin_amdgcn_alignbit(d[3], d[2], 16), a3 = __builtin_amdgcn_alignbit(e[0], d[3], 16),
                     a4 = __builtin_amdgcn_alignbit(e[1], e[0], 16);
      unsigned char* hb = sm + CV_H + ch * 4 * CV_HSTR + 32 + i8 * 16;
      *(v4u*)(hb) = d;
      *(v4u*)(hb + CV_HSTR) = v4u{a0, a1, a2, a3};
      *(v4u*)(hb + 2 * CV_HSTR) = v4u{d[1], d[2], d[3], e[0]};
      *(v4u*)(hb + 3 * CV_HSTR) = v4u{a1, a2, a3, a4};
      const unsigned d0 = __builtin_amdgcn_readfirstlane(d[0]), d1 = __builtin_amdgcn_readfirstlane(d[1]);
      if (i8 < 40) {
        const int q8 = i8 - 32, sg = q8 >> 1;
        v4u pv = {0u, 0u, 0u, 0u};
        if (q8 == 3) pv[3] = d0 << 16;
        if (q8 == 5) pv[3] = d0;
        if (q8 == 7) { pv[2] = d0 << 16; pv[3] = __builtin_amdgcn_alignbit(d1, d0, 16); }
        *(v4u*)(sm + (q8 < 0 ? CV_U + ch * CV_USTR + 4096 + i8 * 16 : CV_H + (ch * 4 + sg) * CV_HSTR + (q8 & 1) * 16)) = pv;
      }
    }
    STAMP(1, 2);
    float dcoef;
    auto own_load = [&]() AI {
#pragma unroll
      for (int u = 0; u < 2; ++u) xres[u] = *(const v2u*)(q + (size_t)((Ta + u) * 32 + 4 * w + (lane >> 4)) * D + js * 64 + i16 * 4);
      dcoef = bflo(ssd[c0 + ((lane >> 4) & 1)]);
    };
    if (w < 4) {
      v4u ug[4];
      auto u_load = [&]() AI {
#pragma unroll
        for (int i = 0; i < 4; ++i) ug[i] = ld128<SC1>(rs, tid * 16, OFF_KVG + c0 * 8192 + i * 4096);
      };
      __builtin_amdgcn_s_sleep(DELAY1);
      u_load();
      SPIN(tag_ok(ug[0]) && tag_ok(ug[1]) && tag_ok(ug[2]) && tag_ok(ug[3]), u_load());
#pragma unroll
      for (int i = 0; i < 4; ++i) *(v2u*)(sm + CV_U + (i >> 1) * CV_USTR + (i & 1) * 2048 + tid * 8) = v2u{ug[i][0], ug[i][2]};
      own_load();
    } else {
      own_load();
      unsigned char* wb = sm + CV_W + (w - 4) * (32 * 144) + rl * 144 + qq * 16;
      *(v4u*)wb = wa[0]; *(v4u*)(wb + 8 * 144) = wa[1]; *(v4u*)(wb + 16 * 144) = wa[2]; *(v4u*)(wb + 24 * 144) = wa[3];
#pragma unroll
      for (int fgl = 0; fgl < 4; ++fgl) {
        const v4u o = tr_frag(L + CV_W + (w - 4) * (32 * 144) + ((lane >> 4) & 1) * (16 * 144) + tr_off + fgl * 32, 4 * 144);
        st128<SC1>(o, rs, bid < 196 ? l16 : OOB, OFF_W1 + ((fb * 4 + fgl) * KS1 + ks) * 1024);
        const float cs = dot2(o[3], ONES, dot2(o[2], ONES, dot2(o[1], ONES, dot2(o[0], ONES, 0.f))));
        st32<SC1>(asu(half_sum(cs)), rs, (bid < 196 && lane < 32) ? lane * 4 : OOB, OFF_CSP + (ks * 112 + fb * 4 + fgl) * 128);
      }
      DRAIN();
      st64<SC1>(v2u{sopq(tag0), sopq(tag1)}, rs, (bid < 196 && lane < 16) ? lane * 8 : OOB, OFF_FW1 + (fb * 32 + ks) * 128);
    }
    STAMP(1, 22);
    auto fw_poll = [&]() AI {
      const int ln = opaque(lane);
      const int o1 = OFF_FW1 + ((fg >> 2) * 32 + min(ln, 27)) * 128, o2 = OFF_FW2 + (js * KS2 + ((ln & 8) ? ksl : kse) + min(ln & 7, 6)) * 128;
      return ld64<SC1>(rs, (ln & 32) ? o2 : o1, 0);
    };
    __syncthreads();
    STAMP(1, 3);
    float r0[2], c0n[2];
    v2u fw;
    {
      const int cw = w >> 2, sp = w & 3;
      const int gq = lane >> 4;
      const int sg = (1 + i16) & 3;
      const unsigned char* fbp = sm + CV_H + (cw * 4 + sg) * CV_HSTR + (1 + i16 + 8 * gq - sg) * 2 + sp * 128;
      const unsigned char* gb = sm + CV_U + cw * CV_USTR + (2032 - 16 * i16 + 8 * gq) * 2 + sp * 128 - 3584;
      auto rdF = [&](int n) AI -> v4u {
        const v2u lo = *(const v2u*)(fbp + (n >> 3) * 64 + (n & 7) * 512), hi = *(const v2u*)(fbp + (n >> 3) * 64 + (n & 7) * 512 + 8);
        return v4u{lo[0], lo[1], hi[0], hi[1]};
      };
      f32x4 acc[8];
#pragma unroll
      for (int i = 0; i < 8; ++i) acc[i] = f32x4{0.f, 0.f, 0.f, 0.f};
      v4u Gf[8], Ff[3];
      Ff[0] = rdF(0);
#pragma unroll
      for (int dl = 7; dl >= 0; --dl) Gf[dl] = *(const v4u*)(gb + (7 - dl) * 512);
      Ff[1] = rdF(1);
      SBAR();
      static_for<0, 16>([&](auto N) AI {
        constexpr int n = N.value, jj = n & 7;
        if constexpr (n + 2 < 16) Ff[(n + 2) % 3] = rdF(n + 2);
#pragma unroll
        for (int dl = 7 - jj; dl >= 0; --dl) { acc[jj + dl] = mfma16(Ff[n % 3], Gf[dl], acc[jj + dl]); asm volatile("" : "+v"(acc[jj + dl])); }
        if constexpr (n < 8) Gf[7 - jj] = *(const v4u*)(gb + 64 + jj * 512);
        if constexpr (n == FWP) fw = fw_poll();
        if constexpr (n == FWC) { SPIN(flag_ok(fw), fw = fw_poll()); ORDER(); }
        if constexpr (n >= FWC) ld_wgt(n - FWC);
        const v4u ev = {EMPTY, EMPTY, EMPTY, EMPTY};
        if constexpr (n == 1) st128<SC1>(ev, rs, (tid >> 8) * XBUF + (tid & 255) * 16, OFF_X + (Ta * 7 + js) * 4096);
        if constexpr (n == 2)
          st128<SC1>(ev, rs, w < 4 ? OFF_SP + w * SP_BYTES + ((Ta + hh) * 7 + js) * 512 + tokl * 16
                           : w < 6 ? OFF_FH + (((w - 4) * 64 + g * 4 + (lane >> 4)) * KS2 + j * 8) * FSTR + i16 * 16 : OOB, 0);
        SBAR();
      });
      STAMP(1, 4);
      const int slot = (4 * i16 + gq) ^ ((i16 >> 1) & 3);
#pragma unroll
      for (int i = 0; i < 8; ++i) *(f32x4*)(sm + CV_RED + ((w * 8 + i) * 64 + slot) * 16) = acc[i];
    }
    const int fch = (lane >> 4) & 1;
    v2u ur[2];
#pragma unroll
    for (int i = 0; i < 2; ++i) ur[i] = *(const v2u*)(sm + CV_U + fch * CV_USTR + (2044 - (256 * w + (2 * i + hh) * 64 + i16 * 4)) * 2);
    wait_vm<16 - FWC>();
    __syncthreads();
    STAMP(1, 5);
#pragma unroll
    for (int i = 0; i < 2; ++i) {
      const int qi = (2 * i + hh) * 16 + i16, sl = qi ^ ((qi >> 3) & 3);
      f32x4 y = *(const f32x4*)(sm + CV_RED + ((fch * 32 + w) * 64 + sl) * 16);
#pragma unroll
      for (int sp = 1; sp < 4; ++sp) y += *(const f32x4*)(sm + CV_RED + (((fch * 4 + sp) * 8 + w) * 64 + sl) * 16);
      unsigned yy[2];
#pragma unroll
      for (int e2 = 0; e2 < 2; ++e2) {
        const unsigned cb = pk2(y[2 * e2], y[2 * e2 + 1]);
        const unsigned ud = pk2(bfhi(ur[i][1 - e2]) * dcoef, bflo(ur[i][1 - e2]) * dcoef);
        yy[e2] = pk2(bflo(cb) + bflo(ud), bfhi(cb) + bfhi(ud));
      }
      st128<SC1>(v4u{yy[0], sopq(tag1), yy[1], sopq(tag0)}, rs, fch * 8192 + hh * 256 + i16 * 16, OFF_KVG + c0 * 8192 + (4 * w + 2 * i) * 256);
    }
    SBAR();
    STAMP(1, 24);
    sp_publish(0, 0);
    sp_publish(0, 1);
    raw_barrier();
#pragma unroll
    for (int i = 16 - FWC; i < NPRE; ++i) ld_wgt(i);
    SBAR();
    STAMP(1, 23);
    {
      v4u yg[2];
      auto y_load = [&]() AI {
        yg[0] = ld128<SC1>(rs, (tid >> 4) * 8192 + i16 * 16, OFF_KVG + js * 64 * 8192 + (Ta >> 1) * 256);
        yg[1] = ld128<SC1>(rs, (tid >> 4) * 8192 + i16 * 16, OFF_KVG + (js * 64 + 32) * 8192 + (Ta >> 1) * 256);
      };
      __builtin_amdgcn_s_sleep(DELAY2);
      y_load();
      v4u sp0 = sp_load(0, Ta), sp1 = sp_load(0, Ta + 1), spo = sp_load(0, Ta ^ 2);
      SPIN(__all(((yg[0][1] ^ tag1) | (yg[0][3] ^ tag0) | (yg[1][1] ^ tag1) | (yg[1][3] ^ tag0)) == 0u), y_load());
      STAMP(1, 27);
#pragma unroll
      for (int i = 0; i < 2; ++i) {
        const int cl = i * 32 + (tid >> 4);
        *(v2u*)(sm + L_XB + XBUF + cl * 128 + ((i16 ^ (cl >> 2)) * 8)) = v2u{yg[i][0], yg[i][2]};
      }
      { float m; sp_stats(0, 0, sp0, m, r0[0]); c0n[0] = -m * r0[0]; sp_stats(0, 1, sp1, m, r0[1]); c0n[1] = -m * r0[1]; }
      SPIN(tag_ok(spo), spo = sp_load(0, Ta ^ 2));
      __syncthreads();
      STAMP(1, 25);
#pragma unroll
      for (int u = 0; u < 2; ++u) {
        const uint16_t* gp = (const uint16_t*)(sm + L_XB + XBUF + i16 * 512 + (((u * 8 + w) ^ i16) * 8) + (lane >> 4) * 2);
        v2u yv = {(unsigned)gp[0] | ((unsigned)gp[64] << 16), (unsigned)gp[128] | ((unsigned)gp[192] << 16)};
        asm volatile("" : "+v"(yv));
        yres[u] = yv;
        gate_publish(0, u, r0[u], c0n[u]);
      }
    }
    SBAR();
    STAMP(1, 26);
    raw_barrier();
#pragma unroll
    for (int i = NPRE; i < NPRE2; ++i) ld_wgt(i);
    SBAR();
  }

  v4u xs[4];
  v2u sst;
  auto tile_of = [&](int t) AI { return g * 4 + (((t & 1) ^ hs) << 1) + (t >> 1); };
  auto x_src = [&](int ly, int t) AI { return OFF_X + ly * ACT_BYTES + tile_of(t) * XBUF + sj * 4096; };
  auto s_load = [&](int ly, int t) AI { sst = ld64<SC1>(rs, (l16 & 511) + (l16 >> 9) * 8, OFF_SP + (2 * ly + 1) * SP_BYTES + (tile_of(t) * 7 + sj) * 512); };
  auto x0_load = [&]() AI {
#pragma unroll
    for (int i = 0; i < 4; ++i) xs[i] = ldfrag<SC1>(rs, l16, x_src(1, 0), i);
    s_load(1, 0);
  };

  auto layer = [&](auto LY) AI {
    constexpr int ly = decltype(LY)::value;
    const int lane = opaque(tid & 63), tokl = lane & 31, hh = lane >> 5;
    auto x_ok = [&]() AI {
      unsigned mx = 0u;
#pragma unroll
      for (int i = 0; i < 4; ++i) mx = umax3(umax3(mx, xs[i][0], xs[i][1]), xs[i][2], xs[i][3]);
      return __all((mx != EMPTY) & (sst[1] == (hh ? tag1 : tag0)));
    };
    auto x_dma = [&](int t, int half) AI {
      const unsigned dst = lds0 + L_XB + (t & 1) * XBUF + sj * 4096;
      if (half) { dma2<true, 2048>(rs, l16, x_src(ly, t), dst); s_load(ly, t); }
      else dma2<true, 0>(rs, l16, x_src(ly, t), dst);
    };
    auto x_fetch = [&](int t) AI {
      unsigned spins = 0;
      bool ok;
#pragma unroll 1
      do {
        x_dma(t, 0); x_dma(t, 1); DRAIN();
        unsigned mx = 0u;
#pragma unroll
        for (int i = 0; i < 4; i += 2) {
          const v4u a = *(const v4u*)(sm + L_XB + (t & 1) * XBUF + sj * 4096 + i * 1024 + l16), b = *(const v4u*)(sm + L_XB + (t & 1) * XBUF + sj * 4096 + i * 1024 + 1024 + l16);
          mx = umax3(umax3(a[0], a[1], a[2]), umax3(a[3], b[0], b[1]), umax3(b[2], b[3], mx));
          asm volatile("" : "+v"(mx) :: "memory");
        }
        ok = __all((mx != EMPTY) & (sst[1] == (hh ? tag1 : tag0)));
      } while (!ok && ++spins < SPIN_CAP);
    };
    auto st_write = [&](int t) AI {
      unsigned char* sp = sm + L_ST + (t % 3) * 2048 + (sj >> 2) * 512 + tokl * 16 + (sj & 3) * 4;
      *(unsigned*)(sp + hh * 1024) = sst[0];
    };
    const int xrd = tokl * 128 + ((hh ^ ((tokl >> 1) & 7)) << 4);
    auto xfrag = [&](int t, int ks) AI -> v4u { return *(const v4u*)(sm + L_XB + (t & 1) * XBUF + (xrd ^ ((ks & 3) << 5)) + (ks >> 2) * 4096); };
    const int hbase = OFF_H + ly * H_BYTES;
    v4u hf[14];
    auto h_load = [&](int u, int i) AI { hf[i] = ldfrag<SC1>(rs, l16, hbase + ((Ta + u) * KS2 + (i < 7 ? kse : ksl)) * 1024, i % 7); };
    v2u tagv = {tag0, tag1};
    asm volatile("" : "+v"(tagv));
    auto h_flag = [&](int t) AI { st64<SC1>(tagv, rs, opaque(l16) == 0 ? 0 : OOB, OFF_FH + ((ly * 64 + tile_of(t)) * KS2 + fg) * FSTR); };
    auto hf_poll = [&](int u, int half) AI { return ld64<SC1>(rs, min(opaque(l16) * (FSTR / 16), 6 * FSTR), OFF_FH + ((ly * 64 + Ta + u) * KS2 + (half ? ksl : kse)) * FSTR); };
    auto hf_check = [&](int u, int half, v2u v) AI { SPIN(flag_ok(v), v = hf_poll(u, half)); ORDER(); };
    v2u hpl;

    v4u sgr;
    unsigned csp[14];
    auto cs_write = [&]() AI {
      const int ln = opaque(lane), csrow = (ln & 16) + 8 * ((ln >> 2) & 1) + (ln & 3) + 4 * ((ln >> 3) & 1);
      *(float*)(sm + L_CS + w * 128 + csrow * 4) = csum;
    };
    if (ly == 0) {
      __builtin_amdgcn_s_sleep(DELAY3);
      x_fetch(0);
      STAMP(1, 17);
      st_write(0);
#pragma unroll
      for (int i = NPRE2; i < KS1; ++i) ld_wgt(i);
#pragma unroll
      for (int i = 0; i < 14; ++i) csp[i] = ld32<SC1>(rs, (hh * 3584 + tokl) * 4, OFF_CSP + (i * 2 * 3584 + fg * 32) * 4);
      STAMP(1, 6);
      __syncthreads();
      x_dma(1, 0); x_dma(1, 1);
    } else {
      SPIN(x_ok(), x0_load());
      STAMP(1, 30);
      __syncthreads();
      cs_write();
#pragma unroll
      for (int i = 0; i < 4; ++i) *(v4u*)(sm + L_XB + sj * 4096 + i * 1024 + l16) = xs[i];
      st_write(0);
      sgr = sp_load(2, Ta + 1);
      x_dma(1, 0); x_dma(1, 1);
      asm volatile("s_waitcnt lgkmcnt(0)" ::: "memory");
      raw_barrier();
    }
    STAMP(1, 7 + ly * 24);
    if (ly == 1) STAMPC(1, 62);

#define FST(q)
    f32x16 acc[2];
    constexpr int PD = PDV;
    v4u xf[PD], ho;
    f32x4 p0, p1, p2, p3;
    float mean = 0.f, r2[2];
#pragma unroll
    for (int i = 0; i < PD; ++i) xf[i] = xfrag(0, i + (ly == 1 ? NDROP : 0));
    auto ep_cs = [&]() AI {
      const unsigned char* cp = sm + L_CS + w * 128 + hh * 32;
      p0 = *(const f32x4*)cp; p1 = *(const f32x4*)(cp + 64); p2 = *(const f32x4*)(cp + 16); p3 = *(const f32x4*)(cp + 80);
    };
    float hv[2], sq = 0.f;
    auto ep_one = [&](const f32x16& a, int i) AI {
      hv[i & 1] = fmaxf(fmaf(mean, i < 4 ? p0[i & 3] : p2[i & 3], a[i]), 0.f) * fmaf(mean, i < 4 ? p1[i & 3] : p3[i & 3], a[8 + i]);
      asm volatile("" : "+v"(hv[i & 1]));
    };
    auto h_store = [&](int t) AI { st128<SC1>(ho, rs, l16, hbase + (tile_of(t) * KS2 + fg) * 1024); };

    static_for<0, 4>([&](auto TT) AI {
      constexpr int t = TT.value;
      constexpr int rot = (ly == 1 && t == 0) ? NDROP : 0;
      v4u cb0, cb1;
      unsigned cmx = 0u;
      static_for<0, KS1>([&](auto II) AI {
        constexpr int i = II.value, n = t * KS1 + i;
        if constexpr (i == 0) {
          f32x16 z;
#pragma unroll
          for (int e = 0; e < 16; ++e) z[e] = 0.f;
          acc[t & 1] = mfma32(W1[rot], xf[n % PD], z);
        } else acc[t & 1] = mfma32(W1[(i + rot) % KS1], xf[n % PD], acc[t & 1]);
        asm volatile("" : "+v"(acc[t & 1]));
        SBAR();
        if constexpr (i + PD < KS1) xf[n % PD] = xfrag(t, (i + PD + rot) % KS1);
        else if constexpr (t < 3) xf[n % PD] = xfrag(t + 1, i + PD - KS1);
        if constexpr (t == 2 && i == 0) { wait_vm<5 + (ly == 0 ? 6 : 0)>(); h_flag(0); }
        if constexpr (t == 2 && i == 24) { wait_vm<(ly == 0 ? 6 : 1)>(); h_flag(1); }
        if constexpr (t == 3 && i == 23) { wait_vm<5>(); h_flag(2); }
        if constexpr (t > 0) {
          if constexpr (i >= 3 && i <= 9 && (i & 1)) { ho[(i - 3) / 2] = pk2(hv[0], hv[1]); asm volatile("" : "+v"(ho[(i - 3) / 2])); }
          if constexpr (i >= 1 && i <= 8) ep_one(acc[(t - 1) & 1], i - 1);
          if constexpr (i == H_ST) h_store(t - 1);
        }
        if constexpr (t < 3) {
          const unsigned char* xb = sm + L_XB + ((t + 1) & 1) * XBUF + sj * 4096 + l16;
          if constexpr (i == XC) {
            wait_vm<(t > 0) + (t == 2) + (t == 0 ? (ly == 1 ? 8 : 1) : 0) + (ly == 0 && t > 0 ? 4 : 0)>();
            asm volatile("" : "+v"(sst));
            cb0 = *(const v4u*)xb; cb1 = *(const v4u*)(xb + 1024);
          }
          if constexpr (i == XC + 3) {
            cmx = umax3(umax3(cb0[0], cb0[1], cb0[2]), umax3(cb0[3], cb1[0], cb1[1]), max(cb1[2], cb1[3]));
            asm volatile("" : "+v"(cmx));
          }
          if constexpr (i == XC + 4) {
            cb0 = *(const v4u*)(xb + 2048); cb1 = *(const v4u*)(xb + 3072);
            cmx = sst[1] != (hh ? tagv[1] : tagv[0]) ? EMPTY : cmx;
            asm volatile("" : "+v"(cmx));
          }
          if constexpr (i == XC + 7) {
            cmx = umax3(umax3(cb0[0], cb0[1], cb0[2]), umax3(cb0[3], cb1[0], cb1[1]), umax3(cb1[2], cb1[3], cmx));
            asm volatile("" : "+v"(cmx));
          }
          if constexpr (i == XC + 8) {
            if (__builtin_expect(!__all(cmx != EMPTY), 0)) x_fetch(t + 1);
            st_write(t + 1);
          }
        }
        if constexpr (ly == 0 && t == 0 && i == 4)
          st128<SC1>(v4u{EMPTY, EMPTY, EMPTY, EMPTY}, rs, l16, OFF_X + ACT_BYTES + ((Ta + (w >> 2)) * 7 + js) * 4096 + (w & 3) * 1024);
        if constexpr (ly == 0 && t == 0 && i == XC + 1) {
          float cs = 0.f;
#pragma unroll
          for (int q7 = 0; q7 < 14; ++q7) cs += asf(csp[q7]);
          csum = half_sum(cs);
          cs_write();
        }
        if constexpr (ly == 0 && (t == 1 || t == 2) && (i == 2 || i == 6 || i == 12)) ld_wgt(KS1 + (t - 1) * 4 + i / 5);
        if constexpr (ly == 0 && (t == 1 || t == 2) && i == 21) ld_wgt(KS1 + (t - 1) * 4 + 3);
        if constexpr (ly == 0 && t == 2 && i >= 24 && i < 27) ld_wgt(KS1 + 8 + i - 24);
        if constexpr (wl_slot(ly, t, i) >= 0) {
          constexpr int qn = wl_slot(ly, t, i), f = NWS + qn;
          dma1(rs, l16, wd_base(f) + ((f >> 1) % 7) * 1024, lds0 + L_WD + (w * NWL + qn) * 1024);
        }
        if constexpr (ly == 1 && t == 0 && i == X3S) x3_publish(1, sgr);
        if constexpr (ly == 1 && t == 0 && i >= 3 && i < 15 && (i & 1)) W1[8 + (i - 3) / 2] = ldfrag<SC1>(rs, l16, w1o, 8 + (i - 3) / 2);
        if constexpr (t < 2 && (i == BAR + 1 || i == BAR + 3)) x_dma(t + 2, (i - BAR - 1) / 2);
        if constexpr (t == 2 && i == 15) hpl = hf_poll(0, 0);
        if constexpr (t == 3) {
          if constexpr (i == 0) hf_check(0, 0, hpl);
          if constexpr (i == 9) hpl = hf_poll(0, 1);
          if constexpr (i == 19) hf_check(0, 1, hpl);
          if constexpr (pace(i) >= 0) h_load(0, pace(i));
        }
        if constexpr (i == 11) {
          const unsigned char* sp = sm + L_ST + (t % 3) * 2048 + tokl * 16;
          p0 = *(const f32x4*)sp; p1 = *(const f32x4*)(sp + 512);
        }
        if constexpr (i == 13) { p0[0] += p0[1]; p0[2] += p0[3]; p1[0] += p1[1]; asm volatile("" : "+v"(p0[0]), "+v"(p0[2]), "+v"(p1[0])); }
        if constexpr (i == 15) { mean = ((p0[0] + p0[2]) + (p1[0] + p1[2])) * (-1.0f / D); asm volatile("" : "+v"(mean)); }
        if constexpr (!(t & 1)) {
          if constexpr (i == 16) { const unsigned char* sp = sm + L_ST + (t % 3) * 2048 + tokl * 16; p2 = *(const f32x4*)(sp + 1024); p3 = *(const f32x4*)(sp + 1536); }
          if constexpr (i == 19) { p2[0] += p2[1]; p2[2] += p2[3]; p3[0] += p3[1]; asm volatile("" : "+v"(p2[0]), "+v"(p2[2]), "+v"(p3[0])); }
          if constexpr (i == 20) { sq = (p2[0] + p2[2]) + (p3[0] + p3[2]); asm volatile("" : "+v"(sq)); }
          if constexpr (i == 26) { sq = __builtin_amdgcn_rcpf(fmaxf(fmaf(sq, 1.0f / D, -mean * mean), 0.0f) + EPS); asm volatile("" : "+v"(sq)); }
          if constexpr (i == 27) r2[t >> 1] = asf(__builtin_amdgcn_ds_bpermute(w * 16 + (lane >> 4) * 4, asu(sq)));
        }
        if constexpr (i == 25) ep_cs();
        if constexpr (i == BAR && t < 3) {
          asm volatile("s_waitcnt lgkmcnt(1)" ::: "memory");
          raw_barrier();
        }
        SBAR();
      });
      STAMP(1, 8 + t + ly * 24);
      if (ly == 1 && t == 2) STAMPC(1, 63);
    });
#pragma unroll
    for (int i = 0; i < 8; ++i) { ep_one(acc[1], i); if (i & 1) ho[i >> 1] = pk2(hv[0], hv[1]); }
    h_store(3);
    h_load(0, 11); h_load(0, 12);
    SBAR();
    STAMP(1, 12 + ly * 24);

    auto finalize = [&](auto UU, float r2) AI {
      constexpr int u = UU.value;
      const int T = Ta + u;
      const int ln = opaque(lane), i16 = ln & 15, ftok = 4 * w + (ln >> 4);
      const int rj = 2 * (i16 >> 2) + (i16 & 1), rslot = ((i16 >> 1) & 1) * 32 + (ftok & 16) + ((ftok + i16) & 15);
      f32x4 a = *(const f32x4*)(sm + L_RED + (rj * 64 + rslot) * 16);
#pragma unroll
      for (int ww = 1; ww < 8; ++ww) a += *(const f32x4*)(sm + L_RED + ((ww * 8 + rj) * 64 + rslot) * 16);
      const unsigned f01 = pk2(a[0] * r2, a[1] * r2), f23 = pk2(a[2] * r2, a[3] * r2);
      const v2u xn = {pk2(bflo(xres[u][0]) + bflo(f01), bfhi(xres[u][0]) + bfhi(f01)), pk2(bflo(xres[u][1]) + bflo(f23), bfhi(xres[u][1]) + bfhi(f23))};
      if constexpr (ly == 1) {
        *(v2u*)(out + (size_t)(T * 32 + ftok) * D + js * 64 + i16 * 4) = xn;
      } else {
        xres[u] = xn;
        sp_publish(2, u);
      }
    };
    v4u sga;
    auto unit = [&](auto UU) AI {
      constexpr int u = UU.value;
      v4u wl[3];
      static_for<0, 28>([&](auto FF) AI {
        constexpr int f = FF.value;
        v4u wf;
        if constexpr (f < NWS) wf = Wd[f]; else if constexpr (f < NWS + NWL) wf = wl[f % 3]; else wf = Wd[f - NWL];
        if constexpr (f < 2) {
          f32x16 z;
#pragma unroll
          for (int e = 0; e < 16; ++e) z[e] = 0.f;
          acc[f] = mfma32(wf, hf[0], z);
        } else acc[f & 1] = mfma32(wf, hf[f >> 1], acc[f & 1]);
        asm volatile("" : "+v"(acc[f & 1]));
        SBAR();
        if constexpr (f + 3 >= NWS && f + 3 < NWS + NWL) wl[f % 3] = *(const v4u*)(sm + L_WD + (w * NWL + (f + 3 - NWS)) * 1024 + l16);
        if constexpr (u == 0) {
          if constexpr (f == 7) hpl = hf_poll(1, 0);
          if constexpr (f == 15) { wait_vm<9>(); h_flag(3); }
          if constexpr (f == 19) hf_check(1, 0, hpl);
          if constexpr (pace(f) == 0) h_load(0, 13);
          if constexpr (pace(f) >= 1 && pace(f) <= NWR - NWS) Wd[NWS + pace(f) - 1] = ld_wd(NWL + NWS + pace(f) - 1);
          if constexpr (pace(f) >= 7) h_load(1, pace(f) - 7);
        }
        if constexpr (u == 1 && pace(f) >= 0 && pace(f) < 3) h_load(1, 11 + pace(f));
        if constexpr (u == 1 && ly == 0) {
          if constexpr (pace(f) >= 3) W1[pace(f) - 3] = ldfrag<SC1>(rs, l16, w1o, pace(f) - 3);
          if constexpr (f == SGA) sga = sp_load(2, Ta);
        }
        if constexpr (f == 4) __syncthreads();
        SBAR();
      });
      STAMP(1, 13 + u * 5 + ly * 24);
      if constexpr (u == 1 && ly == 0) { x3_publish(0, sga); SBAR(); STAMP(1, 46); }
      const int ln = opaque(lane);
#pragma unroll
      for (int c = 0; c < 2; ++c)
#pragma unroll
        for (int qd = 0; qd < 4; ++qd) {
          const int jq = c * 4 + qd;
          const int slot = (ln & 48) + ((ln + 4 * (jq >> 1) + 2 * (ln >> 5) + (jq & 1)) & 15);
          *(f32x4*)(sm + L_RED + ((w * 8 + jq) * 64 + slot) * 16) = f32x4{acc[c][4 * qd], acc[c][4 * qd + 1], acc[c][4 * qd + 2], acc[c][4 * qd + 3]};
        }
      SBAR();
      if constexpr (u == 0) { h_load(1, 4); h_load(1, 5); h_load(1, 6); hpl = hf_poll(1, 1); }
      SBAR();
      STAMP(1, 14 + u * 5 + ly * 24);
      __syncthreads();
      STAMP(1, 15 + u * 5 + ly * 24);
      if constexpr (u == 1 && ly == 0) x0_load();
      finalize(UU, r2[u]);
      SBAR();
      if constexpr (u == 0) {
        hf_check(1, 1, hpl);
#pragma unroll
        for (int i = 7; i < 11; ++i) h_load(1, i);
        SBAR();
      }
      STAMP(1, 16 + u * 5 + ly * 24);
    };
    unit(IC<0>{});
    unit(IC<1>{});
  };
  layer(IC<0>{});
  layer(IC<1>{});
  st64<SC1>(v2u{sopq(tag0), sopq(tag1)}, rs, (bid == 0 && tid == 0) ? 0 : OOB, OFF_EP);
  STAMP(1, 60);
}

extern "C" void kernel_launch(void* const* d_in, const int* in_sizes, int n_in, void* d_out, int out_size, void* d_ws,
                              size_t ws_size, hipStream_t stream) {
  const uint16_t* q = (const uint16_t*)d_in[0];
  const uint16_t* k = (const uint16_t*)d_in[1];
  const uint16_t* v = (const uint16_t*)d_in[2];
  const uint16_t* shk = (const uint16_t*)d_in[3];
  const uint16_t* ssk = (const uint16_t*)d_in[4];
  const uint16_t* ssd = (const uint16_t*)d_in[5];
  const uint16_t* wg = (const uint16_t*)d_in[7];
  const uint16_t* wu = (const uint16_t*)d_in[8];
  const uint16_t* wd = (const uint16_t*)d_in[9];
  unsigned char* ws = (unsigned char*)d_ws;
  uint16_t* out = (uint16_t*)d_out;
  (void)hipFuncSetAttribute((const void*)k_main, hipFuncAttributeMaxDynamicSharedMemorySize, L_BYTES);
  k_main<<<NAUX + 224, 512, L_BYTES, stream>>>(q, k, v, shk, ssk, ssd, wg, wu, wd, ws, out);
}
```

```cpp
#include <hip/hip_runtime.h>
#include <hip/hip_bf16.h>
#include <stdint.h>

#define DI __device__ __forceinline__
#define AI __attribute__((always_inline))
#define LAS __attribute__((address_space(3)))
typedef unsigned v4u __attribute__((ext_vector_type(4)));
typedef unsigned v2u __attribute__((ext_vector_type(2)));
typedef float f32x4 __attribute__((ext_vector_type(4)));
typedef float f32x2 __attribute__((ext_vector_type(2)));
typedef float f32x16 __attribute__((ext_vector_type(16)));
typedef short bf16x8 __attribute__((ext_vector_type(8)));
typedef short s16x4 __attribute__((ext_vector_type(4)));
typedef __bf16 bf2 __attribute__((ext_vector_type(2)));
typedef __amdgpu_buffer_rsrc_t rsrc_t;

constexpr int S = 2048, D = 448, F = 1792;
constexpr int KS1 = D / 16;
constexpr int KS2 = F / 16;
constexpr float EPS = 1e-5f;

constexpr int ACT_BYTES = S * D * 2;
constexpr int FSTR = 32;
constexpr int OFF_EP = 0;
constexpr int OFF_FW1 = 256;
constexpr int OFF_FW2 = OFF_FW1 + 28 * 32 * 128;
constexpr int OFF_FH = OFF_FW2 + 7 * KS2 * 128;
constexpr int OFF_SP = OFF_FH + 2 * 64 * KS2 * FSTR;
constexpr int SP_BYTES = 64 * 7 * 32 * 16;
constexpr int OFF_KVG = OFF_SP + 4 * SP_BYTES;
constexpr int OFF_X = OFF_KVG + 2 * ACT_BYTES;
constexpr int OFF_H = OFF_X + 2 * ACT_BYTES;
constexpr int H_BYTES = 64 * KS2 * 1024;
constexpr int OFF_W1 = OFF_H + 2 * H_BYTES;
constexpr int OFF_W2 = OFF_W1 + 112 * KS1 * 1024;
constexpr int OFF_CSP = OFF_W2 + 14 * KS2 * 1024;
constexpr int WS_BYTES = OFF_CSP + 28 * 112 * 32 * 4;
static_assert(OFF_SP % 128 == 0 && OFF_KVG % 128 == 0, "alignment");

constexpr int SC1 = 16;
DI rsrc_t mk_rsrc(const void* p, int bytes) { return __builtin_amdgcn_make_buffer_rsrc((void*)p, 0, bytes, 0x00020000); }
template <int AUX = 0> DI v4u ld128(rsrc_t r, int voff, int soff) { return __builtin_amdgcn_raw_buffer_load_b128(r, voff, soff, AUX); }
template <int AUX = 0> DI v2u ld64(rsrc_t r, int voff, int soff) { return __builtin_amdgcn_raw_buffer_load_b64(r, voff, soff, AUX); }
template <int AUX = 0> DI unsigned ld32(rsrc_t r, int voff, int soff) { return __builtin_amdgcn_raw_buffer_load_b32(r, voff, soff, AUX); }
template <int AUX = 0> DI unsigned ld16(rsrc_t r, int voff, int soff) { return __builtin_amdgcn_raw_buffer_load_b16(r, voff, soff, AUX); }
template <int AUX = 0> DI void st128(v4u v, rsrc_t r, int voff, int soff) {
  __builtin_amdgcn_raw_buffer_store_b128(v, r, voff, soff, AUX);
  asm volatile("s_nop 1" ::"v"(v));
}
template <int AUX = 0> DI void st64(v2u v, rsrc_t r, int voff, int soff) { __builtin_amdgcn_raw_buffer_store_b64(v, r, voff, soff, AUX); }
template <int AUX = 0> DI void st32(unsigned v, rsrc_t r, int voff, int soff) { __builtin_amdgcn_raw_buffer_store_b32(v, r, voff, soff, AUX); }
DI float asf(unsigned u) { return __builtin_bit_cast(float, u); }
DI unsigned asu(float f) { return __builtin_bit_cast(unsigned, f); }
DI float bflo(unsigned u) { return asf(u << 16); }
DI float bfhi(unsigned u) { return asf(u & 0xffff0000u); }
DI unsigned pk2(float a, float b) {
  f32x2 v = {a, b};
  return __builtin_bit_cast(unsigned, __builtin_convertvector(v, bf2));
}
DI float rbf(float a) { return bflo(pk2(a, a)); }
DI float dot2(unsigned a, unsigned b, float c) {
  return __builtin_amdgcn_fdot2_f32_bf16(__builtin_bit_cast(bf2, a), __builtin_bit_cast(bf2, b), c, false);
}
DI f32x16 mfma32(v4u a, v4u b, f32x16 c) {
  return __builtin_amdgcn_mfma_f32_32x32x16_bf16(__builtin_bit_cast(bf16x8, a), __builtin_bit_cast(bf16x8, b), c, 0, 0, 0);
}
DI f32x4 mfma16(v4u a, v4u b, f32x4 c) {
  return __builtin_amdgcn_mfma_f32_16x16x32_bf16(__builtin_bit_cast(bf16x8, a), __builtin_bit_cast(bf16x8, b), c, 0, 0, 0);
}
DI v4u tr_frag(LAS unsigned char* p, int second) {
  const v2u l2 = __builtin_bit_cast(v2u, __builtin_amdgcn_ds_read_tr16_b64_v4i16((LAS s16x4*)p));
  const v2u h2 = __builtin_bit_cast(v2u, __builtin_amdgcn_ds_read_tr16_b64_v4i16((LAS s16x4*)(p + second)));
  return v4u{l2[0], l2[1], h2[0], h2[1]};
}
template <int CTRL> DI float dpp_add(float v) {
  return v + asf(__builtin_amdgcn_update_dpp(0, asu(v), CTRL, 0xf, 0xf, true));
}
DI float row16_sum(float v) {
  v = dpp_add<0x128>(v); v = dpp_add<0x124>(v); v = dpp_add<0x122>(v); v = dpp_add<0x121>(v);
  return v;
}
#define PIN2(a, b) asm volatile("" : "+v"(a), "+v"(b))
#define SBAR() __builtin_amdgcn_sched_barrier(0)
#define DRAIN() asm volatile("s_waitcnt vmcnt(0)" ::: "memory")
#define ORDER() __builtin_amdgcn_fence(__ATOMIC_ACQUIRE, "wavefront")
#define STAMPC(kid, i)
#define STAMP(kid, i)
constexpr unsigned ONES = 0x3f803f80u;
constexpr unsigned EMPTY = 0xffffffffu;
#define POLL_SLEEP 2
constexpr unsigned SPIN_CAP = 1u << 16;

DI void row_stats(float s, float ss, float& m, float& r) {
  m = s * (1.0f / D);
  const float var = fmaxf(fmaf(ss, 1.0f / D, -m * m), 0.0f);
  r = __builtin_amdgcn_rsqf(var + EPS);
}
constexpr int NWL = 12;
constexpr int NWR = 28 - NWL;
constexpr int NDROP = 14;
constexpr int XBUF = D * 64;
constexpr int L_XB = 0, L_ST = 2 * XBUF, L_CS = L_ST + 3 * 2048, L_RED = 0, L_WD = 65536, L_BYTES = L_WD + NWL * 8 * 1024;
static_assert(L_CS + 8 * 128 <= L_WD && L_BYTES <= 163840, "LDS map");
constexpr int CV_HSTR = 4160, CV_USTR = 2304 * 2;
constexpr int CV_H = 0, CV_U = CV_H + 2 * 4 * CV_HSTR, CV_RED = L_WD, CV_W = 43008;
static_assert(CV_U + 2 * CV_USTR <= CV_W && CV_W + 18432 <= L_WD && CV_RED + 65536 <= L_BYTES, "conv LDS map");

#define ABL 0
#define PDV 4
template <int V> struct IC { static constexpr int value = V; };
template <int I, int N, class Fn> DI void static_for(Fn&& fn) {
  if constexpr (I < N) { fn(IC<I>{}); static_for<I + 1, N>(fn); }
}
template <int N> DI void wait_vm() { asm volatile("s_waitcnt vmcnt(%0)" ::"n"(N) : "memory"); }
DI float half_sum(float v) {
  const auto r = __builtin_amdgcn_permlane32_swap(asu(v), asu(v), false, false);
  return asf(r[0]) + asf(r[1]);
}
DI unsigned umax3(unsigned a, unsigned b, unsigned c) { return max(max(a, b), c); }
constexpr int OOB = (int)0x80000000;
DI void raw_barrier() { asm volatile("" ::: "memory"); __builtin_amdgcn_s_barrier(); asm volatile("" ::: "memory"); }
template <int AUX = 0> DI v4u ldfrag(rsrc_t r, int l16, int base, int i) { return ld128<AUX>(r, l16, base + i * 1024); }
template <bool DEV, int O0> DI void dma2(rsrc_t r, int l16, int soff, unsigned lds) {
  if constexpr (DEV)
    asm volatile("s_mov_b32 m0, %0\n\ts_nop 0\n\tbuffer_load_dwordx4 %1, %2, %3 offen offset:%4 sc1 lds\n\tbuffer_load_dwordx4 %1, %2, %3 offen offset:%5 sc1 lds"
                 ::"s"(lds), "v"(l16), "s"(r), "s"(soff), "n"(O0), "n"(O0 + 1024) : "m0", "memory");
  else
    asm volatile("s_mov_b32 m0, %0\n\ts_nop 0\n\tbuffer_load_dwordx4 %1, %2, %3 offen offset:%4 lds\n\tbuffer_load_dwordx4 %1, %2, %3 offen offset:%5 lds"
                 ::"s"(lds), "v"(l16), "s"(r), "s"(soff), "n"(O0), "n"(O0 + 1024) : "m0", "memory");
}
DI void dma1(rsrc_t r, int l16, int soff, unsigned lds) {
  asm volatile("s_mov_b32 m0, %0\n\ts_nop 0\n\tbuffer_load_dwordx4 %1, %2, %3 offen sc1 lds" ::"s"(lds), "v"(l16), "s"(r), "s"(soff) : "m0", "memory");
}
DI int opaque(int v) { asm volatile("" : "+v"(v)); return v; }
DI unsigned sopq(unsigned v) { asm volatile("" : "+s"(v)); return v; }
DI unsigned nz(unsigned v) { return min(v, EMPTY - 1u); }

#define NWSV 10
constexpr int NWS = NWSV;
constexpr int BAR = KS1 - 1 - PDV;
constexpr int H_ST = 10;
constexpr int XC = 14;
constexpr int pace(int i) {
  constexpr int sl[11] = {0, 3, 5, 8, 11, 13, 16, 19, 21, 24, 26};
  for (int n = 0; n < 11; ++n) if (sl[n] == i) return n;
  return -1;
}
constexpr int wl_slot(int ly, int t, int i) {
  return (ly != 0 || t == 3) ? -1 : t == 0 ? (i >= 24 ? i - 24 : -1) : i == 16 ? t * 4 : i == 19 ? t * 4 + 1 : i == 23 ? t * 4 + 2 : i == 27 ? t * 4 + 3 : -1;
}

#define SPIN(cond_ok, reload) do { if (__builtin_expect(!(cond_ok), 0)) { unsigned spins_ = 0; \
  _Pragma("unroll 1") do { asm volatile("" ::: "memory"); reload; } while (!(cond_ok) && ++spins_ < SPIN_CAP); } } while (0)
DI unsigned mk_tag1(v2u ep, unsigned tag0) {
  unsigned t = __builtin_amdgcn_readfirstlane(ep[1]) + 0x9e3779b9u;
  if (t - tag0 + 1u <= 2u) t += 0x10001u;
  return t;
}
constexpr int NAUX = 32;
#define AUXD 4
#define WD 1
#define XB 7
#define GB 6
#define X3S 2
#define SGA 17
#define DELAY2 0
#define DELAY3 0
#define FWP 0
#define FWC 5
#define PSLEEP
#define NPRE 18
#define NPRE2 24
#define DELAY1 40
#define LATE_N 0

__global__ void __launch_bounds__(512, 2) k_main(const uint16_t* __restrict__ q, const uint16_t* __restrict__ kin,
                                                 const uint16_t* __restrict__ vin, const uint16_t* __restrict__ shk,
                                                 const uint16_t* __restrict__ ssk, const uint16_t* __restrict__ ssd,
                                                 const uint16_t* __restrict__ wg, const uint16_t* __restrict__ wu,
                                                 const uint16_t* __restrict__ wdn, unsigned char* __restrict__ ws,
                                                 uint16_t* __restrict__ out) {
  extern __shared__ __attribute__((aligned(16))) unsigned char sm[];
  LAS unsigned char* L = (LAS unsigned char*)sm;
  const int tid = threadIdx.x, lane = tid & 63;
  int w_ = __builtin_amdgcn_readfirstlane(tid >> 6);
  asm volatile("" : "+s"(w_));
  const int w = w_;
  const rsrc_t rs = mk_rsrc(ws, WS_BYTES);
  const int l16 = lane * 16, tokl = lane & 31, hh = lane >> 5, i16 = lane & 15;
  STAMP(1, 0);
  const v2u ep = ld64<SC1>(rs, 0, OFF_EP);

  const int tr_off = (hh * 8 + (i16 >> 2)) * 144 + (8 * (i16 & 1) + 4 * ((i16 >> 1) & 1)) * 2;

  if (blockIdx.x < NAUX) {
    const int r = tid >> 3, qq = tid & 7;
    v4u a[4][2];
    for (int i = 0; i < AUXD; ++i) __builtin_amdgcn_s_sleep(36);
#pragma unroll
    for (int n = 0; n < 4; ++n) {
      const int b = min((int)blockIdx.x + 32 * n, 97);
      const size_t src = (size_t)((b / 7) * 128 + r) * D + (b % 7) * 64 + qq * 8;
      a[n][0] = *(const v4u*)(wdn + src); a[n][1] = *(const v4u*)(wdn + src + 64 * D);
    }
#pragma unroll
    for (int n = 0; n < 4; ++n) {
      *(v4u*)(sm + n * 18432 + r * 144 + qq * 16) = a[n][0];
      *(v4u*)(sm + n * 18432 + (64 + r) * 144 + qq * 16) = a[n][1];
    }
    __syncthreads();
    const unsigned tg0 = __builtin_amdgcn_readfirstlane(ep[0]) + 1u;
    const v2u tg = {tg0, mk_tag1(ep, tg0)};
#pragma unroll
    for (int n = 0; n < 4; ++n) {
      const int b = blockIdx.x + 32 * n, kb = b / 7, cb = b % 7;
#pragma unroll
      for (int ct = 0; ct < 2; ++ct) {
        const v4u o = tr_frag(L + n * 18432 + w * 16 * 144 + tr_off + (ct * 32 + ((lane >> 4) & 1) * 16) * 2, 4 * 144);
        st128<SC1>(o, rs, b < 98 ? l16 : OOB, OFF_W2 + ((cb * 2 + ct) * KS2 + kb * 8 + w) * 1024);
      }
    }
    DRAIN();
    {
      const int b = blockIdx.x + 32 * (lane >> 4);
      st64<SC1>(tg, rs, b < 98 ? ((b % 7) * KS2 + (b / 7) * 8 + w) * 128 + i16 * 8 : OOB, OFF_FW2);
    }
    return;
  }

  const int bid = blockIdx.x - NAUX;
  const int xcd = bid & 7, li = bid >> 3;
  const int g = (xcd >> 1) * 4 + li / 7, hs = xcd & 1, js = li % 7, j = hs * 7 + js;
  const int fg = j * 8 + w;
  const int sj = w < 7 ? w : 6;
  const int w1o = OFF_W1 + fg * KS1 * 1024;
  const int Ta = g * 4 + hs * 2;
  const unsigned lds0 = (unsigned)(size_t)L;

  v4u W1[KS1], Wd[NWR];
  float csum;
  unsigned csp[14];
  v2u xres[2], yres[2];
  unsigned tag0, tag1;
  const int kse = hs * 56 + w * 7, ksl = (hs ^ 1) * 56 + w * 7;
  auto wd_src = [&](int f) AI { return OFF_W2 + ((js * 2 + ((f / 7) & 1)) * KS2 + (f < 14 ? kse : ksl) + f % 7) * 1024; };
  auto ld_wd = [&](int f) AI { return ld128<SC1>(rs, l16, wd_src(f)); };
  auto ld_wgt = [&](int n) AI {
    if (n < KS1) W1[n] = ldfrag<SC1>(rs, l16, w1o, n);
    else if (n < KS1 + NWS) Wd[n - KS1] = ld_wd(n - KS1);
  };
  auto tag_ok = [&](v4u v) AI { return __all(((v[1] ^ sopq(tag0)) | (v[3] ^ sopq(tag1))) == 0u); };
  auto flag_ok = [&](v2u v) AI { return __all(((v[0] ^ sopq(tag0)) | (v[1] ^ sopq(tag1))) == 0u); };
  auto sp_publish = [&](int which, int u) AI {
    const int ln = opaque(lane);
    const v2u xn = xres[u];
    const float s = row16_sum(dot2(xn[0], ONES, dot2(xn[1], ONES, 0.f))), ss = row16_sum(dot2(xn[0], xn[0], dot2(xn[1], xn[1], 0.f)));
    st128<SC1>(v4u{asu(s), sopq(tag0), asu(ss), sopq(tag1)}, rs, (ln & 15) == 0 ? ln : OOB, OFF_SP + which * SP_BYTES + ((Ta + u) * 7 + js) * 512 + w * 64);
  };
  auto sp_load = [&](int which, int T) AI {
    const int ln = opaque(lane);
    return ld128<SC1>(rs, ((ln & 15) < 7 ? (ln & 15) * 512 : 0) + (ln & 48), OFF_SP + which * SP_BYTES + T * 7 * 512 + w * 64);
  };
  auto sp_stats = [&](int which, int u, v4u sp, float& m, float& r) AI {
    SPIN(tag_ok(sp), sp = sp_load(which, Ta + u));
    row_stats(row16_sum(i16 < 7 ? asf(sp[0]) : 0.f), row16_sum(i16 < 7 ? asf(sp[2]) : 0.f), m, r);
  };
  auto gate_publish = [&](int ly, int u, float r, float c) AI {
    v2u xn;
#pragma unroll
    for (int e = 0; e < 2; ++e) {
      const float xl = bflo(xres[u][e]), xh = bfhi(xres[u][e]);
      xn[e] = nz(pk2(fmaf(fmaf(xl, r, c), bflo(yres[u][e]), xl), fmaf(fmaf(xh, r, c), bfhi(yres[u][e]), xh)));
    }
    xres[u] = xn;
    const int ln = opaque(lane);
    st64<SC1>(xn, rs, (ln * 8) ^ (((2 * w + (ln >> 5)) & 7) << 4), OFF_X + ly * ACT_BYTES + ((Ta + u) * 7 + js) * 4096 + w * 512);
    sp_publish(2 * ly + 1, u);
  };
  auto x3_publish = [&](int u, v4u sp) AI {
    float m, r;
    sp_stats(2, u, sp, m, r);
    gate_publish(1, u, r, -m * r);
  };

  {
    const int c0 = bid * 2;
    const int r = tid >> 3, qq = tid & 7;
    const int tok = Ta * 32 + r, ca = js * 64 + qq * 8;
    const rsrc_t rk = mk_rsrc(kin, ACT_BYTES), rh = mk_rsrc(ssk, ACT_BYTES);
    v4u kk[4], sw[4];
    const v4u swl = *(const v4u*)(shk + js * 256 + tokl * 8);
#pragma unroll
    for (int i = 0; i < 4; ++i) kk[i] = ld128(rk, ((tok - i) * D + ca) * 2, 0);
    const v4u vv = *(const v4u*)(vin + (size_t)tok * D + ca);
    const int ch = tid >> 8, i8 = tid & 255;
    const v4u d = ld128(rh, tid * 16, c0 * S * 2);
    const v2u e = ld64(rh, tid * 16 + 16 + c0 * S * 2, 0);
    STAMP(1, 28);
    SBAR();
    *(v4u*)(sm + CV_RED + 16384 + w * 1024 + l16) = swl;
#pragma unroll
    for (int i = 0; i < 4; ++i) sw[i] = *(const v4u*)(sm + CV_RED + 16384 + w * 1024 + qq * 64 + i * 16);
    tag0 = __builtin_amdgcn_readfirstlane(ep[0]) + 1u;
    tag1 = mk_tag1(ep, tag0);
    {
      uint16_t* tl = (uint16_t*)(sm + CV_RED) + qq * 8 * 64 + ((63 - r) ^ (qq << 3));
#pragma unroll
      for (int e2 = 0; e2 < 4; ++e2) {
        const unsigned wa01 = sw[e2][0], wa23 = sw[e2][1], wb01 = sw[e2][2], wb23 = sw[e2][3];
        unsigned p = pk2(bflo(kk[3][e2]) * bfhi(wa23), bfhi(kk[3][e2]) * bfhi(wb23));
        unsigned acc = p;
        p = pk2(bflo(kk[2][e2]) * bflo(wa23), bfhi(kk[2][e2]) * bflo(wb23));
        acc = pk2(bflo(acc) + bflo(p), bfhi(acc) + bfhi(p));
        p = pk2(bflo(kk[1][e2]) * bfhi(wa01), bfhi(kk[1][e2]) * bfhi(wb01));
        acc = pk2(bflo(acc) + bflo(p), bfhi(acc) + bfhi(p));
        p = pk2(bflo(kk[0][e2]) * bflo(wa01), bfhi(kk[0][e2]) * bflo(wb01));
        acc = pk2(bflo(acc) + bflo(p), bfhi(acc) + bfhi(p));
        const unsigned kv = pk2(bflo(acc) * bflo(vv[e2]), bfhi(acc) * bfhi(vv[e2]));
        tl[(2 * e2) * 64] = (uint16_t)kv;
        tl[(2 * e2 + 1) * 64] = (uint16_t)(kv >> 16);
      }
    }
    SBAR();
    const int wt = min(bid, 195), kb = wt / 28, fb = wt % 28, ks = kb * 4 + (w & 3), rl = lane >> 3;
    v4u wa[4];
    {
      const rsrc_t rg = mk_rsrc(wg, D * F * 2), ru = mk_rsrc(wu, D * F * 2);
      const int vo = (w < 4 || WD) ? OOB : ((ks * 16 + rl) * F + fb * 64 + qq * 8) * 2;
      wa[0] = ld128(rg, vo, 0); wa[1] = ld128(rg, vo, 16 * F); wa[2] = ld128(ru, vo, 0); wa[3] = ld128(ru, vo, 16 * F);
    }
    SBAR();
    STAMP(1, 29);
    __syncthreads();
    STAMP(1, 1);
#pragma unroll
    for (int i = 0; i < 2; ++i) {
      const int cl = i * 32 + (tid >> 4);
      const v2u o = *(const v2u*)(sm + CV_RED + cl * 128 + ((i16 ^ ((cl >> 3) << 1)) * 8));
      st128<SC1>(v4u{o[0], sopq(tag0), o[1], sopq(tag1)}, rs, (tid >> 4) * 8192 + i16 * 16, OFF_KVG + (js * 64 + i * 32) * 8192 + (S - 64 - Ta * 32) * 4);
    }
    SBAR();
    {
      const unsigned a0 = __builtin_amdgcn_alignbit(d[1], d[0], 16), a1 = __builtin_amdgcn_alignbit(d[2], d[1], 16),
                     a2 = __builtin_amdgcn_alignbit(d[3], d[2], 16), a3 = __builtin_amdgcn_alignbit(e[0], d[3], 16),
                     a4 = __builtin_amdgcn_alignbit(e[1], e[0], 16);
      unsigned char* hb = sm + CV_H + ch * 4 * CV_HSTR + 32 + i8 * 16;
      *(v4u*)(hb) = d;
      *(v4u*)(hb + CV_HSTR) = v4u{a0, a1, a2, a3};
      *(v4u*)(hb + 2 * CV_HSTR) = v4u{d[1], d[2], d[3], e[0]};
      *(v4u*)(hb + 3 * CV_HSTR) = v4u{a1, a2, a3, a4};
      const unsigned d0 = __builtin_amdgcn_readfirstlane(d[0]), d1 = __builtin_amdgcn_readfirstlane(d[1]);
      if (i8 < 40) {
        const int q8 = i8 - 32, sg = q8 >> 1;
        v4u pv = {0u, 0u, 0u, 0u};
        if (q8 == 3) pv[3] = d0 << 16;
        if (q8 == 5) pv[3] = d0;
        if (q8 == 7) { pv[2] = d0 << 16; pv[3] = __builtin_amdgcn_alignbit(d1, d0, 16); }
        *(v4u*)(sm + (q8 < 0 ? CV_U + ch * CV_USTR + 4096 + i8 * 16 : CV_H + (ch * 4 + sg) * CV_HSTR + (q8 & 1) * 16)) = pv;
      }
    }
    STAMP(1, 2);
    float dcoef;
    auto own_load = [&]() AI {
#pragma unroll
      for (int u = 0; u < 2; ++u) xres[u] = *(const v2u*)(q + (size_t)((Ta + u) * 32 + 4 * w + (lane >> 4)) * D + js * 64 + i16 * 4);
      dcoef = bflo(ssd[c0 + ((lane >> 4) & 1)]);
    };
    if (w < 4) {
      v4u ug[4];
      auto u_load = [&]() AI {
#pragma unroll
        for (int i = 0; i < 4; ++i) ug[i] = ld128<SC1>(rs, tid * 16, OFF_KVG + c0 * 8192 + i * 4096);
      };
      __builtin_amdgcn_s_sleep(DELAY1);
      u_load();
      SPIN(tag_ok(ug[0]) && tag_ok(ug[1]) && tag_ok(ug[2]) && tag_ok(ug[3]), u_load());
#pragma unroll
      for (int i = 0; i < 4; ++i) *(v2u*)(sm + CV_U + (i >> 1) * CV_USTR + (i & 1) * 2048 + tid * 8) = v2u{ug[i][0], ug[i][2]};
      own_load();
    } else {
      if (WD) {
        __builtin_amdgcn_s_sleep(WD);
        const rsrc_t rg = mk_rsrc(wg, D * F * 2), ru = mk_rsrc(wu, D * F * 2);
        const int vo = ((ks * 16 + rl) * F + fb * 64 + qq * 8) * 2;
        wa[0] = ld128(rg, vo, 0); wa[1] = ld128(rg, vo, 16 * F); wa[2] = ld128(ru, vo, 0); wa[3] = ld128(ru, vo, 16 * F);
      }
      own_load();
      unsigned char* wb = sm + CV_W + (w - 4) * (32 * 144) + rl * 144 + qq * 16;
      *(v4u*)wb = wa[0]; *(v4u*)(wb + 8 * 144) = wa[1]; *(v4u*)(wb + 16 * 144) = wa[2]; *(v4u*)(wb + 24 * 144) = wa[3];
#pragma unroll
      for (int fgl = 0; fgl < 4; ++fgl) {
        const v4u o = tr_frag(L + CV_W + (w - 4) * (32 * 144) + ((lane >> 4) & 1) * (16 * 144) + tr_off + fgl * 32, 4 * 144);
        st128<SC1>(o, rs, bid < 196 ? l16 : OOB, OFF_W1 + ((fb * 4 + fgl) * KS1 + ks) * 1024);
        const float cs = dot2(o[3], ONES, dot2(o[2], ONES, dot2(o[1], ONES, dot2(o[0], ONES, 0.f))));
        st32<SC1>(asu(half_sum(cs)), rs, (bid < 196 && lane < 32) ? lane * 4 : OOB, OFF_CSP + (ks * 112 + fb * 4 + fgl) * 128);
      }
      DRAIN();
      st64<SC1>(v2u{sopq(tag0), sopq(tag1)}, rs, (bid < 196 && lane < 16) ? lane * 8 : OOB, OFF_FW1 + (fb * 32 + ks) * 128);
    }
    STAMP(1, 22);
    auto fw_poll = [&]() AI { return ld64<SC1>(rs, min(opaque(lane), 27) * 128, OFF_FW1 + (fg >> 2) * 4096); };
    __syncthreads();
    STAMP(1, 3);
    float r0[2], c0n[2];
    v2u fw;
    {
      const int cw = w >> 2, sp = w & 3;
      const int gq = lane >> 4;
      const int sg = (1 + i16) & 3;
      const unsigned char* fbp = sm + CV_H + (cw * 4 + sg) * CV_HSTR + (1 + i16 + 8 * gq - sg) * 2 + sp * 128;
      const unsigned char* gb = sm + CV_U + cw * CV_USTR + (2032 - 16 * i16 + 8 * gq) * 2 + sp * 128 - 3584;
      auto rdF = [&](int n) AI -> v4u {
        const v2u lo = *(const v2u*)(fbp + (n >> 3) * 64 + (n & 7) * 512), hi = *(const v2u*)(fbp + (n >> 3) * 64 + (n & 7) * 512 + 8);
        return v4u{lo[0], lo[1], hi[0], hi[1]};
      };
      f32x4 acc[8];
#pragma unroll
      for (int i = 0; i < 8; ++i) acc[i] = f32x4{0.f, 0.f, 0.f, 0.f};
      v4u Gf[8], Ff[3];
      Ff[0] = rdF(0);
#pragma unroll
      for (int dl = 7; dl >= 0; --dl) Gf[dl] = *(const v4u*)(gb + (7 - dl) * 512);
      Ff[1] = rdF(1);
      SBAR();
      static_for<0, 16>([&](auto N) AI {
        constexpr int n = N.value, jj = n & 7;
        if constexpr (n + 2 < 16) Ff[(n + 2) % 3] = rdF(n + 2);
#pragma unroll
        for (int dl = 7 - jj; dl >= 0; --dl) { acc[jj + dl] = mfma16(Ff[n % 3], Gf[dl], acc[jj + dl]); asm volatile("" : "+v"(acc[jj + dl])); }
        if constexpr (n < 8) Gf[7 - jj] = *(const v4u*)(gb + 64 + jj * 512);
        if constexpr (n == FWP) fw = fw_poll();
        if constexpr (n == FWC) { SPIN(flag_ok(fw), fw = fw_poll()); ORDER(); }
        if constexpr (n >= FWC) ld_wgt(n - FWC);
        const v4u ev = {EMPTY, EMPTY, EMPTY, EMPTY};
        if constexpr (n == 1) st128<SC1>(ev, rs, (tid >> 8) * XBUF + (tid & 255) * 16, OFF_X + (Ta * 7 + js) * 4096);
        if constexpr (n == 2)
          st128<SC1>(ev, rs, w < 4 ? OFF_SP + w * SP_BYTES + ((Ta + hh) * 7 + js) * 512 + tokl * 16
                           : w < 6 ? OFF_FH + (((w - 4) * 64 + g * 4 + (lane >> 4)) * KS2 + j * 8) * FSTR + i16 * 16 : OOB, 0);
        SBAR();
      });
      STAMP(1, 4);
      const int slot = (4 * i16 + gq) ^ ((i16 >> 1) & 3);
#pragma unroll
      for (int i = 0; i < 8; ++i) *(f32x4*)(sm + CV_RED + ((w * 8 + i) * 64 + slot) * 16) = acc[i];
    }
    const int fch = (lane >> 4) & 1;
    v2u ur[2];
#pragma unroll
    for (int i = 0; i < 2; ++i) ur[i] = *(const v2u*)(sm + CV_U + fch * CV_USTR + (2044 - (256 * w + (2 * i + hh) * 64 + i16 * 4)) * 2);
    wait_vm<16 - FWC>();
    __syncthreads();
    STAMP(1, 5);
#pragma unroll
    for (int i = 0; i < 2; ++i) {
      const int qi = (2 * i + hh) * 16 + i16, sl = qi ^ ((qi >> 3) & 3);
      f32x4 y = *(const f32x4*)(sm + CV_RED + ((fch * 32 + w) * 64 + sl) * 16);
#pragma unroll
      for (int sp = 1; sp < 4; ++sp) y += *(const f32x4*)(sm + CV_RED + (((fch * 4 + sp) * 8 + w) * 64 + sl) * 16);
      unsigned yy[2];
#pragma unroll
      for (int e2 = 0; e2 < 2; ++e2) {
        const unsigned cb = pk2(y[2 * e2], y[2 * e2 + 1]);
        const unsigned ud = pk2(bfhi(ur[i][1 - e2]) * dcoef, bflo(ur[i][1 - e2]) * dcoef);
        yy[e2] = pk2(bflo(cb) + bflo(ud), bfhi(cb) + bfhi(ud));
      }
      st128<SC1>(v4u{yy[0], sopq(tag1), yy[1], sopq(tag0)}, rs, fch * 8192 + hh * 256 + i16 * 16, OFF_KVG + c0 * 8192 + (4 * w + 2 * i) * 256);
    }
    SBAR();
    STAMP(1, 24);
    sp_publish(0, 0);
    sp_publish(0, 1);
    raw_barrier();
#pragma unroll
    for (int i = 16 - FWC; i < NPRE; ++i) ld_wgt(i);
    SBAR();
    STAMP(1, 23);
    {
      v4u yg[2];
      auto y_load = [&]() AI {
        yg[0] = ld128<SC1>(rs, (tid >> 4) * 8192 + i16 * 16, OFF_KVG + js * 64 * 8192 + (Ta >> 1) * 256);
        yg[1] = ld128<SC1>(rs, (tid >> 4) * 8192 + i16 * 16, OFF_KVG + (js * 64 + 32) * 8192 + (Ta >> 1) * 256);
      };
      __builtin_amdgcn_s_sleep(DELAY2);
      y_load();
      v4u sp0 = sp_load(0, Ta), sp1 = sp_load(0, Ta + 1), spo = sp_load(0, Ta ^ 2);
      SPIN(__all(((yg[0][1] ^ tag1) | (yg[0][3] ^ tag0) | (yg[1][1] ^ tag1) | (yg[1][3] ^ tag0)) == 0u), y_load());
      STAMP(1, 27);
#pragma unroll
      for (int i = 0; i < 2; ++i) {
        const int cl = i * 32 + (tid >> 4);
        *(v2u*)(sm + L_XB + XBUF + cl * 128 + ((i16 ^ (cl >> 2)) * 8)) = v2u{yg[i][0], yg[i][2]};
      }
      { float m; sp_stats(0, 0, sp0, m, r0[0]); c0n[0] = -m * r0[0]; sp_stats(0, 1, sp1, m, r0[1]); c0n[1] = -m * r0[1]; }
      SPIN(tag_ok(spo), spo = sp_load(0, Ta ^ 2));
      __syncthreads();
      STAMP(1, 25);
#pragma unroll
      for (int u = 0; u < 2; ++u) {
        const uint16_t* gp = (const uint16_t*)(sm + L_XB + XBUF + i16 * 512 + (((u * 8 + w) ^ i16) * 8) + (lane >> 4) * 2);
        v2u yv = {(unsigned)gp[0] | ((unsigned)gp[64] << 16), (unsigned)gp[128] | ((unsigned)gp[192] << 16)};
        asm volatile("" : "+v"(yv));
        yres[u] = yv;
        gate_publish(0, u, r0[u], c0n[u]);
      }
    }
    SBAR();
    STAMP(1, 26);
    raw_barrier();
#pragma unroll
    for (int i = 0; i < 14; ++i) csp[i] = ld32<SC1>(rs, (hh * 3584 + tokl) * 4, OFF_CSP + (i * 2 * 3584 + fg * 32) * 4);
#pragma unroll
    for (int i = NPRE; i < NPRE2; ++i) ld_wgt(i);
    SBAR();
  }

  v4u xs[4];
  v2u sst;
  auto tile_of = [&](int t) AI { return g * 4 + (((t & 1) ^ hs) << 1) + (t >> 1); };
  auto x_src = [&](int ly, int t) AI { return OFF_X + ly * ACT_BYTES + tile_of(t) * XBUF + sj * 4096; };
  auto s_load = [&](int ly, int t) AI { sst = ld64<SC1>(rs, (l16 & 511) + (l16 >> 9) * 8, OFF_SP + (2 * ly + 1) * SP_BYTES + (tile_of(t) * 7 + sj) * 512); };
  auto x0_load = [&]() AI {
#pragma unroll
    for (int i = 0; i < 4; ++i) xs[i] = ldfrag<SC1>(rs, l16, x_src(1, 0), i);
    s_load(1, 0);
  };

  auto layer = [&](auto LY) AI {
    constexpr int ly = decltype(LY)::value;
    const int lane = opaque(tid & 63), tokl = lane & 31, hh = lane >> 5;
    auto x_ok = [&]() AI {
      unsigned mx = 0u;
#pragma unroll
      for (int i = 0; i < 4; ++i) mx = umax3(umax3(mx, xs[i][0], xs[i][1]), xs[i][2], xs[i][3]);
      return __all((mx != EMPTY) & (sst[1] == (hh ? tag1 : tag0)));
    };
    auto x_dma = [&](int t, int half) AI {
      const unsigned dst = lds0 + L_XB + (t & 1) * XBUF + sj * 4096;
      if (half) { dma2<true, 2048>(rs, l16, x_src(ly, t), dst); s_load(ly, t); }
      else dma2<true, 0>(rs, l16, x_src(ly, t), dst);
    };
    auto x_fetch = [&](int t) AI {
      unsigned spins = 0;
      bool ok;
#pragma unroll 1
      do {
        x_dma(t, 0); x_dma(t, 1); DRAIN();
        unsigned mx = 0u;
#pragma unroll
        for (int i = 0; i < 4; i += 2) {
          const v4u a = *(const v4u*)(sm + L_XB + (t & 1) * XBUF + sj * 4096 + i * 1024 + l16), b = *(const v4u*)(sm + L_XB + (t & 1) * XBUF + sj * 4096 + i * 1024 + 1024 + l16);
          mx = umax3(umax3(a[0], a[1], a[2]), umax3(a[3], b[0], b[1]), umax3(b[2], b[3], mx));
          asm volatile("" : "+v"(mx) :: "memory");
        }
        ok = __all((mx != EMPTY) & (sst[1] == (hh ? tag1 : tag0)));
      } while (!ok && ++spins < SPIN_CAP);
    };
    auto st_write = [&](int t) AI {
      unsigned char* sp = sm + L_ST + (t % 3) * 2048 + (sj >> 2) * 512 + tokl * 16 + (sj & 3) * 4;
      *(unsigned*)(sp + hh * 1024) = sst[0];
    };
    const int xrd = tokl * 128 + ((hh ^ ((tokl >> 1) & 7)) << 4);
    auto xfrag = [&](int t, int ks) AI -> v4u { return *(const v4u*)(sm + L_XB + (t & 1) * XBUF + (xrd ^ ((ks & 3) << 5)) + (ks >> 2) * 4096); };
    const int hbase = OFF_H + ly * H_BYTES;
    v4u hf[14];
    auto h_load = [&](int u, int i) AI { hf[i] = ldfrag<SC1>(rs, l16, hbase + ((Ta + u) * KS2 + (i < 7 ? kse : ksl)) * 1024, i % 7); };
    v2u tagv = {tag0, tag1};
    asm volatile("" : "+v"(tagv));
    auto h_flag = [&](int t) AI { st64<SC1>(tagv, rs, opaque(l16) == 0 ? 0 : OOB, OFF_FH + ((ly * 64 + tile_of(t)) * KS2 + fg) * FSTR); };
    auto hf_poll = [&](int u, int half) AI { return ld64<SC1>(rs, min(opaque(l16) * (FSTR / 16), 6 * FSTR), OFF_FH + ((ly * 64 + Ta + u) * KS2 + (half ? ksl : kse)) * FSTR); };
    auto hf_check = [&](int u, int half, v2u v) AI { SPIN(flag_ok(v), v = hf_poll(u, half)); ORDER(); };
    v2u hpl;

    v4u sgr;
    auto cs_write = [&]() AI {
      const int ln = opaque(lane), csrow = (ln & 16) + 8 * ((ln >> 2) & 1) + (ln & 3) + 4 * ((ln >> 3) & 1);
      *(float*)(sm + L_CS + w * 128 + csrow * 4) = csum;
    };
    if (ly == 0) {
      __builtin_amdgcn_s_sleep(DELAY3);
      x_fetch(0);
      STAMP(1, 17);
      st_write(0);
      STAMP(1, 6);
      __syncthreads();
      x_dma(1, 0); x_dma(1, 1);
    } else {
      SPIN(x_ok(), x0_load());
      STAMP(1, 30);
      __syncthreads();
      cs_write();
#pragma unroll
      for (int i = 0; i < 4; ++i) *(v4u*)(sm + L_XB + sj * 4096 + i * 1024 + l16) = xs[i];
      st_write(0);
      sgr = sp_load(2, Ta + 1);
      x_dma(1, 0); x_dma(1, 1);
      asm volatile("s_waitcnt lgkmcnt(0)" ::: "memory");
      raw_barrier();
    }
    STAMP(1, 7 + ly * 24);
    if (ly == 1) STAMPC(1, 62);

#define FST(q)
    f32x16 acc[2];
    constexpr int PD = PDV;
    v4u xf[PD], ho;
    f32x4 p0, p1, p2, p3;
    float mean = 0.f, r2[2];
#pragma unroll
    for (int i = 0; i < PD; ++i) xf[i] = xfrag(0, i + (ly == 1 ? NDROP : 0));
    auto ep_cs = [&]() AI {
      const unsigned char* cp = sm + L_CS + w * 128 + hh * 32;
      p0 = *(const f32x4*)cp; p1 = *(const f32x4*)(cp + 64); p2 = *(const f32x4*)(cp + 16); p3 = *(const f32x4*)(cp + 80);
    };
    float hv[2], sq = 0.f;
    auto ep_one = [&](const f32x16& a, int i) AI {
      hv[i & 1] = fmaxf(fmaf(mean, i < 4 ? p0[i & 3] : p2[i & 3], a[i]), 0.f) * fmaf(mean, i < 4 ? p1[i & 3] : p3[i & 3], a[8 + i]);
      asm volatile("" : "+v"(hv[i & 1]));
    };
    auto h_store = [&](int t) AI { st128<SC1>(ho, rs, l16, hbase + (tile_of(t) * KS2 + fg) * 1024); };

    static_for<0, 4>([&](auto TT) AI {
      constexpr int t = TT.value;
      constexpr int rot = (ly == 1 && t == 0) ? NDROP : 0;
      v4u cb0, cb1;
      unsigned cmx = 0u;
      float ga = 0.f, gb = 0.f;
      static_for<0, KS1>([&](auto II) AI {
        constexpr int i = II.value, n = t * KS1 + i;
        if constexpr (i == 0) {
          f32x16 z;
#pragma unroll
          for (int e = 0; e < 16; ++e) z[e] = 0.f;
          acc[t & 1] = mfma32(W1[rot], xf[n % PD], z);
        } else acc[t & 1] = mfma32(W1[(i + rot) % KS1], xf[n % PD], acc[t & 1]);
        asm volatile("" : "+v"(acc[t & 1]));
        SBAR();
        if constexpr (i + PD < KS1) xf[n % PD] = xfrag(t, (i + PD + rot) % KS1);
        else if constexpr (t < 3) xf[n % PD] = xfrag(t + 1, i + PD - KS1);
        if constexpr (t == 2 && i == 0) { wait_vm<5 + (ly == 0 ? 6 : 0)>(); h_flag(0); }
        if constexpr (t == 2 && i == 24) { wait_vm<(ly == 0 ? 6 : 1)>(); h_flag(1); }
        if constexpr (t == 3 && i == 23) { wait_vm<6>(); h_flag(2); }
        if constexpr (t > 0) {
          if constexpr (i >= 3 && i <= 9 && (i & 1)) { ho[(i - 3) / 2] = pk2(hv[0], hv[1]); asm volatile("" : "+v"(ho[(i - 3) / 2])); }
          if constexpr (i >= 1 && i <= 8) ep_one(acc[(t - 1) & 1], i - 1);
          if constexpr (i == H_ST) h_store(t - 1);
        }
        if constexpr (t < 3) {
          const unsigned char* xb = sm + L_XB + ((t + 1) & 1) * XBUF + sj * 4096 + l16;
          if constexpr (i == XC) {
            wait_vm<(t > 0) + (t == 2) + (t == 0 ? (ly == 1 ? 8 : 2 + (KS1 - NPRE2 < XC ? KS1 - NPRE2 : XC)) : 0) + (ly == 0 && t > 0 ? 4 : 0)>();
            asm volatile("" : "+v"(sst));
            cb0 = *(const v4u*)xb; cb1 = *(const v4u*)(xb + 1024);
          }
          if constexpr (i == XC + 3) {
            cmx = umax3(umax3(cb0[0], cb0[1], cb0[2]), umax3(cb0[3], cb1[0], cb1[1]), max(cb1[2], cb1[3]));
            asm volatile("" : "+v"(cmx));
          }
          if constexpr (i == XC + 4) {
            cb0 = *(const v4u*)(xb + 2048); cb1 = *(const v4u*)(xb + 3072);
            cmx = sst[1] != (hh ? tagv[1] : tagv[0]) ? EMPTY : cmx;
            asm volatile("" : "+v"(cmx));
          }
          if constexpr (i == XC + 7) {
            cmx = umax3(umax3(cb0[0], cb0[1], cb0[2]), umax3(cb0[3], cb1[0], cb1[1]), umax3(cb1[2], cb1[3], cmx));
            asm volatile("" : "+v"(cmx));
          }
          if constexpr (i == XC + 8) {
            if (__builtin_expect(!__all(cmx != EMPTY), 0)) x_fetch(t + 1);
            st_write(t + 1);
          }
        }
        if constexpr (ly == 0 && t == 0 && i == 4)
          st128<SC1>(v4u{EMPTY, EMPTY, EMPTY, EMPTY}, rs, l16, OFF_X + ACT_BYTES + ((Ta + (w >> 2)) * 7 + js) * 4096 + (w & 3) * 1024);
        if constexpr (ly == 0 && t == 0 && NPRE2 + i < KS1) ld_wgt(NPRE2 + i);
        if constexpr (ly == 0 && t == 0) {
          auto fw2_poll = [&]() AI { const int ln = opaque(lane); return ld64<SC1>(rs, (((ln & 8) ? ksl : kse) + min(ln & 7, 6)) * 128, OFF_FW2 + js * KS2 * 128); };
          if constexpr (i == 6) hpl = fw2_poll();
          if constexpr (i == BAR) { asm volatile("" : "+v"(W1[KS1 - 1])); SPIN(flag_ok(hpl), hpl = fw2_poll()); ORDER(); }
        }
        if constexpr (ly == 0 && t == 0 && i == 1) {
          float cs = 0.f;
#pragma unroll
          for (int q7 = 0; q7 < 14; ++q7) cs += asf(csp[q7]);
          csum = half_sum(cs);
          cs_write();
        }
        if constexpr (ly == 0 && (t == 1 || t == 2) && (i == 2 || i == 6 || i == 12)) ld_wgt(KS1 + (t - 1) * 4 + i / 5);
        if constexpr (ly == 0 && (t == 1 || t == 2) && i == 21) ld_wgt(KS1 + (t - 1) * 4 + 3);
        if constexpr (ly == 0 && t == 2 && i >= 24 && i < 27) ld_wgt(KS1 + 8 + i - 24);
        if constexpr (wl_slot(ly, t, i) >= 0) {
          constexpr int qn = wl_slot(ly, t, i), f = NWS + qn;
          dma1(rs, l16, wd_src(f), lds0 + L_WD + (w * NWL + qn) * 1024);
        }
        if constexpr (ly == 1 && t == 0) {
          constexpr int st = i - X3S;
          if constexpr (st == 0) SPIN(tag_ok(sgr), sgr = sp_load(2, Ta + 1));
          if constexpr (st == 1) { ga = i16 < 7 ? asf(sgr[0]) : 0.f; gb = i16 < 7 ? asf(sgr[2]) : 0.f; }
          if constexpr (st == 2 || st == 9) { ga = dpp_add<0x128>(ga); gb = dpp_add<0x128>(gb); PIN2(ga, gb); ga = dpp_add<0x124>(ga); gb = dpp_add<0x124>(gb); }
          if constexpr (st == 3 || st == 10) { ga = dpp_add<0x122>(ga); gb = dpp_add<0x122>(gb); PIN2(ga, gb); ga = dpp_add<0x121>(ga); gb = dpp_add<0x121>(gb); }
          if constexpr (st == 4) { float m, r; row_stats(ga, gb, m, r); ga = r; gb = -m * r; }
          if constexpr (st == 5 || st == 6) {
            constexpr int e = st - 5;
            const float xl = bflo(xres[1][e]), xh = bfhi(xres[1][e]);
            float ol = fmaf(fmaf(xl, ga, gb), bflo(yres[1][e]), xl), oh = fmaf(fmaf(xh, ga, gb), bfhi(yres[1][e]), xh);
            PIN2(ol, oh);
            unsigned o = nz(pk2(ol, oh));
            asm volatile("" : "+v"(o));
            xres[1][e] = o;
          }
          if constexpr (st == 7) st64<SC1>(xres[1], rs, (lane * 8) ^ (((2 * w + hh) & 7) << 4), OFF_X + ACT_BYTES + ((Ta + 1) * 7 + js) * 4096 + w * 512);
          if constexpr (st == 8) { ga = dot2(xres[1][0], ONES, dot2(xres[1][1], ONES, 0.f)); gb = dot2(xres[1][0], xres[1][0], dot2(xres[1][1], xres[1][1], 0.f)); }
          if constexpr (st == 11) st128<SC1>(v4u{asu(ga), tagv[0], asu(gb), tagv[1]}, rs, (lane & 15) == 0 ? lane : OOB, OFF_SP + 3 * SP_BYTES + ((Ta + 1) * 7 + js) * 512 + w * 64);
          if constexpr (st >= 1 && st <= 10) PIN2(ga, gb);
          if constexpr (i == BAR) asm volatile("" : "+v"(W1[NDROP - 1]));
        }
        if constexpr (ly == 1 && t == 0 && i >= 3 && i < 15 && (i & 1)) W1[8 + (i - 3) / 2] = ldfrag<SC1>(rs, l16, w1o, 8 + (i - 3) / 2);
        if constexpr (t < 2 && (i == BAR + 1 || i == BAR + 3)) x_dma(t + 2, (i - BAR - 1) / 2);
        if constexpr (t == 2 && i == 15) hpl = hf_poll(0, 0);
        if constexpr (t == 3) {
          if constexpr (i == 0) hf_check(0, 0, hpl);
          if constexpr (i == 7) hpl = hf_poll(0, 1);
          if constexpr (i == 16) hf_check(0, 1, hpl);
          if constexpr (i < 14 && !(i & 1)) h_load(0, i / 2);
          if constexpr (i >= 16 && !(i & 1)) h_load(0, 7 + (i - 16) / 2);
        }
        if constexpr (i == 11) {
          const unsigned char* sp = sm + L_ST + (t % 3) * 2048 + tokl * 16;
          p0 = *(const f32x4*)sp; p1 = *(const f32x4*)(sp + 512);
        }
        if constexpr (i == 13) { p0[0] += p0[1]; p0[2] += p0[3]; p1[0] += p1[1]; asm volatile("" : "+v"(p0[0]), "+v"(p0[2]), "+v"(p1[0])); }
        if constexpr (i == 15) { mean = ((p0[0] + p0[2]) + (p1[0] + p1[2])) * (-1.0f / D); asm volatile("" : "+v"(mean)); }
        if constexpr (!(t & 1)) {
          if constexpr (i == 16) { const unsigned char* sp = sm + L_ST + (t % 3) * 2048 + tokl * 16; p2 = *(const f32x4*)(sp + 1024); p3 = *(const f32x4*)(sp + 1536); }
          if constexpr (i == 19) { p2[0] += p2[1]; p2[2] += p2[3]; p3[0] += p3[1]; asm volatile("" : "+v"(p2[0]), "+v"(p2[2]), "+v"(p3[0])); }
          if constexpr (i == 20) { sq = (p2[0] + p2[2]) + (p3[0] + p3[2]); asm volatile("" : "+v"(sq)); }
          if constexpr (i == 26) { sq = __builtin_amdgcn_rcpf(fmaxf(fmaf(sq, 1.0f / D, -mean * mean), 0.0f) + EPS); asm volatile("" : "+v"(sq)); }
          if constexpr (i == 27) r2[t >> 1] = asf(__builtin_amdgcn_ds_bpermute(w * 16 + (lane >> 4) * 4, asu(sq)));
        }
        if constexpr (i == 25) ep_cs();
        if constexpr ((XB & 1) && t == 3 && i == BAR) raw_barrier();
        if constexpr ((XB & 2) && t == 3 && i == 11) raw_barrier();
        if constexpr ((XB & 4) && t < 3 && i == 11) raw_barrier();
        if constexpr (i == BAR && t < 3) {
          asm volatile("s_waitcnt lgkmcnt(1)" ::: "memory");
          raw_barrier();
        }
        SBAR();
      });
      STAMP(1, 8 + t + ly * 24);
      if (ly == 1 && t == 2) STAMPC(1, 63);
    });
    v4u sga;
    auto unit = [&](auto UU) AI {
      constexpr int u = UU.value;
      v4u wl[3];
      int wad = 0, rad = 0;
      auto red_write = [&](int jq) AI {
        const f32x16& a = acc[jq >> 2];
        const int qd = jq & 3;
        *(f32x4*)(sm + L_RED + (wad ^ ((4 * (jq >> 1) + (jq & 1)) << 4)) + jq * 1024) = f32x4{a[4 * qd], a[4 * qd + 1], a[4 * qd + 2], a[4 * qd + 3]};
      };
      static_for<0, 28>([&](auto FF) AI {
        constexpr int f = FF.value, ct = (f / 7) & 1, hi = f % 7 + 7 * (f / 14);
        v4u wf;
        if constexpr (f < NWS) wf = Wd[f]; else if constexpr (f < NWS + NWL) wf = wl[f % 3]; else wf = Wd[f - NWL];
        if constexpr (f == 0 || f == 7) {
          f32x16 z;
#pragma unroll
          for (int e = 0; e < 16; ++e) z[e] = 0.f;
          acc[ct] = mfma32(wf, hf[hi], z);
        } else acc[ct] = mfma32(wf, hf[hi], acc[ct]);
        asm volatile("" : "+v"(acc[ct]));
        SBAR();
        if constexpr (f + 3 >= NWS && f + 3 < NWS + NWL) wl[f % 3] = *(const v4u*)(sm + L_WD + (w * NWL + (f + 3 - NWS)) * 1024 + l16);
        if constexpr (u == 0) {
          if constexpr (f < 6) {
            constexpr int eb[7] = {0, 1, 3, 4, 6, 7, 8};
#pragma unroll
            for (int e = eb[f]; e < eb[f + 1]; ++e) { ep_one(acc[1], e); if (e & 1) ho[e >> 1] = pk2(hv[0], hv[1]); }
          }
          if constexpr (f == 6) h_store(3);
          if constexpr (f == 9) hpl = hf_poll(1, 0);
          if constexpr (f == 18) { wait_vm<5>(); h_flag(3); }
          if constexpr (f == 19) hf_check(1, 0, hpl);
          if constexpr (f == 0) h_load(0, 13);
          if constexpr (f >= 2 && f < 2 + 2 * (NWR - NWS) && !(f & 1)) Wd[NWS + (f - 2) / 2] = ld_wd(NWL + NWS + (f - 2) / 2);
          if constexpr (f >= 19 && (f & 1)) h_load(1, (f - 19) / 2);
        }
        if constexpr (u == 1 && pace(f) >= 0 && pace(f) < 3) h_load(1, 11 + pace(f));
        if constexpr (u == 1 && ly == 0) {
          if constexpr (pace(f) >= 3) W1[pace(f) - 3] = ldfrag<SC1>(rs, l16, w1o, pace(f) - 3);
          if constexpr (f == SGA) sga = sp_load(2, Ta);
        }
        if constexpr (f == 22) { const int ln = opaque(lane); wad = opaque(w * 8192 + ((ln ^ ((ln >> 5) << 1)) << 4)); }
        if constexpr (f == 27) {
          const int ln = opaque(lane), li16 = ln & 15, ftok = 4 * w + (ln >> 4);
          rad = opaque((2 * (li16 >> 2) + (li16 & 1)) * 1024 + (((li16 >> 1) & 1) * 32 + (ftok ^ li16)) * 16);
        }
        if constexpr (f >= 23 && f < 27) red_write(f - 23);
        if constexpr ((XB & 8) && f == 17) raw_barrier();
        if constexpr (f == GB) __syncthreads();
        SBAR();
      });
      STAMP(1, 13 + u * 5 + ly * 24);
      if constexpr (u == 1 && ly == 0) { x3_publish(0, sga); SBAR(); STAMP(1, 46); }
#pragma unroll
      for (int jq = 4; jq < 8; ++jq) red_write(jq);
      SBAR();
      if constexpr (u == 0) { h_load(1, 5); h_load(1, 6); hpl = hf_poll(1, 1); }
      SBAR();
      STAMP(1, 14 + u * 5 + ly * 24);
      __syncthreads();
      STAMP(1, 15 + u * 5 + ly * 24);
      if constexpr (u == 1 && ly == 0) x0_load();
      {
        f32x4 a = *(const f32x4*)(sm + L_RED + rad);
#pragma unroll
        for (int ww = 1; ww < 8; ++ww) a += *(const f32x4*)(sm + L_RED + rad + ww * 8192);
        const unsigned f01 = pk2(a[0] * r2[u], a[1] * r2[u]), f23 = pk2(a[2] * r2[u], a[3] * r2[u]);
        const v2u xn = {pk2(bflo(xres[u][0]) + bflo(f01), bfhi(xres[u][0]) + bfhi(f01)), pk2(bflo(xres[u][1]) + bflo(f23), bfhi(xres[u][1]) + bfhi(f23))};
        if constexpr (ly == 1) {
          const int ln = opaque(lane);
          st64(xn, mk_rsrc(out, ACT_BYTES), (ln >> 4) * (D * 2) + (ln & 15) * 8, (((Ta + u) * 32 + 4 * w) * D + js * 64) * 2);
        } else {
          xres[u] = xn;
          sp_publish(2, u);
        }
      }
      SBAR();
      if constexpr (u == 0) {
        hf_check(1, 1, hpl);
#pragma unroll
        for (int i = 7; i < 11; ++i) h_load(1, i);
        SBAR();
      }
      STAMP(1, 16 + u * 5 + ly * 24);
    };
    unit(IC<0>{});
    unit(IC<1>{});
  };
  layer(IC<0>{});
  layer(IC<1>{});
  st64<SC1>(v2u{sopq(tag0), sopq(tag1)}, rs, (bid == 0 && tid == 0) ? 0 : OOB, OFF_EP);
  STAMP(1, 60);
}

extern "C" void kernel_launch(void* const* d_in, const int* in_sizes, int n_in, void* d_out, int out_size, void* d_ws,
                              size_t ws_size, hipStream_t stream) {
  const uint16_t* q = (const uint16_t*)d_in[0];
  const uint16_t* k = (const uint16_t*)d_in[1];
  const uint16_t* v = (const uint16_t*)d_in[2];
  const uint16_t* shk = (const uint16_t*)d_in[3];
  const uint16_t* ssk = (const uint16_t*)d_in[4];
  const uint16_t* ssd = (const uint16_t*)d_in[5];
  const uint16_t* wg = (const uint16_t*)d_in[7];
  const uint16_t* wu = (const uint16_t*)d_in[8];
  const uint16_t* wd = (const uint16_t*)d_in[9];
  unsigned char* ws = (unsigned char*)d_ws;
  uint16_t* out = (uint16_t*)d_out;
  (void)hipFuncSetAttribute((const void*)k_main, hipFuncAttributeMaxDynamicSharedMemorySize, L_BYTES);
  k_main<<<NAUX + 224, 512, L_BYTES, stream>>>(q, k, v, shk, ssk, ssd, wg, wu, wd, ws, out);
}
```

```cpp
#include <hip/hip_runtime.h>
#include <hip/hip_bf16.h>
#include <stdint.h>

#define DI __device__ __forceinline__
#define AI __attribute__((always_inline))
#define LAS __attribute__((address_space(3)))
typedef unsigned v4u __attribute__((ext_vector_type(4)));
typedef unsigned v2u __attribute__((ext_vector_type(2)));
typedef float f32x4 __attribute__((ext_vector_type(4)));
typedef float f32x2 __attribute__((ext_vector_type(2)));
typedef float f32x16 __attribute__((ext_vector_type(16)));
typedef short bf16x8 __attribute__((ext_vector_type(8)));
typedef short s16x4 __attribute__((ext_vector_type(4)));
typedef __bf16 bf2 __attribute__((ext_vector_type(2)));
typedef __amdgpu_buffer_rsrc_t rsrc_t;

constexpr int S = 2048, D = 448, F = 1792;
constexpr int KS1 = D / 16;
constexpr int KS2 = F / 16;
constexpr float EPS = 1e-5f;

constexpr int ACT_BYTES = S * D * 2;
constexpr int FSTR = 32;
constexpr int OFF_EP = 0;
constexpr int OFF_FW1 = 256;
constexpr int OFF_FW2 = OFF_FW1 + 28 * 32 * 128;
constexpr int OFF_FH = OFF_FW2 + 7 * KS2 * 128;
constexpr int OFF_SP = OFF_FH + 2 * 64 * KS2 * FSTR;
constexpr int SP_BYTES = 64 * 7 * 32 * 16;
constexpr int OFF_KVG = OFF_SP + 4 * SP_BYTES;
constexpr int OFF_X = OFF_KVG + 2 * ACT_BYTES;
constexpr int OFF_H = OFF_X + 2 * ACT_BYTES;
constexpr int H_BYTES = 64 * KS2 * 1024;
constexpr int OFF_W1 = OFF_H + 2 * H_BYTES;
constexpr int OFF_W2 = OFF_W1 + 112 * KS1 * 1024;
constexpr int OFF_CSP = OFF_W2 + 14 * KS2 * 1024;
constexpr int WS_BYTES = OFF_CSP + 28 * 112 * 32 * 4;
static_assert(OFF_SP % 128 == 0 && OFF_KVG % 128 == 0, "alignment");

constexpr int SC1 = 16;
DI rsrc_t mk_rsrc(const void* p, int bytes) { return __builtin_amdgcn_make_buffer_rsrc((void*)p, 0, bytes, 0x00020000); }
template <int AUX = 0> DI v4u ld128(rsrc_t r, int voff, int soff) { return __builtin_amdgcn_raw_buffer_load_b128(r, voff, soff, AUX); }
template <int AUX = 0> DI v2u ld64(rsrc_t r, int voff, int soff) { return __builtin_amdgcn_raw_buffer_load_b64(r, voff, soff, AUX); }
template <int AUX = 0> DI unsigned ld32(rsrc_t r, int voff, int soff) { return __builtin_amdgcn_raw_buffer_load_b32(r, voff, soff, AUX); }
template <int AUX = 0> DI unsigned ld16(rsrc_t r, int voff, int soff) { return __builtin_amdgcn_raw_buffer_load_b16(r, voff, soff, AUX); }
template <int AUX = 0> DI void st128(v4u v, rsrc_t r, int voff, int soff) {
  __builtin_amdgcn_raw_buffer_store_b128(v, r, voff, soff, AUX);
  asm volatile("s_nop 1" ::"v"(v));
}
template <int AUX = 0> DI void st64(v2u v, rsrc_t r, int voff, int soff) { __builtin_amdgcn_raw_buffer_store_b64(v, r, voff, soff, AUX); }
template <int AUX = 0> DI void st32(unsigned v, rsrc_t r, int voff, int soff) { __builtin_amdgcn_raw_buffer_store_b32(v, r, voff, soff, AUX); }
DI float asf(unsigned u) { return __builtin_bit_cast(float, u); }
DI unsigned asu(float f) { return __builtin_bit_cast(unsigned, f); }
DI float bflo(unsigned u) { return asf(u << 16); }
DI float bfhi(unsigned u) { return asf(u & 0xffff0000u); }
DI unsigned pk2(float a, float b) {
  f32x2 v = {a, b};
  return __builtin_bit_cast(unsigned, __builtin_convertvector(v, bf2));
}
DI float rbf(float a) { return bflo(pk2(a, a)); }
DI float dot2(unsigned a, unsigned b, float c) {
  return __builtin_amdgcn_fdot2_f32_bf16(__builtin_bit_cast(bf2, a), __builtin_bit_cast(bf2, b), c, false);
}
DI f32x16 mfma32(v4u a, v4u b, f32x16 c) {
  return __builtin_amdgcn_mfma_f32_32x32x16_bf16(__builtin_bit_cast(bf16x8, a), __builtin_bit_cast(bf16x8, b), c, 0, 0, 0);
}
DI f32x4 mfma16(v4u a, v4u b, f32x4 c) {
  return __builtin_amdgcn_mfma_f32_16x16x32_bf16(__builtin_bit_cast(bf16x8, a), __builtin_bit_cast(bf16x8, b), c, 0, 0, 0);
}
DI v4u tr_frag(LAS unsigned char* p, int second) {
  const v2u l2 = __builtin_bit_cast(v2u, __builtin_amdgcn_ds_read_tr16_b64_v4i16((LAS s16x4*)p));
  const v2u h2 = __builtin_bit_cast(v2u, __builtin_amdgcn_ds_read_tr16_b64_v4i16((LAS s16x4*)(p + second)));
  return v4u{l2[0], l2[1], h2[0], h2[1]};
}
template <int CTRL> DI float dpp_add(float v) {
  return v + asf(__builtin_amdgcn_update_dpp(0, asu(v), CTRL, 0xf, 0xf, true));
}
DI float row16_sum(float v) {
  v = dpp_add<0x128>(v); v = dpp_add<0x124>(v); v = dpp_add<0x122>(v); v = dpp_add<0x121>(v);
  return v;
}
#define PIN2(a, b) asm volatile("" : "+v"(a), "+v"(b))
#define SBAR() __builtin_amdgcn_sched_barrier(0)
#define DRAIN() asm volatile("s_waitcnt vmcnt(0)" ::: "memory")
#define ORDER() __builtin_amdgcn_fence(__ATOMIC_ACQUIRE, "wavefront")
#define STAMPC(kid, i)
#define STAMP(kid, i)
constexpr unsigned ONES = 0x3f803f80u;
constexpr unsigned EMPTY = 0xffffffffu;
#define POLL_SLEEP 2
constexpr unsigned SPIN_CAP = 1u << 16;

DI void row_stats(float s, float ss, float& m, float& r) {
  m = s * (1.0f / D);
  const float var = fmaxf(fmaf(ss, 1.0f / D, -m * m), 0.0f);
  r = __builtin_amdgcn_rsqf(var + EPS);
}
constexpr int NWL = 12;
constexpr int NWR = 28 - NWL;
constexpr int NDROP = 14;
constexpr int XBUF = D * 64;
constexpr int L_XB = 0, L_ST = 2 * XBUF, L_CS = L_ST + 3 * 2048, L_RED = 0, L_WD = 65536, L_BYTES = L_WD + NWL * 8 * 1024;
static_assert(L_CS + 8 * 128 <= L_WD && L_BYTES <= 163840, "LDS map");
constexpr int CV_HSTR = 4160, CV_USTR = 2304 * 2;
constexpr int CV_H = 0, CV_U = CV_H + 2 * 4 * CV_HSTR, CV_RED = L_WD, CV_W = 43008;
static_assert(CV_U + 2 * CV_USTR <= CV_W && CV_W + 18432 <= L_WD && CV_RED + 65536 <= L_BYTES, "conv LDS map");

#define ABL 0
#define PDV 3
template <int V> struct IC { static constexpr int value = V; };
template <int I, int N, class Fn> DI void static_for(Fn&& fn) {
  if constexpr (I < N) { fn(IC<I>{}); static_for<I + 1, N>(fn); }
}
template <int N> DI void wait_vm() { asm volatile("s_waitcnt vmcnt(%0)" ::"n"(N) : "memory"); }
DI float half_sum(float v) {
  const auto r = __builtin_amdgcn_permlane32_swap(asu(v), asu(v), false, false);
  return asf(r[0]) + asf(r[1]);
}
DI unsigned umax3(unsigned a, unsigned b, unsigned c) { return max(max(a, b), c); }
constexpr int OOB = (int)0x80000000;
DI void raw_barrier() { asm volatile("" ::: "memory"); __builtin_amdgcn_s_barrier(); asm volatile("" ::: "memory"); }
template <int AUX = 0> DI v4u ldfrag(rsrc_t r, int l16, int base, int i) { return ld128<AUX>(r, l16, base + i * 1024); }
template <bool DEV, int O0> DI void dma2(rsrc_t r, int l16, int soff, unsigned lds) {
  if constexpr (DEV)
    asm volatile("s_mov_b32 m0, %0\n\ts_nop 0\n\tbuffer_load_dwordx4 %1, %2, %3 offen offset:%4 sc1 lds\n\tbuffer_load_dwordx4 %1, %2, %3 offen offset:%5 sc1 lds"
                 ::"s"(lds), "v"(l16), "s"(r), "s"(soff), "n"(O0), "n"(O0 + 1024) : "m0", "memory");
  else
    asm volatile("s_mov_b32 m0, %0\n\ts_nop 0\n\tbuffer_load_dwordx4 %1, %2, %3 offen offset:%4 lds\n\tbuffer_load_dwordx4 %1, %2, %3 offen offset:%5 lds"
                 ::"s"(lds), "v"(l16), "s"(r), "s"(soff), "n"(O0), "n"(O0 + 1024) : "m0", "memory");
}
DI void dma1(rsrc_t r, int l16, int soff, unsigned lds) {
  asm volatile("s_mov_b32 m0, %0\n\ts_nop 0\n\tbuffer_load_dwordx4 %1, %2, %3 offen sc1 lds" ::"s"(lds), "v"(l16), "s"(r), "s"(soff) : "m0", "memory");
}
DI int opaque(int v) { asm volatile("" : "+v"(v)); return v; }
DI unsigned sopq(unsigned v) { asm volatile("" : "+s"(v)); return v; }
DI unsigned nz(unsigned v) { return min(v, EMPTY - 1u); }

#define NWSV 11
constexpr int NWS = NWSV;
constexpr int BAR = KS1 - 1 - PDV;
constexpr int H_ST = 10;
constexpr int XC = 14;
constexpr int pace(int i) {
  constexpr int sl[11] = {0, 3, 5, 8, 11, 13, 16, 19, 21, 24, 26};
  for (int n = 0; n < 11; ++n) if (sl[n] == i) return n;
  return -1;
}
constexpr int wl_slot(int ly, int t, int i) {
  return (ly != 0 || t == 3) ? -1 : t == 0 ? (i >= 24 ? i - 24 : -1) : i == 16 ? t * 4 : i == 19 ? t * 4 + 1 : i == 23 ? t * 4 + 2 : i == 27 ? t * 4 + 3 : -1;
}

#define SPIN(cond_ok, reload) do { if (__builtin_expect(!(cond_ok), 0)) { unsigned spins_ = 0; \
  _Pragma("unroll 1") do { asm volatile("" ::: "memory"); reload; } while (!(cond_ok) && ++spins_ < SPIN_CAP); } } while (0)
DI unsigned mk_tag1(v2u ep, unsigned tag0) {
  unsigned t = __builtin_amdgcn_readfirstlane(ep[1]) + 0x9e3779b9u;
  if (t - tag0 + 1u <= 2u) t += 0x10001u;
  return t;
}
constexpr int NAUX = 32;
#define OAUX 2
#define PR 0
#define KT 1
#define AUXD 4
#define WD 1
#define XB 7
#define GB 6
#define X3S 2
#define SGA 17
#define DELAY2 0
#define DELAY3 0
#define FWP 0
#define FWC 5
#define PSLEEP
#define NPRE 18
#define NPRE2 24
#define DELAY1 40
#define LATE_N 0

__global__ void __launch_bounds__(512, 2) k_main(const uint16_t* __restrict__ q, const uint16_t* __restrict__ kin,
                                                 const uint16_t* __restrict__ vin, const uint16_t* __restrict__ shk,
                                                 const uint16_t* __restrict__ ssk, const uint16_t* __restrict__ ssd,
                                                 const uint16_t* __restrict__ wg, const uint16_t* __restrict__ wu,
                                                 const uint16_t* __restrict__ wdn, unsigned char* __restrict__ ws,
                                                 uint16_t* __restrict__ out) {
  extern __shared__ __attribute__((aligned(16))) unsigned char sm[];
  LAS unsigned char* L = (LAS unsigned char*)sm;
  const int tid = threadIdx.x, lane = tid & 63;
  int w_ = __builtin_amdgcn_readfirstlane(tid >> 6);
  asm volatile("" : "+s"(w_));
  const int w = w_;
  const rsrc_t rs = mk_rsrc(ws, WS_BYTES);
  const int l16 = lane * 16, tokl = lane & 31, hh = lane >> 5, i16 = lane & 15;
  STAMP(1, 0);
  const v2u ep = ld64<SC1>(rs, 0, OFF_EP);

  const int tr_off = (hh * 8 + (i16 >> 2)) * 144 + (8 * (i16 & 1) + 4 * ((i16 >> 1) & 1)) * 2;

  if (blockIdx.x < NAUX) {
    const int r = tid >> 3, qq = tid & 7;
    v4u a[4][2];
    for (int i = 0; i < AUXD; ++i) __builtin_amdgcn_s_sleep(36);
#pragma unroll
    for (int n = 0; n < 4; ++n) {
      const int b = min((int)blockIdx.x + 32 * n, 97);
      const size_t src = (size_t)((b / 7) * 128 + r) * D + (b % 7) * 64 + qq * 8;
      a[n][0] = *(const v4u*)(wdn + src); a[n][1] = *(const v4u*)(wdn + src + 64 * D);
    }
#pragma unroll
    for (int n = 0; n < 4; ++n) {
      *(v4u*)(sm + n * 18432 + r * 144 + qq * 16) = a[n][0];
      *(v4u*)(sm + n * 18432 + (64 + r) * 144 + qq * 16) = a[n][1];
    }
    __syncthreads();
    const unsigned tg0 = __builtin_amdgcn_readfirstlane(ep[0]) + 1u;
    const v2u tg = {tg0, mk_tag1(ep, tg0)};
#pragma unroll
    for (int n = 0; n < 4; ++n) {
      const int b = blockIdx.x + 32 * n, kb = b / 7, cb = b % 7;
#pragma unroll
      for (int ct = 0; ct < 2; ++ct) {
        const v4u o = tr_frag(L + n * 18432 + w * 16 * 144 + tr_off + (ct * 32 + ((lane >> 4) & 1) * 16) * 2, 4 * 144);
        st128<SC1>(o, rs, b < 98 ? l16 : OOB, OFF_W2 + ((cb * 2 + ct) * KS2 + kb * 8 + w) * 1024);
      }
    }
    DRAIN();
    {
      const int b = blockIdx.x + 32 * (lane >> 4);
      st64<SC1>(tg, rs, b < 98 ? ((b % 7) * KS2 + (b / 7) * 8 + w) * 128 + i16 * 8 : OOB, OFF_FW2);
    }
    return;
  }

  if (PR == 1 && w >= 4) __builtin_amdgcn_s_setprio(1);
  const int bid = blockIdx.x - NAUX;
  const int xcd = bid & 7, li = bid >> 3;
  const int g = (xcd >> 1) * 4 + li / 7, hs = xcd & 1, js = li % 7, j = hs * 7 + js;
  const int fg = j * 8 + w;
  const int sj = w < 7 ? w : 6;
  const int w1o = OFF_W1 + fg * KS1 * 1024;
  const int Ta = g * 4 + hs * 2;
  const unsigned lds0 = (unsigned)(size_t)L;

  v4u W1[KS1], Wd[NWR];
  float csum;
  unsigned csp[14];
  v2u xres[2], yres[2];
  unsigned tag0, tag1;
  const int kse = hs * 56 + w * 7, ksl = (hs ^ 1) * 56 + w * 7;
  auto wd_src = [&](int f) AI { return OFF_W2 + ((js * 2 + ((f / 7) & 1)) * KS2 + (f < 14 ? kse : ksl) + f % 7) * 1024; };
  auto ld_wd = [&](int f) AI { return ld128<SC1>(rs, l16, wd_src(f)); };
  auto ld_wgt = [&](int n) AI {
    if (n < KS1) W1[n] = ldfrag<SC1>(rs, l16, w1o, n);
    else if (n < KS1 + NWS) Wd[n - KS1] = ld_wd(n - KS1);
  };
  auto tag_ok = [&](v4u v) AI { return __all(((v[1] ^ sopq(tag0)) | (v[3] ^ sopq(tag1))) == 0u); };
  auto flag_ok = [&](v2u v) AI { return __all(((v[0] ^ sopq(tag0)) | (v[1] ^ sopq(tag1))) == 0u); };
  auto sp_publish = [&](int which, int u) AI {
    const int ln = opaque(lane);
    const v2u xn = xres[u];
    const float s = row16_sum(dot2(xn[0], ONES, dot2(xn[1], ONES, 0.f))), ss = row16_sum(dot2(xn[0], xn[0], dot2(xn[1], xn[1], 0.f)));
    st128<SC1>(v4u{asu(s), sopq(tag0), asu(ss), sopq(tag1)}, rs, (ln & 15) == 0 ? ln : OOB, OFF_SP + which * SP_BYTES + ((Ta + u) * 7 + js) * 512 + w * 64);
  };
  auto sp_load = [&](int which, int T) AI {
    const int ln = opaque(lane);
    return ld128<SC1>(rs, ((ln & 15) < 7 ? (ln & 15) * 512 : 0) + (ln & 48), OFF_SP + which * SP_BYTES + T * 7 * 512 + w * 64);
  };
  auto sp_stats = [&](int which, int u, v4u sp, float& m, float& r) AI {
    SPIN(tag_ok(sp), sp = sp_load(which, Ta + u));
    row_stats(row16_sum(i16 < 7 ? asf(sp[0]) : 0.f), row16_sum(i16 < 7 ? asf(sp[2]) : 0.f), m, r);
  };
  auto gate_publish = [&](int ly, int u, float r, float c) AI {
    v2u xn;
#pragma unroll
    for (int e = 0; e < 2; ++e) {
      const float xl = bflo(xres[u][e]), xh = bfhi(xres[u][e]);
      xn[e] = nz(pk2(fmaf(fmaf(xl, r, c), bflo(yres[u][e]), xl), fmaf(fmaf(xh, r, c), bfhi(yres[u][e]), xh)));
    }
    xres[u] = xn;
    const int ln = opaque(lane);
    st64<SC1>(xn, rs, (ln * 8) ^ (((2 * w + (ln >> 5)) & 7) << 4), OFF_X + ly * ACT_BYTES + ((Ta + u) * 7 + js) * 4096 + w * 512);
    sp_publish(2 * ly + 1, u);
  };
  auto x3_publish = [&](int u, v4u sp) AI {
    float m, r;
    sp_stats(2, u, sp, m, r);
    gate_publish(1, u, r, -m * r);
  };

  {
    const int c0 = bid * 2;
    const int r = tid >> 3, qq = tid & 7;
    const int tok = Ta * 32 + r, ca = js * 64 + qq * 8;
    const rsrc_t rk = mk_rsrc(kin, ACT_BYTES), rh = mk_rsrc(ssk, ACT_BYTES);
    v4u kk[4], sw[4];
    const v4u swl = *(const v4u*)(shk + js * 256 + tokl * 8);
#pragma unroll
    for (int i = 0; i < 4; ++i) kk[i] = ld128(rk, ((tok - i) * D + ca) * 2, 0);
    const v4u vv = *(const v4u*)(vin + (size_t)tok * D + ca);
    const int ch = tid >> 8, i8 = tid & 255;
    const v4u d = ld128(rh, tid * 16, c0 * S * 2);
    const v2u e = ld64(rh, tid * 16 + 16 + c0 * S * 2, 0);
    STAMP(1, 28);
    SBAR();
    *(v4u*)(sm + CV_RED + 16384 + w * 1024 + l16) = swl;
#pragma unroll
    for (int i = 0; i < 4; ++i) sw[i] = *(const v4u*)(sm + CV_RED + 16384 + w * 1024 + qq * 64 + i * 16);
    tag0 = __builtin_amdgcn_readfirstlane(ep[0]) + 1u;
    tag1 = mk_tag1(ep, tag0);
    {
      uint16_t* tl = (uint16_t*)(sm + CV_RED) + qq * 8 * 64 + ((63 - r) ^ (qq << 3));
#pragma unroll
      for (int e2 = 0; e2 < 4; ++e2) {
        const unsigned wa01 = sw[e2][0], wa23 = sw[e2][1], wb01 = sw[e2][2], wb23 = sw[e2][3];
        unsigned p = pk2(bflo(kk[3][e2]) * bfhi(wa23), bfhi(kk[3][e2]) * bfhi(wb23));
        unsigned acc = p;
        p = pk2(bflo(kk[2][e2]) * bflo(wa23), bfhi(kk[2][e2]) * bflo(wb23));
        acc = pk2(bflo(acc) + bflo(p), bfhi(acc) + bfhi(p));
        p = pk2(bflo(kk[1][e2]) * bfhi(wa01), bfhi(kk[1][e2]) * bfhi(wb01));
        acc = pk2(bflo(acc) + bflo(p), bfhi(acc) + bfhi(p));
        p = pk2(bflo(kk[0][e2]) * bflo(wa01), bfhi(kk[0][e2]) * bflo(wb01));
        acc = pk2(bflo(acc) + bflo(p), bfhi(acc) + bfhi(p));
        const unsigned kv = pk2(bflo(acc) * bflo(vv[e2]), bfhi(acc) * bfhi(vv[e2]));
        tl[(2 * e2) * 64] = (uint16_t)kv;
        tl[(2 * e2 + 1) * 64] = (uint16_t)(kv >> 16);
      }
    }
    SBAR();
    const int wt = min(bid, 195), kb = wt / 28, fb = wt % 28, ks = kb * 4 + (w & 3), rl = lane >> 3;
    v4u wa[4];
    {
      const rsrc_t rg = mk_rsrc(wg, D * F * 2), ru = mk_rsrc(wu, D * F * 2);
      const int vo = (w < 4 || WD) ? OOB : ((ks * 16 + rl) * F + fb * 64 + qq * 8) * 2;
      wa[0] = ld128(rg, vo, 0); wa[1] = ld128(rg, vo, 16 * F); wa[2] = ld128(ru, vo, 0); wa[3] = ld128(ru, vo, 16 * F);
    }
    SBAR();
    STAMP(1, 29);
    __syncthreads();
    STAMP(1, 1);
#pragma unroll
    for (int i = 0; i < 2; ++i) {
      const int cl = i * 32 + (tid >> 4);
      const v2u o = *(const v2u*)(sm + CV_RED + cl * 128 + ((i16 ^ ((cl >> 3) << 1)) * 8));
      st128<SC1>(v4u{o[0], sopq(tag0), o[1], sopq(tag1)}, rs, (tid >> 4) * 8192 + i16 * 16, OFF_KVG + (js * 64 + i * 32) * 8192 + (S - 64 - Ta * 32) * 4);
    }
    SBAR();
    {
      const unsigned a0 = __builtin_amdgcn_alignbit(d[1], d[0], 16), a1 = __builtin_amdgcn_alignbit(d[2], d[1], 16),
                     a2 = __builtin_amdgcn_alignbit(d[3], d[2], 16), a3 = __builtin_amdgcn_alignbit(e[0], d[3], 16),
                     a4 = __builtin_amdgcn_alignbit(e[1], e[0], 16);
      unsigned char* hb = sm + CV_H + ch * 4 * CV_HSTR + 32 + i8 * 16;
      *(v4u*)(hb) = d;
      *(v4u*)(hb + CV_HSTR) = v4u{a0, a1, a2, a3};
      *(v4u*)(hb + 2 * CV_HSTR) = v4u{d[1], d[2], d[3], e[0]};
      *(v4u*)(hb + 3 * CV_HSTR) = v4u{a1, a2, a3, a4};
      const unsigned d0 = __builtin_amdgcn_readfirstlane(d[0]), d1 = __builtin_amdgcn_readfirstlane(d[1]);
      if (i8 < 40) {
        const int q8 = i8 - 32, sg = q8 >> 1;
        v4u pv = {0u, 0u, 0u, 0u};
        if (q8 == 3) pv[3] = d0 << 16;
        if (q8 == 5) pv[3] = d0;
        if (q8 == 7) { pv[2] = d0 << 16; pv[3] = __builtin_amdgcn_alignbit(d1, d0, 16); }
        *(v4u*)(sm + (q8 < 0 ? CV_U + ch * CV_USTR + 4096 + i8 * 16 : CV_H + (ch * 4 + sg) * CV_HSTR + (q8 & 1) * 16)) = pv;
      }
    }
    STAMP(1, 2);
    float dcoef;
    auto own_load = [&]() AI {
#pragma unroll
      for (int u = 0; u < 2; ++u) xres[u] = *(const v2u*)(q + (size_t)((Ta + u) * 32 + 4 * w + (lane >> 4)) * D + js * 64 + i16 * 4);
      dcoef = bflo(ssd[c0 + ((lane >> 4) & 1)]);
    };
    if (w < 4) {
      v4u ug[4];
      auto u_load = [&]() AI {
#pragma unroll
        for (int i = 0; i < 4; ++i) ug[i] = ld128<SC1>(rs, tid * 16, OFF_KVG + c0 * 8192 + i * 4096);
      };
      __builtin_amdgcn_s_sleep(DELAY1);
      u_load();
      SPIN(tag_ok(ug[0]) && tag_ok(ug[1]) && tag_ok(ug[2]) && tag_ok(ug[3]), u_load());
#pragma unroll
      for (int i = 0; i < 4; ++i) *(v2u*)(sm + CV_U + (i >> 1) * CV_USTR + (i & 1) * 2048 + tid * 8) = v2u{ug[i][0], ug[i][2]};
      own_load();
    } else {
      if (WD) {
        __builtin_amdgcn_s_sleep(WD);
        const rsrc_t rg = mk_rsrc(wg, D * F * 2), ru = mk_rsrc(wu, D * F * 2);
        const int vo = ((ks * 16 + rl) * F + fb * 64 + qq * 8) * 2;
        wa[0] = ld128(rg, vo, 0); wa[1] = ld128(rg, vo, 16 * F); wa[2] = ld128(ru, vo, 0); wa[3] = ld128(ru, vo, 16 * F);
      }
      own_load();
      unsigned char* wb = sm + CV_W + (w - 4) * (32 * 144) + rl * 144 + qq * 16;
      *(v4u*)wb = wa[0]; *(v4u*)(wb + 8 * 144) = wa[1]; *(v4u*)(wb + 16 * 144) = wa[2]; *(v4u*)(wb + 24 * 144) = wa[3];
#pragma unroll
      for (int fgl = 0; fgl < 4; ++fgl) {
        const v4u o = tr_frag(L + CV_W + (w - 4) * (32 * 144) + ((lane >> 4) & 1) * (16 * 144) + tr_off + fgl * 32, 4 * 144);
        st128<SC1>(o, rs, bid < 196 ? l16 : OOB, OFF_W1 + ((fb * 4 + fgl) * KS1 + ks) * 1024);
        const float cs = dot2(o[3], ONES, dot2(o[2], ONES, dot2(o[1], ONES, dot2(o[0], ONES, 0.f))));
        st32<SC1>(asu(half_sum(cs)), rs, (bid < 196 && lane < 32) ? lane * 4 : OOB, OFF_CSP + (ks * 112 + fb * 4 + fgl) * 128);
      }
      DRAIN();
      st64<SC1>(v2u{sopq(tag0), sopq(tag1)}, rs, (bid < 196 && lane < 16) ? lane * 8 : OOB, OFF_FW1 + (fb * 32 + ks) * 128);
    }
    STAMP(1, 22);
    auto fw_poll = [&]() AI { return ld64<SC1>(rs, min(opaque(lane), 27) * 128, OFF_FW1 + (fg >> 2) * 4096); };
    __syncthreads();
    STAMP(1, 3);
    float r0[2], c0n[2];
    v2u fw;
    {
      const int cw = w >> 2, sp = w & 3;
      const int gq = lane >> 4;
      const int sg = (1 + i16) & 3;
      const unsigned char* fbp = sm + CV_H + (cw * 4 + sg) * CV_HSTR + (1 + i16 + 8 * gq - sg) * 2 + sp * 128;
      const unsigned char* gb = sm + CV_U + cw * CV_USTR + (2032 - 16 * i16 + 8 * gq) * 2 + sp * 128 - 3584;
      auto rdF = [&](int n) AI -> v4u {
        const v2u lo = *(const v2u*)(fbp + (n >> 3) * 64 + (n & 7) * 512), hi = *(const v2u*)(fbp + (n >> 3) * 64 + (n & 7) * 512 + 8);
        return v4u{lo[0], lo[1], hi[0], hi[1]};
      };
      f32x4 acc[8];
#pragma unroll
      for (int i = 0; i < 8; ++i) acc[i] = f32x4{0.f, 0.f, 0.f, 0.f};
      v4u Gf[8], Ff[3];
      Ff[0] = rdF(0);
#pragma unroll
      for (int dl = 7; dl >= 0; --dl) Gf[dl] = *(const v4u*)(gb + (7 - dl) * 512);
      Ff[1] = rdF(1);
      SBAR();
      static_for<0, 16>([&](auto N) AI {
        constexpr int n = N.value, jj = n & 7;
        if constexpr (n + 2 < 16) Ff[(n + 2) % 3] = rdF(n + 2);
#pragma unroll
        for (int dl = 7 - jj; dl >= 0; --dl) { acc[jj + dl] = mfma16(Ff[n % 3], Gf[dl], acc[jj + dl]); asm volatile("" : "+v"(acc[jj + dl])); }
        if constexpr (n < 8) Gf[7 - jj] = *(const v4u*)(gb + 64 + jj * 512);
        if constexpr (n == FWP) fw = fw_poll();
        if constexpr (n == FWC) { SPIN(flag_ok(fw), fw = fw_poll()); ORDER(); }
        if constexpr (n >= FWC) ld_wgt(n - FWC);
        const v4u ev = {EMPTY, EMPTY, EMPTY, EMPTY};
        if constexpr (n == 1) st128<SC1>(ev, rs, (tid >> 8) * XBUF + (tid & 255) * 16, OFF_X + (Ta * 7 + js) * 4096);
        if constexpr (n == 2)
          st128<SC1>(ev, rs, w < 4 ? OFF_SP + w * SP_BYTES + ((Ta + hh) * 7 + js) * 512 + tokl * 16
                           : w < 6 ? OFF_FH + (((w - 4) * 64 + g * 4 + (lane >> 4)) * KS2 + j * 8) * FSTR + i16 * 16 : OOB, 0);
        SBAR();
      });
      STAMP(1, 4);
      const int slot = (4 * i16 + gq) ^ ((i16 >> 1) & 3);
#pragma unroll
      for (int i = 0; i < 8; ++i) *(f32x4*)(sm + CV_RED + ((w * 8 + i) * 64 + slot) * 16) = acc[i];
    }
    const int fch = (lane >> 4) & 1;
    v2u ur[2];
#pragma unroll
    for (int i = 0; i < 2; ++i) ur[i] = *(const v2u*)(sm + CV_U + fch * CV_USTR + (2044 - (256 * w + (2 * i + hh) * 64 + i16 * 4)) * 2);
    wait_vm<16 - FWC>();
    __syncthreads();
    STAMP(1, 5);
#pragma unroll
    for (int i = 0; i < 2; ++i) {
      const int qi = (2 * i + hh) * 16 + i16, sl = qi ^ ((qi >> 3) & 3);
      f32x4 y = *(const f32x4*)(sm + CV_RED + ((fch * 32 + w) * 64 + sl) * 16);
#pragma unroll
      for (int sp = 1; sp < 4; ++sp) y += *(const f32x4*)(sm + CV_RED + (((fch * 4 + sp) * 8 + w) * 64 + sl) * 16);
      unsigned yy[2];
#pragma unroll
      for (int e2 = 0; e2 < 2; ++e2) {
        const unsigned cb = pk2(y[2 * e2], y[2 * e2 + 1]);
        const unsigned ud = pk2(bfhi(ur[i][1 - e2]) * dcoef, bflo(ur[i][1 - e2]) * dcoef);
        yy[e2] = pk2(bflo(cb) + bflo(ud), bfhi(cb) + bfhi(ud));
      }
      st128<SC1>(v4u{yy[0], sopq(tag1), yy[1], sopq(tag0)}, rs, fch * 8192 + hh * 256 + i16 * 16, OFF_KVG + c0 * 8192 + (4 * w + 2 * i) * 256);
    }
    SBAR();
    STAMP(1, 24);
    sp_publish(0, 0);
    sp_publish(0, 1);
    raw_barrier();
#pragma unroll
    for (int i = 16 - FWC; i < NPRE; ++i) ld_wgt(i);
    SBAR();
    STAMP(1, 23);
    {
      v4u yg[2];
      auto y_load = [&]() AI {
        yg[0] = ld128<SC1>(rs, (tid >> 4) * 8192 + i16 * 16, OFF_KVG + js * 64 * 8192 + (Ta >> 1) * 256);
        yg[1] = ld128<SC1>(rs, (tid >> 4) * 8192 + i16 * 16, OFF_KVG + (js * 64 + 32) * 8192 + (Ta >> 1) * 256);
      };
      __builtin_amdgcn_s_sleep(DELAY2);
      y_load();
      v4u sp0 = sp_load(0, Ta), sp1 = sp_load(0, Ta + 1), spo = sp_load(0, Ta ^ 2);
      SPIN(__all(((yg[0][1] ^ tag1) | (yg[0][3] ^ tag0) | (yg[1][1] ^ tag1) | (yg[1][3] ^ tag0)) == 0u), y_load());
      STAMP(1, 27);
#pragma unroll
      for (int i = 0; i < 2; ++i) {
        const int cl = i * 32 + (tid >> 4);
        *(v2u*)(sm + L_XB + XBUF + cl * 128 + ((i16 ^ (cl >> 2)) * 8)) = v2u{yg[i][0], yg[i][2]};
      }
      { float m; sp_stats(0, 0, sp0, m, r0[0]); c0n[0] = -m * r0[0]; sp_stats(0, 1, sp1, m, r0[1]); c0n[1] = -m * r0[1]; }
      SPIN(tag_ok(spo), spo = sp_load(0, Ta ^ 2));
      __syncthreads();
      STAMP(1, 25);
#pragma unroll
      for (int u = 0; u < 2; ++u) {
        const uint16_t* gp = (const uint16_t*)(sm + L_XB + XBUF + i16 * 512 + (((u * 8 + w) ^ i16) * 8) + (lane >> 4) * 2);
        v2u yv = {(unsigned)gp[0] | ((unsigned)gp[64] << 16), (unsigned)gp[128] | ((unsigned)gp[192] << 16)};
        asm volatile("" : "+v"(yv));
        yres[u] = yv;
        gate_publish(0, u, r0[u], c0n[u]);
        asm volatile("" : "+v"(yres[u]));
      }
    }
    SBAR();
    STAMP(1, 26);
    raw_barrier();
#pragma unroll
    for (int i = 0; i < 14; ++i) csp[i] = ld32<SC1>(rs, (hh * 3584 + tokl) * 4, OFF_CSP + (i * 2 * 3584 + fg * 32) * 4);
#pragma unroll
    for (int i = NPRE; i < NPRE2; ++i) ld_wgt(i);
    SBAR();
  }

  v4u xs[4];
  v2u sst;
  auto tile_of = [&](int t) AI { return g * 4 + (((t & 1) ^ hs) << 1) + (t >> 1); };
  auto x_src = [&](int ly, int t) AI { return OFF_X + ly * ACT_BYTES + tile_of(t) * XBUF + sj * 4096; };
  auto s_load = [&](int ly, int t) AI { sst = ld64<SC1>(rs, (l16 & 511) + (l16 >> 9) * 8, OFF_SP + (2 * ly + 1) * SP_BYTES + (tile_of(t) * 7 + sj) * 512); };
  auto x0_load = [&]() AI {
#pragma unroll
    for (int i = 0; i < 4; ++i) xs[i] = ldfrag<SC1>(rs, l16, x_src(1, 0), i);
    s_load(1, 0);
  };

  auto layer = [&](auto LY) AI {
    constexpr int ly = decltype(LY)::value;
    const int lane = opaque(tid & 63), tokl = lane & 31, hh = lane >> 5;
    auto x_ok = [&]() AI {
      unsigned mx = 0u;
#pragma unroll
      for (int i = 0; i < 4; ++i) mx = umax3(umax3(mx, xs[i][0], xs[i][1]), xs[i][2], xs[i][3]);
      return __all((mx != EMPTY) & (sst[1] == (hh ? tag1 : tag0)));
    };
    auto x_dma = [&](int t, int half) AI {
      const unsigned dst = lds0 + L_XB + (t & 1) * XBUF + sj * 4096;
      if (half) { dma2<true, 2048>(rs, l16, x_src(ly, t), dst); s_load(ly, t); }
      else dma2<true, 0>(rs, l16, x_src(ly, t), dst);
    };
    auto x_fetch = [&](int t) AI {
      unsigned spins = 0;
      bool ok;
#pragma unroll 1
      do {
        x_dma(t, 0); x_dma(t, 1); DRAIN();
        unsigned mx = 0u;
#pragma unroll
        for (int i = 0; i < 4; i += 2) {
          const v4u a = *(const v4u*)(sm + L_XB + (t & 1) * XBUF + sj * 4096 + i * 1024 + l16), b = *(const v4u*)(sm + L_XB + (t & 1) * XBUF + sj * 4096 + i * 1024 + 1024 + l16);
          mx = umax3(umax3(a[0], a[1], a[2]), umax3(a[3], b[0], b[1]), umax3(b[2], b[3], mx));
          asm volatile("" : "+v"(mx) :: "memory");
        }
        ok = __all((mx != EMPTY) & (sst[1] == (hh ? tag1 : tag0)));
      } while (!ok && ++spins < SPIN_CAP);
    };
    auto st_write = [&](int t) AI {
      unsigned char* sp = sm + L_ST + (t % 3) * 2048 + (sj >> 2) * 512 + tokl * 16 + (sj & 3) * 4;
      *(unsigned*)(sp + hh * 1024) = sst[0];
    };
    const int xrd = tokl * 128 + ((hh ^ ((tokl >> 1) & 7)) << 4);
    auto xfrag = [&](int t, int ks) AI -> v4u { return *(const v4u*)(sm + L_XB + (t & 1) * XBUF + (xrd ^ ((ks & 3) << 5)) + (ks >> 2) * 4096); };
    const int hbase = OFF_H + ly * H_BYTES;
    v4u hf[14];
    auto h_load = [&](int u, int i) AI { hf[i] = ldfrag<SC1>(rs, l16, hbase + ((Ta + u) * KS2 + (i < 7 ? kse : ksl)) * 1024, i % 7); };
    v2u tagv = {tag0, tag1};
    asm volatile("" : "+v"(tagv));
    auto h_flag = [&](int t) AI { st64<SC1>(tagv, rs, opaque(l16) == 0 ? 0 : OOB, OFF_FH + ((ly * 64 + tile_of(t)) * KS2 + fg) * FSTR); };
    auto hf_poll = [&](int u, int half) AI { return ld64<SC1>(rs, min(opaque(l16) * (FSTR / 16), 6 * FSTR), OFF_FH + ((ly * 64 + Ta + u) * KS2 + (half ? ksl : kse)) * FSTR); };
    auto hf_check = [&](int u, int half, v2u v) AI { SPIN(flag_ok(v), v = hf_poll(u, half)); ORDER(); };
    v2u hpl;

    v4u sgr;
    auto cs_write = [&]() AI {
      const int ln = opaque(lane), csrow = (ln & 16) + 8 * ((ln >> 2) & 1) + (ln & 3) + 4 * ((ln >> 3) & 1);
      *(float*)(sm + L_CS + w * 128 + csrow * 4) = csum;
    };
    if (ly == 0) {
      __builtin_amdgcn_s_sleep(DELAY3);
      x_fetch(0);
      STAMP(1, 17);
      st_write(0);
      STAMP(1, 6);
      __syncthreads();
      x_dma(1, 0); x_dma(1, 1);
    } else {
      SPIN(x_ok(), x0_load());
      STAMP(1, 30);
      __syncthreads();
      cs_write();
#pragma unroll
      for (int i = 0; i < 4; ++i) *(v4u*)(sm + L_XB + sj * 4096 + i * 1024 + l16) = xs[i];
      st_write(0);
      sgr = sp_load(2, Ta + 1);
      x_dma(1, 0); x_dma(1, 1);
      asm volatile("s_waitcnt lgkmcnt(0)" ::: "memory");
      raw_barrier();
    }
    STAMP(1, 7 + ly * 24);
    if (ly == 1) STAMPC(1, 62);

#define FST(q)
    f32x16 acc[2];
    constexpr int PD = PDV;
    v4u xf[PD], ho;
    f32x4 p0, p1, p2, p3;
    float mean = 0.f, r2[2];
#pragma unroll
    for (int i = 0; i < PD; ++i) xf[i] = xfrag(0, i + (ly == 1 ? NDROP : 0));
    auto ep_cs = [&](int hq) AI {
      const unsigned char* cp = sm + L_CS + w * 128 + hh * 32;
      if (hq) { p2 = *(const f32x4*)(cp + 16); p3 = *(const f32x4*)(cp + 80); } else { p0 = *(const f32x4*)cp; p1 = *(const f32x4*)(cp + 64); }
    };
    float hv[2], sq = 0.f;
    auto ep_one = [&](const f32x16& a, int i) AI {
      hv[i & 1] = fmaxf(fmaf(mean, i < 4 ? p0[i & 3] : p2[i & 3], a[i]), 0.f) * fmaf(mean, i < 4 ? p1[i & 3] : p3[i & 3], a[8 + i]);
      asm volatile("" : "+v"(hv[i & 1]));
    };
    auto h_store = [&](int t) AI { st128<SC1>(ho, rs, l16, hbase + (tile_of(t) * KS2 + fg) * 1024); };

    static_for<0, 4>([&](auto TT) AI {
      constexpr int t = TT.value;
      constexpr int rot = (ly == 1 && t == 0) ? NDROP : 0;
      v4u cb0, cb1;
      unsigned cmx = 0u;
      float ga = 0.f, gb = 0.f;
      static_for<0, KS1>([&](auto II) AI {
        constexpr int i = II.value, n = t * KS1 + i;
        if constexpr (i == 0) {
          f32x16 z;
#pragma unroll
          for (int e = 0; e < 16; ++e) z[e] = 0.f;
          acc[t & 1] = mfma32(W1[rot], xf[n % PD], z);
        } else acc[t & 1] = mfma32(W1[(i + rot) % KS1], xf[n % PD], acc[t & 1]);
        asm volatile("" : "+v"(acc[t & 1]));
        SBAR();
        if constexpr (i + PD < KS1) xf[n % PD] = xfrag(t, (i + PD + rot) % KS1);
        else if constexpr (t < 3) xf[n % PD] = xfrag(t + 1, i + PD - KS1);
        if constexpr (t == 2 && i == 0) { wait_vm<5 + (ly == 0 ? 6 : 0)>(); h_flag(0); }
        if constexpr (t == 2 && i == 24) { wait_vm<(ly == 0 ? 6 : 1)>(); h_flag(1); }
        if constexpr (t == 3 && i == 23) { wait_vm<6>(); h_flag(2); }
        if constexpr (PR == 2 && t > 0 && i == 0) __builtin_amdgcn_s_setprio(1);
        if constexpr (PR == 2 && t > 0 && i == H_ST + 1) __builtin_amdgcn_s_setprio(0);
        if constexpr (PR == 3 && (i % 4) == 0) { if (((i >> 2) & 1) == (w >> 2)) __builtin_amdgcn_s_setprio(1); else __builtin_amdgcn_s_setprio(0); }
        if constexpr (t > 0) {
          if constexpr (i >= 3 && i <= 9 && (i & 1)) { ho[(i - 3) / 2] = pk2(hv[0], hv[1]); asm volatile("" : "+v"(ho[(i - 3) / 2])); }
          if constexpr (i >= 1 && i <= 8) ep_one(acc[(t - 1) & 1], i - 1);
          if constexpr (i == H_ST) h_store(t - 1);
        }
        if constexpr (t < 3) {
          const unsigned char* xb = sm + L_XB + ((t + 1) & 1) * XBUF + sj * 4096 + l16;
          if constexpr (i == XC) {
            wait_vm<(t > 0) + (t == 2) + (t == 0 ? (ly == 1 ? 8 : 2 + (KS1 - NPRE2 < XC ? KS1 - NPRE2 : XC)) : 0) + (ly == 0 && t > 0 ? 4 : 0)>();
            asm volatile("" : "+v"(sst));
            cb0 = *(const v4u*)xb; cb1 = *(const v4u*)(xb + 1024);
          }
          if constexpr (i == XC + 3) {
            cmx = umax3(umax3(cb0[0], cb0[1], cb0[2]), umax3(cb0[3], cb1[0], cb1[1]), max(cb1[2], cb1[3]));
            asm volatile("" : "+v"(cmx));
          }
          if constexpr (i == XC + 4) {
            cb0 = *(const v4u*)(xb + 2048); cb1 = *(const v4u*)(xb + 3072);
            cmx = sst[1] != (hh ? tagv[1] : tagv[0]) ? EMPTY : cmx;
            asm volatile("" : "+v"(cmx));
          }
          if constexpr (i == XC + 7) {
            cmx = umax3(umax3(cb0[0], cb0[1], cb0[2]), umax3(cb0[3], cb1[0], cb1[1]), umax3(cb1[2], cb1[3], cmx));
            asm volatile("" : "+v"(cmx));
          }
          if constexpr (i == XC + 8) {
            if (__builtin_expect(!__all(cmx != EMPTY), 0)) x_fetch(t + 1);
            st_write(t + 1);
          }
        }
        if constexpr (ly == 0 && t == 0 && i == 4)
          st128<SC1>(v4u{EMPTY, EMPTY, EMPTY, EMPTY}, rs, l16, OFF_X + ACT_BYTES + ((Ta + (w >> 2)) * 7 + js) * 4096 + (w & 3) * 1024);
        if constexpr (ly == 0 && t == 0 && NPRE2 + i < KS1) ld_wgt(NPRE2 + i);
        if constexpr (ly == 0 && t == 0) {
          auto fw2_poll = [&]() AI { const int ln = opaque(lane); return ld64<SC1>(rs, (((ln & 8) ? ksl : kse) + min(ln & 7, 6)) * 128, OFF_FW2 + js * KS2 * 128); };
          if constexpr (i == 6) hpl = fw2_poll();
          if constexpr (i == BAR) { asm volatile("" : "+v"(W1[KS1 - 1])); SPIN(flag_ok(hpl), hpl = fw2_poll()); ORDER(); }
        }
        if constexpr (ly == 0 && t == 0 && i == 1) {
          float cs = 0.f;
#pragma unroll
          for (int q7 = 0; q7 < 14; ++q7) cs += asf(csp[q7]);
          csum = half_sum(cs);
          cs_write();
        }
        if constexpr (ly == 0 && (t == 1 || t == 2) && (i == 2 || i == 6 || i == 12)) ld_wgt(KS1 + (t - 1) * 4 + i / 5);
        if constexpr (ly == 0 && (t == 1 || t == 2) && i == 21) ld_wgt(KS1 + (t - 1) * 4 + 3);
        if constexpr (ly == 0 && t == 2 && i >= 24 && i < 27) ld_wgt(KS1 + 8 + i - 24);
        if constexpr (ly == 0 && t == 0 && (i == 17 || i == 20)) {}
        if constexpr (wl_slot(ly, t, i) >= 0) {
          constexpr int qn = wl_slot(ly, t, i), f = NWS + qn;
          dma1(rs, l16, wd_src(f), lds0 + L_WD + (w * NWL + qn) * 1024);
        }
        if constexpr (ly == 1 && t == 0) {
          constexpr int st = i - X3S;
          if constexpr (st == 0) SPIN(tag_ok(sgr), sgr = sp_load(2, Ta + 1));
          if constexpr (st == 1) { ga = i16 < 7 ? asf(sgr[0]) : 0.f; gb = i16 < 7 ? asf(sgr[2]) : 0.f; }
          if constexpr (st == 2 || st == 9) { ga = dpp_add<0x128>(ga); gb = dpp_add<0x128>(gb); PIN2(ga, gb); ga = dpp_add<0x124>(ga); gb = dpp_add<0x124>(gb); }
          if constexpr (st == 3 || st == 10) { ga = dpp_add<0x122>(ga); gb = dpp_add<0x122>(gb); PIN2(ga, gb); ga = dpp_add<0x121>(ga); gb = dpp_add<0x121>(gb); }
          if constexpr (st == 4) { float m, r; row_stats(ga, gb, m, r); ga = r; gb = -m * r; }
          if constexpr (st == 5 || st == 6) {
            constexpr int e = st - 5;
            const float xl = bflo(xres[1][e]), xh = bfhi(xres[1][e]);
            float ol = fmaf(fmaf(xl, ga, gb), bflo(yres[1][e]), xl), oh = fmaf(fmaf(xh, ga, gb), bfhi(yres[1][e]), xh);
            PIN2(ol, oh);
            unsigned o = nz(pk2(ol, oh));
            asm volatile("" : "+v"(o));
            xres[1][e] = o;
          }
          if constexpr (st == 7) st64<SC1>(xres[1], rs, (lane * 8) ^ (((2 * w + hh) & 7) << 4), OFF_X + ACT_BYTES + ((Ta + 1) * 7 + js) * 4096 + w * 512);
          if constexpr (st == 8) { ga = dot2(xres[1][0], ONES, dot2(xres[1][1], ONES, 0.f)); gb = dot2(xres[1][0], xres[1][0], dot2(xres[1][1], xres[1][1], 0.f)); }
          if constexpr (st == 11) st128<SC1>(v4u{asu(ga), tagv[0], asu(gb), tagv[1]}, rs, (lane & 15) == 0 ? lane : OOB, OFF_SP + 3 * SP_BYTES + ((Ta + 1) * 7 + js) * 512 + w * 64);
          if constexpr (st >= 1 && st <= 10) PIN2(ga, gb);
          if constexpr (i == BAR) asm volatile("" : "+v"(W1[NDROP - 1]));
        }
        if constexpr (ly == 1 && t == 0 && i >= 3 && i < 15 && (i & 1)) W1[8 + (i - 3) / 2] = ldfrag<SC1>(rs, l16, w1o, 8 + (i - 3) / 2);
        if constexpr (t < 2 && (i == BAR + 1 || i == BAR + 3)) x_dma(t + 2, (i - BAR - 1) / 2);
        if constexpr (t == 2 && i == 15) hpl = hf_poll(0, 0);
        if constexpr (t == 3) {
          if constexpr (i == 0) hf_check(0, 0, hpl);
          if constexpr (i == 7) hpl = hf_poll(0, 1);
          if constexpr (i == 16) hf_check(0, 1, hpl);
          if constexpr (i < 14 && !(i & 1)) h_load(0, i / 2);
          if constexpr (i >= 16 && !(i & 1)) h_load(0, 7 + (i - 16) / 2);
        }
        if constexpr (i == 11) {
          const unsigned char* sp = sm + L_ST + (t % 3) * 2048 + tokl * 16;
          p0 = *(const f32x4*)sp; p1 = *(const f32x4*)(sp + 512);
        }
        if constexpr (i == 13) { p0[0] += p0[1]; p0[2] += p0[3]; p1[0] += p1[1]; asm volatile("" : "+v"(p0[0]), "+v"(p0[2]), "+v"(p1[0])); }
        if constexpr (i == 15) { mean = ((p0[0] + p0[2]) + (p1[0] + p1[2])) * (-1.0f / D); asm volatile("" : "+v"(mean)); }
        if constexpr (!(t & 1)) {
          if constexpr (i == 16) { const unsigned char* sp = sm + L_ST + (t % 3) * 2048 + tokl * 16; p2 = *(const f32x4*)(sp + 1024); p3 = *(const f32x4*)(sp + 1536); }
          if constexpr (i == 19) { p2[0] += p2[1]; p2[2] += p2[3]; p3[0] += p3[1]; asm volatile("" : "+v"(p2[0]), "+v"(p2[2]), "+v"(p3[0])); }
          if constexpr (i == 20) { sq = (p2[0] + p2[2]) + (p3[0] + p3[2]); asm volatile("" : "+v"(sq)); }
          if constexpr (i == 26) { sq = __builtin_amdgcn_rcpf(fmaxf(fmaf(sq, 1.0f / D, -mean * mean), 0.0f) + EPS); asm volatile("" : "+v"(sq)); }
          if constexpr (i == 27) r2[t >> 1] = asf(__builtin_amdgcn_ds_bpermute(w * 16 + (lane >> 4) * 4, asu(sq)));
        }
        if constexpr (i == 25) ep_cs(0);
        if constexpr (t > 0 && i == 2) ep_cs(1);
        if constexpr (t == 3 && i == 27) ep_cs(1);
        if constexpr ((XB & 1) && t == 3 && i == BAR) raw_barrier();
        if constexpr ((XB & 2) && t == 3 && i == 11) raw_barrier();
        if constexpr ((XB & 4) && t < 3 && i == 11) raw_barrier();
        if constexpr (i == BAR && t < 3) {
          asm volatile("s_waitcnt lgkmcnt(1)" ::: "memory");
          raw_barrier();
        }
        SBAR();
      });
      STAMP(1, 8 + t + ly * 24);
      if (ly == 1 && t == 2) STAMPC(1, 63);
    });
    v4u sga;
    auto unit = [&](auto UU) AI {
      constexpr int u = UU.value;
      v4u wl[3];
      int wad = 0, rad = 0;
      auto red_write = [&](int jq) AI {
        const f32x16& a = acc[jq >> 2];
        const int qd = jq & 3;
        *(f32x4*)(sm + L_RED + (wad ^ ((4 * (jq >> 1) + (jq & 1)) << 4)) + jq * 1024) = f32x4{a[4 * qd], a[4 * qd + 1], a[4 * qd + 2], a[4 * qd + 3]};
      };
      static_for<0, 28>([&](auto FF) AI {
        constexpr int f = FF.value, ct = (f / 7) & 1, hi = f % 7 + 7 * (f / 14);
        v4u wf;
        if constexpr (f < NWS) wf = Wd[f]; else if constexpr (f < NWS + NWL) wf = wl[f % 3]; else wf = Wd[f - NWL];
        if constexpr (f == 0 || f == 7) {
          f32x16 z;
#pragma unroll
          for (int e = 0; e < 16; ++e) z[e] = 0.f;
          acc[ct] = mfma32(wf, hf[hi], z);
        } else acc[ct] = mfma32(wf, hf[hi], acc[ct]);
        asm volatile("" : "+v"(acc[ct]));
        SBAR();
        if constexpr (f + 3 >= NWS && f + 3 < NWS + NWL) wl[f % 3] = *(const v4u*)(sm + L_WD + (w * NWL + (f + 3 - NWS)) * 1024 + l16);
        if constexpr (u == 0) {
          if constexpr (f < 6) {
            constexpr int eb[7] = {0, 1, 3, 4, 6, 7, 8};
#pragma unroll
            for (int e = eb[f]; e < eb[f + 1]; ++e) { ep_one(acc[1], e); if (e & 1) ho[e >> 1] = pk2(hv[0], hv[1]); }
          }
          if constexpr (f == 6) h_store(3);
          if constexpr (f == 11) hpl = hf_poll(1, 0);
          if constexpr (f == 18) { wait_vm<1>(); h_flag(3); }
          if constexpr (f == 19) hf_check(1, 0, hpl);
          if constexpr (f == 0) h_load(0, 13);
          constexpr int k0 = ly == 0 ? 0 : KT;
          if constexpr (f >= 2 && f < 2 + 2 * (NWR - NWS - k0) && !(f & 1)) Wd[NWS + k0 + (f - 2) / 2] = ld_wd(NWL + NWS + k0 + (f - 2) / 2);
          if constexpr (f >= 19 && (f & 1)) h_load(1, (f - 19) / 2);
        }
        if constexpr (u == 1 && pace(f) >= 0 && pace(f) < 3) h_load(1, 11 + pace(f));
        if constexpr (u == 1 && ly == 0) {
          if constexpr (pace(f) >= 3) W1[pace(f) - 3] = ldfrag<SC1>(rs, l16, w1o, pace(f) - 3);
          if constexpr (f == SGA) sga = sp_load(2, Ta);
        }
        if constexpr (f == 22) { const int ln = opaque(lane); wad = opaque(w * 8192 + ((ln ^ ((ln >> 5) << 1)) << 4)); }
        if constexpr (f == 27) {
          const int ln = opaque(lane), li16 = ln & 15, ftok = 4 * w + (ln >> 4);
          rad = opaque((2 * (li16 >> 2) + (li16 & 1)) * 1024 + (((li16 >> 1) & 1) * 32 + (ftok ^ li16)) * 16);
        }
        if constexpr (f >= 23 && f < 27) red_write(f - 23);
        if constexpr ((XB & 8) && f == 17) raw_barrier();
        if constexpr (f == GB) __syncthreads();
        SBAR();
      });
      STAMP(1, 13 + u * 5 + ly * 24);
      if constexpr (u == 1 && ly == 0) { x3_publish(0, sga); SBAR(); STAMP(1, 46); }
#pragma unroll
      for (int jq = 4; jq < 8; ++jq) red_write(jq);
      SBAR();
      if constexpr (u == 0) { h_load(1, 5); h_load(1, 6); hpl = hf_poll(1, 1); }
      SBAR();
      STAMP(1, 14 + u * 5 + ly * 24);
      __syncthreads();
      STAMP(1, 15 + u * 5 + ly * 24);
      if constexpr (u == 1 && ly == 0) x0_load();
      {
        f32x4 pr[8];
#pragma unroll
        for (int ww = 0; ww < 8; ++ww) pr[ww] = *(const f32x4*)(sm + L_RED + rad + ww * 8192);
        SBAR();
        const f32x4 a = ((pr[0] + pr[1]) + (pr[2] + pr[3])) + ((pr[4] + pr[5]) + (pr[6] + pr[7]));
        const unsigned f01 = pk2(a[0] * r2[u], a[1] * r2[u]), f23 = pk2(a[2] * r2[u], a[3] * r2[u]);
        const v2u xn = {pk2(bflo(xres[u][0]) + bflo(f01), bfhi(xres[u][0]) + bfhi(f01)), pk2(bflo(xres[u][1]) + bflo(f23), bfhi(xres[u][1]) + bfhi(f23))};
        if constexpr (ly == 1) {
          const int ln = opaque(lane);
          st64<OAUX>(xn, mk_rsrc(out, ACT_BYTES), (ln >> 4) * (D * 2) + (ln & 15) * 8, (((Ta + u) * 32 + 4 * w) * D + js * 64) * 2);
        } else {
          xres[u] = xn;
          sp_publish(2, u);
        }
      }
      SBAR();
      if constexpr (u == 0) {
        hf_check(1, 1, hpl);
#pragma unroll
        for (int i = 7; i < 11; ++i) h_load(1, i);
        SBAR();
      }
      STAMP(1, 16 + u * 5 + ly * 24);
    };
    unit(IC<0>{});
    unit(IC<1>{});
  };
  layer(IC<0>{});
  layer(IC<1>{});
  st64<SC1>(v2u{sopq(tag0), sopq(tag1)}, rs, (bid == 0 && tid == 0) ? 0 : OOB, OFF_EP);
  STAMP(1, 60);
}

extern "C" void kernel_launch(void* const* d_in, const int* in_sizes, int n_in, void* d_out, int out_size, void* d_ws,
                              size_t ws_size, hipStream_t stream) {
  const uint16_t* q = (const uint16_t*)d_in[0];
  const uint16_t* k = (const uint16_t*)d_in[1];
  const uint16_t* v = (const uint16_t*)d_in[2];
  const uint16_t* shk = (const uint16_t*)d_in[3];
  const uint16_t* ssk = (const uint16_t*)d_in[4];
  const uint16_t* ssd = (const uint16_t*)d_in[5];
  const uint16_t* wg = (const uint16_t*)d_in[7];
  const uint16_t* wu = (const uint16_t*)d_in[8];
  const uint16_t* wd = (const uint16_t*)d_in[9];
  unsigned char* ws = (unsigned char*)d_ws;
  uint16_t* out = (uint16_t*)d_out;
  (void)hipFuncSetAttribute((const void*)k_main, hipFuncAttributeMaxDynamicSharedMemorySize, L_BYTES);
  k_main<<<NAUX + 224, 512, L_BYTES, stream>>>(q, k, v, shk, ssk, ssd, wg, wu, wd, ws, out);
}
```

```cpp
#include <hip/hip_runtime.h>
#include <hip/hip_bf16.h>
#include <stdint.h>

#define DI __device__ __forceinline__
#define AI __attribute__((always_inline))
#define LAS __attribute__((address_space(3)))
typedef unsigned v4u __attribute__((ext_vector_type(4)));
typedef unsigned v2u __attribute__((ext_vector_type(2)));
typedef float f32x4 __attribute__((ext_vector_type(4)));
typedef float f32x2 __attribute__((ext_vector_type(2)));
typedef float f32x16 __attribute__((ext_vector_type(16)));
typedef short bf16x8 __attribute__((ext_vector_type(8)));
typedef short s16x4 __attribute__((ext_vector_type(4)));
typedef __bf16 bf2 __attribute__((ext_vector_type(2)));
typedef __amdgpu_buffer_rsrc_t rsrc_t;

constexpr int S = 2048, D = 448, F = 1792;
constexpr int KS1 = D / 16;
constexpr int KS2 = F / 16;
constexpr float EPS = 1e-5f;

constexpr int ACT_BYTES = S * D * 2;
constexpr int FSTR = 32;
constexpr int OFF_EP = 0;
constexpr int OFF_FW1 = 256;
constexpr int OFF_FW2 = OFF_FW1 + 28 * 32 * 128;
constexpr int OFF_FH = OFF_FW2 + 7 * KS2 * 128;
constexpr int OFF_SP = OFF_FH + 2 * 64 * KS2 * FSTR;
constexpr int SP_BYTES = 64 * 7 * 32 * 16;
constexpr int OFF_KVG = OFF_SP + 4 * SP_BYTES;
constexpr int OFF_X = OFF_KVG + 2 * ACT_BYTES;
constexpr int OFF_H = OFF_X + 2 * ACT_BYTES;
constexpr int H_BYTES = 64 * KS2 * 1024;
constexpr int OFF_W1 = OFF_H + 2 * H_BYTES;
constexpr int OFF_W2 = OFF_W1 + 112 * KS1 * 1024;
constexpr int OFF_CSP = OFF_W2 + 14 * KS2 * 1024;
constexpr int WS_BYTES = OFF_CSP + 28 * 112 * 32 * 4;
static_assert(OFF_SP % 128 == 0 && OFF_KVG % 128 == 0, "alignment");

constexpr int SC1 = 16;
DI rsrc_t mk_rsrc(const void* p, int bytes) { return __builtin_amdgcn_make_buffer_rsrc((void*)p, 0, bytes, 0x00020000); }
template <int AUX = 0> DI v4u ld128(rsrc_t r, int voff, int soff) { return __builtin_amdgcn_raw_buffer_load_b128(r, voff, soff, AUX); }
template <int AUX = 0> DI v2u ld64(rsrc_t r, int voff, int soff) { return __builtin_amdgcn_raw_buffer_load_b64(r, voff, soff, AUX); }
template <int AUX = 0> DI unsigned ld32(rsrc_t r, int voff, int soff) { return __builtin_amdgcn_raw_buffer_load_b32(r, voff, soff, AUX); }
template <int AUX = 0> DI unsigned ld16(rsrc_t r, int voff, int soff) { return __builtin_amdgcn_raw_buffer_load_b16(r, voff, soff, AUX); }
template <int AUX = 0> DI void st128(v4u v, rsrc_t r, int voff, int soff) {
  __builtin_amdgcn_raw_buffer_store_b128(v, r, voff, soff, AUX);
  asm volatile("s_nop 1" ::"v"(v));
}
template <int AUX = 0> DI void st64(v2u v, rsrc_t r, int voff, int soff) { __builtin_amdgcn_raw_buffer_store_b64(v, r, voff, soff, AUX); }
template <int AUX = 0> DI void st32(unsigned v, rsrc_t r, int voff, int soff) { __builtin_amdgcn_raw_buffer_store_b32(v, r, voff, soff, AUX); }
DI float asf(unsigned u) { return __builtin_bit_cast(float, u); }
DI unsigned asu(float f) { return __builtin_bit_cast(unsigned, f); }
DI float bflo(unsigned u) { return asf(u << 16); }
DI float bfhi(unsigned u) { return asf(u & 0xffff0000u); }
DI unsigned pk2(float a, float b) {
  f32x2 v = {a, b};
  return __builtin_bit_cast(unsigned, __builtin_convertvector(v, bf2));
}
DI float rbf(float a) { return bflo(pk2(a, a)); }
DI float dot2(unsigned a, unsigned b, float c) {
  return __builtin_amdgcn_fdot2_f32_bf16(__builtin_bit_cast(bf2, a), __builtin_bit_cast(bf2, b), c, false);
}
DI f32x16 mfma32(v4u a, v4u b, f32x16 c) {
  return __builtin_amdgcn_mfma_f32_32x32x16_bf16(__builtin_bit_cast(bf16x8, a), __builtin_bit_cast(bf16x8, b), c, 0, 0, 0);
}
DI f32x4 mfma16(v4u a, v4u b, f32x4 c) {
  return __builtin_amdgcn_mfma_f32_16x16x32_bf16(__builtin_bit_cast(bf16x8, a), __builtin_bit_cast(bf16x8, b), c, 0, 0, 0);
}
DI v4u tr_frag(LAS unsigned char* p, int second) {
  const v2u l2 = __builtin_bit_cast(v2u, __builtin_amdgcn_ds_read_tr16_b64_v4i16((LAS s16x4*)p));
  const v2u h2 = __builtin_bit_cast(v2u, __builtin_amdgcn_ds_read_tr16_b64_v4i16((LAS s16x4*)(p + second)));
  return v4u{l2[0], l2[1], h2[0], h2[1]};
}
template <int CTRL> DI float dpp_add(float v) {
  return v + asf(__builtin_amdgcn_update_dpp(0, asu(v), CTRL, 0xf, 0xf, true));
}
DI float row16_sum(float v) {
  v = dpp_add<0x128>(v); v = dpp_add<0x124>(v); v = dpp_add<0x122>(v); v = dpp_add<0x121>(v);
  return v;
}
#define PIN2(a, b) asm volatile("" : "+v"(a), "+v"(b))
#define SBAR() __builtin_amdgcn_sched_barrier(0)
#define DRAIN() asm volatile("s_waitcnt vmcnt(0)" ::: "memory")
#define ORDER() __builtin_amdgcn_fence(__ATOMIC_ACQUIRE, "wavefront")
#define STAMPC(kid, i)
#define STAMP(kid, i)
constexpr unsigned ONES = 0x3f803f80u;
constexpr unsigned EMPTY = 0xffffffffu;
#define POLL_SLEEP 2
constexpr unsigned SPIN_CAP = 1u << 16;

DI void row_stats(float s, float ss, float& m, float& r) {
  m = s * (1.0f / D);
  const float var = fmaxf(fmaf(ss, 1.0f / D, -m * m), 0.0f);
  r = __builtin_amdgcn_rsqf(var + EPS);
}
constexpr int NWL = 12;
constexpr int NWR = 28 - NWL;
constexpr int NDROP = 14;
constexpr int XBUF = D * 64;
constexpr int L_XB = 0, L_ST = 2 * XBUF, L_CS = L_ST + 3 * 2048, L_RED = 0, L_WD = 65536, L_BYTES = L_WD + NWL * 8 * 1024;
static_assert(L_CS + 8 * 128 <= L_WD && L_BYTES <= 163840, "LDS map");
constexpr int CV_HSTR = 4160, CV_USTR = 2304 * 2;
constexpr int CV_H = 0, CV_U = CV_H + 2 * 4 * CV_HSTR, CV_RED = L_WD, CV_W = 43008;
static_assert(CV_U + 2 * CV_USTR <= CV_W && CV_W + 18432 <= L_WD && CV_RED + 65536 <= L_BYTES, "conv LDS map");

#define ABL 0
#define PDV 3
template <int V> struct IC { static constexpr int value = V; };
template <int I, int N, class Fn> DI void static_for(Fn&& fn) {
  if constexpr (I < N) { fn(IC<I>{}); static_for<I + 1, N>(fn); }
}
template <int N> DI void wait_vm() { asm volatile("s_waitcnt vmcnt(%0)" ::"n"(N) : "memory"); }
DI float half_sum(float v) {
  const auto r = __builtin_amdgcn_permlane32_swap(asu(v), asu(v), false, false);
  return asf(r[0]) + asf(r[1]);
}
DI unsigned umax3(unsigned a, unsigned b, unsigned c) { return max(max(a, b), c); }
constexpr int OOB = (int)0x80000000;
DI void raw_barrier() { asm volatile("" ::: "memory"); __builtin_amdgcn_s_barrier(); asm volatile("" ::: "memory"); }
template <int AUX = 0> DI v4u ldfrag(rsrc_t r, int l16, int base, int i) { return ld128<AUX>(r, l16, base + i * 1024); }
template <bool DEV, int O0> DI void dma2(rsrc_t r, int l16, int soff, unsigned lds) {
  if constexpr (DEV)
    asm volatile("s_mov_b32 m0, %0\n\ts_nop 0\n\tbuffer_load_dwordx4 %1, %2, %3 offen offset:%4 sc1 lds\n\tbuffer_load_dwordx4 %1, %2, %3 offen offset:%5 sc1 lds"
                 ::"s"(lds), "v"(l16), "s"(r), "s"(soff), "n"(O0), "n"(O0 + 1024) : "m0", "memory");
  else
    asm volatile("s_mov_b32 m0, %0\n\ts_nop 0\n\tbuffer_load_dwordx4 %1, %2, %3 offen offset:%4 lds\n\tbuffer_load_dwordx4 %1, %2, %3 offen offset:%5 lds"
                 ::"s"(lds), "v"(l16), "s"(r), "s"(soff), "n"(O0), "n"(O0 + 1024) : "m0", "memory");
}
DI void dma1(rsrc_t r, int l16, int soff, unsigned lds) {
  asm volatile("s_mov_b32 m0, %0\n\ts_nop 0\n\tbuffer_load_dwordx4 %1, %2, %3 offen sc1 lds" ::"s"(lds), "v"(l16), "s"(r), "s"(soff) : "m0", "memory");
}
DI int opaque(int v) { asm volatile("" : "+v"(v)); return v; }
DI unsigned sopq(unsigned v) { asm volatile("" : "+s"(v)); return v; }
DI unsigned nz(unsigned v) { return min(v, EMPTY - 1u); }

#define NWSV 11
constexpr int NWS = NWSV;
constexpr int BAR = KS1 - 1 - PDV;
constexpr int H_ST = 10;
constexpr int XC = 14;
constexpr int pace(int i) {
  constexpr int sl[11] = {0, 3, 5, 8, 11, 13, 16, 19, 21, 24, 26};
  for (int n = 0; n < 11; ++n) if (sl[n] == i) return n;
  return -1;
}
constexpr int wl_slot(int ly, int t, int i) {
  return (ly != 0 || t == 3) ? -1 : t == 0 ? (i == 19 ? 0 : i == 21 ? 1 : i == 23 ? 2 : i == 26 ? 3 : -1) : i == 16 ? t * 4 : i == 19 ? t * 4 + 1 : i == 23 ? t * 4 + 2 : i == 27 ? t * 4 + 3 : -1;
}

#define SPIN(cond_ok, reload) do { if (__builtin_expect(!(cond_ok), 0)) { unsigned spins_ = 0; \
  _Pragma("unroll 1") do { asm volatile("" ::: "memory"); reload; } while (!(cond_ok) && ++spins_ < SPIN_CAP); } } while (0)
DI unsigned mk_tag1(v2u ep, unsigned tag0) {
  unsigned t = __builtin_amdgcn_readfirstlane(ep[1]) + 0x9e3779b9u;
  if (t - tag0 + 1u <= 2u) t += 0x10001u;
  return t;
}
constexpr int NAUX = 32;
#define XC0 18
#define SGE 0
#define OAUX 0
#define PR 0
#define KT 1
#define AUXD 4
#define WD 1
#define XB 7
#define GB 6
#define X3S 2
#define SGA 17
#define DELAY2 0
#define DELAY3 0
#define FWP 0
#define FWC 5
#define PSLEEP
#define NPRE 18
#define NPRE2 24
#define DELAY1 40
#define LATE_N 0

__global__ void __launch_bounds__(512, 2) k_main(const uint16_t* __restrict__ q, const uint16_t* __restrict__ kin,
                                                 const uint16_t* __restrict__ vin, const uint16_t* __restrict__ shk,
                                                 const uint16_t* __restrict__ ssk, const uint16_t* __restrict__ ssd,
                                                 const uint16_t* __restrict__ wg, const uint16_t* __restrict__ wu,
                                                 const uint16_t* __restrict__ wdn, unsigned char* __restrict__ ws,
                                                 uint16_t* __restrict__ out) {
  extern __shared__ __attribute__((aligned(16))) unsigned char sm[];
  LAS unsigned char* L = (LAS unsigned char*)sm;
  const int tid = threadIdx.x, lane = tid & 63;
  int w_ = __builtin_amdgcn_readfirstlane(tid >> 6);
  asm volatile("" : "+s"(w_));
  const int w = w_;
  const rsrc_t rs = mk_rsrc(ws, WS_BYTES);
  const int l16 = lane * 16, tokl = lane & 31, hh = lane >> 5, i16 = lane & 15;
  STAMP(1, 0);
  const v2u ep = ld64<SC1>(rs, 0, OFF_EP);

  const int tr_off = (hh * 8 + (i16 >> 2)) * 144 + (8 * (i16 & 1) + 4 * ((i16 >> 1) & 1)) * 2;

  if (blockIdx.x < NAUX) {
    const int r = tid >> 3, qq = tid & 7;
    v4u a[4][2];
    for (int i = 0; i < AUXD; ++i) __builtin_amdgcn_s_sleep(36);
#pragma unroll
    for (int n = 0; n < 4; ++n) {
      const int b = min((int)blockIdx.x + 32 * n, 97);
      const size_t src = (size_t)((b / 7) * 128 + r) * D + (b % 7) * 64 + qq * 8;
      a[n][0] = *(const v4u*)(wdn + src); a[n][1] = *(const v4u*)(wdn + src + 64 * D);
    }
#pragma unroll
    for (int n = 0; n < 4; ++n) {
      *(v4u*)(sm + n * 18432 + r * 144 + qq * 16) = a[n][0];
      *(v4u*)(sm + n * 18432 + (64 + r) * 144 + qq * 16) = a[n][1];
    }
    __syncthreads();
    const unsigned tg0 = __builtin_amdgcn_readfirstlane(ep[0]) + 1u;
    const v2u tg = {tg0, mk_tag1(ep, tg0)};
#pragma unroll
    for (int n = 0; n < 4; ++n) {
      const int b = blockIdx.x + 32 * n, kb = b / 7, cb = b % 7;
#pragma unroll
      for (int ct = 0; ct < 2; ++ct) {
        const v4u o = tr_frag(L + n * 18432 + w * 16 * 144 + tr_off + (ct * 32 + ((lane >> 4) & 1) * 16) * 2, 4 * 144);
        st128<SC1>(o, rs, b < 98 ? l16 : OOB, OFF_W2 + ((cb * 2 + ct) * KS2 + kb * 8 + w) * 1024);
      }
    }
    DRAIN();
    {
      const int b = blockIdx.x + 32 * (lane >> 4);
      st64<SC1>(tg, rs, b < 98 ? ((b % 7) * KS2 + (b / 7) * 8 + w) * 128 + i16 * 8 : OOB, OFF_FW2);
    }
    return;
  }

  if (PR == 1 && w >= 4) __builtin_amdgcn_s_setprio(1);
  const int bid = blockIdx.x - NAUX;
  const int xcd = bid & 7, li = bid >> 3;
  const int g = (xcd >> 1) * 4 + li / 7, hs = xcd & 1, js = li % 7, j = hs * 7 + js;
  const int fg = j * 8 + w;
  const int sj = w < 7 ? w : 6;
  const int w1o = OFF_W1 + fg * KS1 * 1024;
  const int Ta = g * 4 + hs * 2;
  const unsigned lds0 = (unsigned)(size_t)L;

  v4u W1[KS1], Wd[NWR];
  float csum;
  unsigned csp[14];
  v2u xres[2], yres[2];
  unsigned tag0, tag1;
  const int kse = hs * 56 + w * 7, ksl = (hs ^ 1) * 56 + w * 7;
  auto wd_src = [&](int f) AI { return OFF_W2 + ((js * 2 + ((f / 7) & 1)) * KS2 + (f < 14 ? kse : ksl) + f % 7) * 1024; };
  auto ld_wd = [&](int f) AI { return ld128<SC1>(rs, (ABL & 1) ? OOB : l16, wd_src(f)); };
  auto ld_wgt = [&](int n) AI {
    if (n < KS1) W1[n] = ldfrag<SC1>(rs, l16, (ABL & 8) ? OFF_W1 : w1o, (ABL & 8) ? 0 : n);
    else if (n < KS1 + NWS) Wd[n - KS1] = ld_wd(n - KS1);
  };
  auto tag_ok = [&](v4u v) AI { return __all(((v[1] ^ sopq(tag0)) | (v[3] ^ sopq(tag1))) == 0u); };
  auto flag_ok = [&](v2u v) AI { return __all(((v[0] ^ sopq(tag0)) | (v[1] ^ sopq(tag1))) == 0u); };
  auto sp_publish = [&](int which, int u) AI {
    const int ln = opaque(lane);
    const v2u xn = xres[u];
    const float s = row16_sum(dot2(xn[0], ONES, dot2(xn[1], ONES, 0.f))), ss = row16_sum(dot2(xn[0], xn[0], dot2(xn[1], xn[1], 0.f)));
    st128<SC1>(v4u{asu(s), sopq(tag0), asu(ss), sopq(tag1)}, rs, (ln & 15) == 0 ? ln : OOB, OFF_SP + which * SP_BYTES + ((Ta + u) * 7 + js) * 512 + w * 64);
  };
  auto sp_load = [&](int which, int T) AI {
    const int ln = opaque(lane);
    return ld128<SC1>(rs, ((ln & 15) < 7 ? (ln & 15) * 512 : 0) + (ln & 48), OFF_SP + which * SP_BYTES + T * 7 * 512 + w * 64);
  };
  auto sp_stats = [&](int which, int u, v4u sp, float& m, float& r) AI {
    SPIN(tag_ok(sp), sp = sp_load(which, Ta + u));
    row_stats(row16_sum(i16 < 7 ? asf(sp[0]) : 0.f), row16_sum(i16 < 7 ? asf(sp[2]) : 0.f), m, r);
  };
  auto gate_publish = [&](int ly, int u, float r, float c) AI {
    v2u xn;
#pragma unroll
    for (int e = 0; e < 2; ++e) {
      const float xl = bflo(xres[u][e]), xh = bfhi(xres[u][e]);
      xn[e] = nz(pk2(fmaf(fmaf(xl, r, c), bflo(yres[u][e]), xl), fmaf(fmaf(xh, r, c), bfhi(yres[u][e]), xh)));
    }
    xres[u] = xn;
    const int ln = opaque(lane);
    st64<SC1>(xn, rs, (ln * 8) ^ (((2 * w + (ln >> 5)) & 7) << 4), OFF_X + ly * ACT_BYTES + ((Ta + u) * 7 + js) * 4096 + w * 512);
    sp_publish(2 * ly + 1, u);
  };
  auto x3_publish = [&](int u, v4u sp) AI {
    float m, r;
    sp_stats(2, u, sp, m, r);
    gate_publish(1, u, r, -m * r);
  };

  {
    const int c0 = bid * 2;
    const int r = tid >> 3, qq = tid & 7;
    const int tok = Ta * 32 + r, ca = js * 64 + qq * 8;
    const rsrc_t rk = mk_rsrc(kin, ACT_BYTES), rh = mk_rsrc(ssk, ACT_BYTES);
    v4u kk[4], sw[4];
    const v4u swl = *(const v4u*)(shk + js * 256 + tokl * 8);
#pragma unroll
    for (int i = 0; i < 4; ++i) kk[i] = ld128(rk, ((tok - i) * D + ca) * 2, 0);
    const v4u vv = *(const v4u*)(vin + (size_t)tok * D + ca);
    const int ch = tid >> 8, i8 = tid & 255;
    const v4u d = ld128(rh, tid * 16, c0 * S * 2);
    const v2u e = ld64(rh, tid * 16 + 16 + c0 * S * 2, 0);
    STAMP(1, 28);
    SBAR();
    *(v4u*)(sm + CV_RED + 16384 + w * 1024 + l16) = swl;
#pragma unroll
    for (int i = 0; i < 4; ++i) sw[i] = *(const v4u*)(sm + CV_RED + 16384 + w * 1024 + qq * 64 + i * 16);
    tag0 = __builtin_amdgcn_readfirstlane(ep[0]) + 1u;
    tag1 = mk_tag1(ep, tag0);
    {
      uint16_t* tl = (uint16_t*)(sm + CV_RED) + qq * 8 * 64 + ((63 - r) ^ (qq << 3));
#pragma unroll
      for (int e2 = 0; e2 < 4; ++e2) {
        const unsigned wa01 = sw[e2][0], wa23 = sw[e2][1], wb01 = sw[e2][2], wb23 = sw[e2][3];
        unsigned p = pk2(bflo(kk[3][e2]) * bfhi(wa23), bfhi(kk[3][e2]) * bfhi(wb23));
        unsigned acc = p;
        p = pk2(bflo(kk[2][e2]) * bflo(wa23), bfhi(kk[2][e2]) * bflo(wb23));
        acc = pk2(bflo(acc) + bflo(p), bfhi(acc) + bfhi(p));
        p = pk2(bflo(kk[1][e2]) * bfhi(wa01), bfhi(kk[1][e2]) * bfhi(wb01));
        acc = pk2(bflo(acc) + bflo(p), bfhi(acc) + bfhi(p));
        p = pk2(bflo(kk[0][e2]) * bflo(wa01), bfhi(kk[0][e2]) * bflo(wb01));
        acc = pk2(bflo(acc) + bflo(p), bfhi(acc) + bfhi(p));
        const unsigned kv = pk2(bflo(acc) * bflo(vv[e2]), bfhi(acc) * bfhi(vv[e2]));
        tl[(2 * e2) * 64] = (uint16_t)kv;
        tl[(2 * e2 + 1) * 64] = (uint16_t)(kv >> 16);
      }
    }
    SBAR();
    const int wt = min(bid, 195), kb = wt / 28, fb = wt % 28, ks = kb * 4 + (w & 3), rl = lane >> 3;
    v4u wa[4];
    {
      const rsrc_t rg = mk_rsrc(wg, D * F * 2), ru = mk_rsrc(wu, D * F * 2);
      const int vo = (w < 4 || WD) ? OOB : ((ks * 16 + rl) * F + fb * 64 + qq * 8) * 2;
      wa[0] = ld128(rg, vo, 0); wa[1] = ld128(rg, vo, 16 * F); wa[2] = ld128(ru, vo, 0); wa[3] = ld128(ru, vo, 16 * F);
    }
    SBAR();
    STAMP(1, 29);
    __syncthreads();
    STAMP(1, 1);
#pragma unroll
    for (int i = 0; i < 2; ++i) {
      const int cl = i * 32 + (tid >> 4);
      const v2u o = *(const v2u*)(sm + CV_RED + cl * 128 + ((i16 ^ ((cl >> 3) << 1)) * 8));
      st128<SC1>(v4u{o[0], sopq(tag0), o[1], sopq(tag1)}, rs, (tid >> 4) * 8192 + i16 * 16, OFF_KVG + (js * 64 + i * 32) * 8192 + (S - 64 - Ta * 32) * 4);
    }
    SBAR();
    {
      const unsigned a0 = __builtin_amdgcn_alignbit(d[1], d[0], 16), a1 = __builtin_amdgcn_alignbit(d[2], d[1], 16),
                     a2 = __builtin_amdgcn_alignbit(d[3], d[2], 16), a3 = __builtin_amdgcn_alignbit(e[0], d[3], 16),
                     a4 = __builtin_amdgcn_alignbit(e[1], e[0], 16);
      unsigned char* hb = sm + CV_H + ch * 4 * CV_HSTR + 32 + i8 * 16;
      *(v4u*)(hb) = d;
      *(v4u*)(hb + CV_HSTR) = v4u{a0, a1, a2, a3};
      *(v4u*)(hb + 2 * CV_HSTR) = v4u{d[1], d[2], d[3], e[0]};
      *(v4u*)(hb + 3 * CV_HSTR) = v4u{a1, a2, a3, a4};
      const unsigned d0 = __builtin_amdgcn_readfirstlane(d[0]), d1 = __builtin_amdgcn_readfirstlane(d[1]);
      if (i8 < 40) {
        const int q8 = i8 - 32, sg = q8 >> 1;
        v4u pv = {0u, 0u, 0u, 0u};
        if (q8 == 3) pv[3] = d0 << 16;
        if (q8 == 5) pv[3] = d0;
        if (q8 == 7) { pv[2] = d0 << 16; pv[3] = __builtin_amdgcn_alignbit(d1, d0, 16); }
        *(v4u*)(sm + (q8 < 0 ? CV_U + ch * CV_USTR + 4096 + i8 * 16 : CV_H + (ch * 4 + sg) * CV_HSTR + (q8 & 1) * 16)) = pv;
      }
    }
    STAMP(1, 2);
    float dcoef;
    auto own_load = [&]() AI {
#pragma unroll
      for (int u = 0; u < 2; ++u) xres[u] = *(const v2u*)(q + (size_t)((Ta + u) * 32 + 4 * w + (lane >> 4)) * D + js * 64 + i16 * 4);
      dcoef = bflo(ssd[c0 + ((lane >> 4) & 1)]);
    };
    if (w < 4) {
      v4u ug[4];
      auto u_load = [&]() AI {
#pragma unroll
        for (int i = 0; i < 4; ++i) ug[i] = ld128<SC1>(rs, tid * 16, OFF_KVG + c0 * 8192 + i * 4096);
      };
      __builtin_amdgcn_s_sleep(DELAY1);
      u_load();
      SPIN(tag_ok(ug[0]) && tag_ok(ug[1]) && tag_ok(ug[2]) && tag_ok(ug[3]), u_load());
#pragma unroll
      for (int i = 0; i < 4; ++i) *(v2u*)(sm + CV_U + (i >> 1) * CV_USTR + (i & 1) * 2048 + tid * 8) = v2u{ug[i][0], ug[i][2]};
      own_load();
    } else {
      if (WD) {
        __builtin_amdgcn_s_sleep(WD);
        const rsrc_t rg = mk_rsrc(wg, D * F * 2), ru = mk_rsrc(wu, D * F * 2);
        const int vo = ((ks * 16 + rl) * F + fb * 64 + qq * 8) * 2;
        wa[0] = ld128(rg, vo, 0); wa[1] = ld128(rg, vo, 16 * F); wa[2] = ld128(ru, vo, 0); wa[3] = ld128(ru, vo, 16 * F);
      }
      own_load();
      unsigned char* wb = sm + CV_W + (w - 4) * (32 * 144) + rl * 144 + qq * 16;
      *(v4u*)wb = wa[0]; *(v4u*)(wb + 8 * 144) = wa[1]; *(v4u*)(wb + 16 * 144) = wa[2]; *(v4u*)(wb + 24 * 144) = wa[3];
#pragma unroll
      for (int fgl = 0; fgl < 4; ++fgl) {
        const v4u o = tr_frag(L + CV_W + (w - 4) * (32 * 144) + ((lane >> 4) & 1) * (16 * 144) + tr_off + fgl * 32, 4 * 144);
        st128<SC1>(o, rs, bid < 196 ? l16 : OOB, OFF_W1 + ((fb * 4 + fgl) * KS1 + ks) * 1024);
        const float cs = dot2(o[3], ONES, dot2(o[2], ONES, dot2(o[1], ONES, dot2(o[0], ONES, 0.f))));
        st32<SC1>(asu(half_sum(cs)), rs, (bid < 196 && lane < 32) ? lane * 4 : OOB, OFF_CSP + (ks * 112 + fb * 4 + fgl) * 128);
      }
      DRAIN();
      st64<SC1>(v2u{sopq(tag0), sopq(tag1)}, rs, (bid < 196 && lane < 16) ? lane * 8 : OOB, OFF_FW1 + (fb * 32 + ks) * 128);
    }
    STAMP(1, 22);
    auto fw_poll = [&]() AI { return ld64<SC1>(rs, min(opaque(lane), 27) * 128, OFF_FW1 + (fg >> 2) * 4096); };
    __syncthreads();
    STAMP(1, 3);
    float r0[2], c0n[2];
    v2u fw;
    {
      const int cw = w >> 2, sp = w & 3;
      const int gq = lane >> 4;
      const int sg = (1 + i16) & 3;
      const unsigned char* fbp = sm + CV_H + (cw * 4 + sg) * CV_HSTR + (1 + i16 + 8 * gq - sg) * 2 + sp * 128;
      const unsigned char* gb = sm + CV_U + cw * CV_USTR + (2032 - 16 * i16 + 8 * gq) * 2 + sp * 128 - 3584;
      auto rdF = [&](int n) AI -> v4u {
        const v2u lo = *(const v2u*)(fbp + (n >> 3) * 64 + (n & 7) * 512), hi = *(const v2u*)(fbp + (n >> 3) * 64 + (n & 7) * 512 + 8);
        return v4u{lo[0], lo[1], hi[0], hi[1]};
      };
      f32x4 acc[8];
#pragma unroll
      for (int i = 0; i < 8; ++i) acc[i] = f32x4{0.f, 0.f, 0.f, 0.f};
      v4u Gf[8], Ff[3];
      Ff[0] = rdF(0);
#pragma unroll
      for (int dl = 7; dl >= 0; --dl) Gf[dl] = *(const v4u*)(gb + (7 - dl) * 512);
      Ff[1] = rdF(1);
      SBAR();
      static_for<0, 16>([&](auto N) AI {
        constexpr int n = N.value, jj = n & 7;
        if constexpr (n + 2 < 16) Ff[(n + 2) % 3] = rdF(n + 2);
#pragma unroll
        for (int dl = 7 - jj; dl >= 0; --dl) { acc[jj + dl] = mfma16(Ff[n % 3], Gf[dl], acc[jj + dl]); asm volatile("" : "+v"(acc[jj + dl])); }
        if constexpr (n < 8) Gf[7 - jj] = *(const v4u*)(gb + 64 + jj * 512);
        if constexpr (n == FWP) fw = fw_poll();
        if constexpr (n == FWC) { SPIN(flag_ok(fw), fw = fw_poll()); ORDER(); }
        if constexpr (n >= FWC) ld_wgt(n - FWC);
        const v4u ev = {EMPTY, EMPTY, EMPTY, EMPTY};
        if constexpr (n == 1) st128<SC1>(ev, rs, (tid >> 8) * XBUF + (tid & 255) * 16, OFF_X + (Ta * 7 + js) * 4096);
        if constexpr (n == 2)
          st128<SC1>(ev, rs, w < 4 ? OFF_SP + w * SP_BYTES + ((Ta + hh) * 7 + js) * 512 + tokl * 16
                           : w < 6 ? OFF_FH + (((w - 4) * 64 + g * 4 + (lane >> 4)) * KS2 + j * 8) * FSTR + i16 * 16 : OOB, 0);
        SBAR();
      });
      STAMP(1, 4);
      const int slot = (4 * i16 + gq) ^ ((i16 >> 1) & 3);
#pragma unroll
      for (int i = 0; i < 8; ++i) *(f32x4*)(sm + CV_RED + ((w * 8 + i) * 64 + slot) * 16) = acc[i];
    }
    const int fch = (lane >> 4) & 1;
    v2u ur[2];
#pragma unroll
    for (int i = 0; i < 2; ++i) ur[i] = *(const v2u*)(sm + CV_U + fch * CV_USTR + (2044 - (256 * w + (2 * i + hh) * 64 + i16 * 4)) * 2);
    wait_vm<16 - FWC>();
    __syncthreads();
    STAMP(1, 5);
#pragma unroll
    for (int i = 0; i < 2; ++i) {
      const int qi = (2 * i + hh) * 16 + i16, sl = qi ^ ((qi >> 3) & 3);
      f32x4 y = *(const f32x4*)(sm + CV_RED + ((fch * 32 + w) * 64 + sl) * 16);
#pragma unroll
      for (int sp = 1; sp < 4; ++sp) y += *(const f32x4*)(sm + CV_RED + (((fch * 4 + sp) * 8 + w) * 64 + sl) * 16);
      unsigned yy[2];
#pragma unroll
      for (int e2 = 0; e2 < 2; ++e2) {
        const unsigned cb = pk2(y[2 * e2], y[2 * e2 + 1]);
        const unsigned ud = pk2(bfhi(ur[i][1 - e2]) * dcoef, bflo(ur[i][1 - e2]) * dcoef);
        yy[e2] = pk2(bflo(cb) + bflo(ud), bfhi(cb) + bfhi(ud));
      }
      st128<SC1>(v4u{yy[0], sopq(tag1), yy[1], sopq(tag0)}, rs, fch * 8192 + hh * 256 + i16 * 16, OFF_KVG + c0 * 8192 + (4 * w + 2 * i) * 256);
    }
    SBAR();
    STAMP(1, 24);
    sp_publish(0, 0);
    sp_publish(0, 1);
    raw_barrier();
#pragma unroll
    for (int i = 16 - FWC; i < NPRE; ++i) ld_wgt(i);
    SBAR();
    STAMP(1, 23);
    {
      v4u yg[2];
      auto y_load = [&]() AI {
        yg[0] = ld128<SC1>(rs, (tid >> 4) * 8192 + i16 * 16, OFF_KVG + js * 64 * 8192 + (Ta >> 1) * 256);
        yg[1] = ld128<SC1>(rs, (tid >> 4) * 8192 + i16 * 16, OFF_KVG + (js * 64 + 32) * 8192 + (Ta >> 1) * 256);
      };
      __builtin_amdgcn_s_sleep(DELAY2);
      y_load();
      v4u sp0 = sp_load(0, Ta), sp1 = sp_load(0, Ta + 1), spo = sp_load(0, Ta ^ 2);
      SPIN(__all(((yg[0][1] ^ tag1) | (yg[0][3] ^ tag0) | (yg[1][1] ^ tag1) | (yg[1][3] ^ tag0)) == 0u), y_load());
      STAMP(1, 27);
#pragma unroll
      for (int i = 0; i < 2; ++i) {
        const int cl = i * 32 + (tid >> 4);
        *(v2u*)(sm + L_XB + XBUF + cl * 128 + ((i16 ^ (cl >> 2)) * 8)) = v2u{yg[i][0], yg[i][2]};
      }
      { float m; sp_stats(0, 0, sp0, m, r0[0]); c0n[0] = -m * r0[0]; sp_stats(0, 1, sp1, m, r0[1]); c0n[1] = -m * r0[1]; }
      SPIN(tag_ok(spo), spo = sp_load(0, Ta ^ 2));
      __syncthreads();
      STAMP(1, 25);
#pragma unroll
      for (int u = 0; u < 2; ++u) {
        const uint16_t* gp = (const uint16_t*)(sm + L_XB + XBUF + i16 * 512 + (((u * 8 + w) ^ i16) * 8) + (lane >> 4) * 2);
        v2u yv = {(unsigned)gp[0] | ((unsigned)gp[64] << 16), (unsigned)gp[128] | ((unsigned)gp[192] << 16)};
        asm volatile("" : "+v"(yv));
        yres[u] = yv;
        gate_publish(0, u, r0[u], c0n[u]);
        asm volatile("" : "+v"(yres[u]));
      }
    }
    SBAR();
    STAMP(1, 26);
    raw_barrier();
#pragma unroll
    for (int i = 0; i < 14; ++i) csp[i] = ld32<SC1>(rs, (hh * 3584 + tokl) * 4, OFF_CSP + (i * 2 * 3584 + fg * 32) * 4);
#pragma unroll
    for (int i = NPRE; i < NPRE2; ++i) ld_wgt(i);
    SBAR();
  }

  v4u xs[4];
  v2u sst;
  auto tile_of = [&](int t) AI { return g * 4 + (((t & 1) ^ hs) << 1) + (t >> 1); };
  auto x_src = [&](int ly, int t) AI { return OFF_X + ly * ACT_BYTES + tile_of(t) * XBUF + sj * 4096; };
  auto s_load = [&](int ly, int t) AI { sst = ld64<SC1>(rs, (l16 & 511) + (l16 >> 9) * 8, OFF_SP + (2 * ly + 1) * SP_BYTES + (tile_of(t) * 7 + sj) * 512); };
  auto x0_load = [&]() AI {
#pragma unroll
    for (int i = 0; i < 4; ++i) xs[i] = ldfrag<SC1>(rs, l16, x_src(1, 0), i);
    s_load(1, 0);
  };

  auto layer = [&](auto LY) AI {
    constexpr int ly = decltype(LY)::value;
    const int lane = opaque(tid & 63), tokl = lane & 31, hh = lane >> 5;
    auto x_ok = [&]() AI {
      unsigned mx = 0u;
#pragma unroll
      for (int i = 0; i < 4; ++i) mx = umax3(umax3(mx, xs[i][0], xs[i][1]), xs[i][2], xs[i][3]);
      return __all((mx != EMPTY) & (sst[1] == (hh ? tag1 : tag0)));
    };
    auto x_dma = [&](int t, int half) AI {
      const unsigned dst = lds0 + L_XB + (t & 1) * XBUF + sj * 4096;
      if (half) { dma2<true, 2048>(rs, l16, x_src(ly, t), dst); s_load(ly, t); }
      else dma2<true, 0>(rs, l16, x_src(ly, t), dst);
    };
    auto x_fetch = [&](int t) AI {
      unsigned spins = 0;
      bool ok;
#pragma unroll 1
      do {
        x_dma(t, 0); x_dma(t, 1); DRAIN();
        unsigned mx = 0u;
#pragma unroll
        for (int i = 0; i < 4; i += 2) {
          const v4u a = *(const v4u*)(sm + L_XB + (t & 1) * XBUF + sj * 4096 + i * 1024 + l16), b = *(const v4u*)(sm + L_XB + (t & 1) * XBUF + sj * 4096 + i * 1024 + 1024 + l16);
          mx = umax3(umax3(a[0], a[1], a[2]), umax3(a[3], b[0], b[1]), umax3(b[2], b[3], mx));
          asm volatile("" : "+v"(mx) :: "memory");
        }
        ok = __all((mx != EMPTY) & (sst[1] == (hh ? tag1 : tag0)));
      } while (!ok && ++spins < SPIN_CAP);
    };
    auto st_write = [&](int t) AI {
      unsigned char* sp = sm + L_ST + (t % 3) * 2048 + (sj >> 2) * 512 + tokl * 16 + (sj & 3) * 4;
      *(unsigned*)(sp + hh * 1024) = sst[0];
    };
    const int xrd = tokl * 128 + ((hh ^ ((tokl >> 1) & 7)) << 4);
    auto xfrag = [&](int t, int ks) AI -> v4u { return *(const v4u*)(sm + L_XB + (t & 1) * XBUF + (xrd ^ ((ks & 3) << 5)) + (ks >> 2) * 4096); };
    const int hbase = OFF_H + ly * H_BYTES;
    v4u hf[14];
    auto h_load = [&](int u, int i) AI { hf[i] = ldfrag<SC1>(rs, (ABL & 2) ? OOB : l16, hbase + ((Ta + u) * KS2 + (i < 7 ? kse : ksl)) * 1024, i % 7); };
    v2u tagv = {tag0, tag1};
    asm volatile("" : "+v"(tagv));
    auto h_flag = [&](int t) AI { st64<SC1>(tagv, rs, opaque(l16) == 0 ? 0 : OOB, OFF_FH + ((ly * 64 + tile_of(t)) * KS2 + fg) * FSTR); };
    auto hf_poll = [&](int u, int half) AI { return ld64<SC1>(rs, min(opaque(l16) * (FSTR / 16), 6 * FSTR), OFF_FH + ((ly * 64 + Ta + u) * KS2 + (half ? ksl : kse)) * FSTR); };
    auto hf_check = [&](int u, int half, v2u v) AI { SPIN(flag_ok(v), v = hf_poll(u, half)); ORDER(); };
    v2u hpl;

    v4u sgr;
    auto cs_write = [&]() AI {
      const int ln = opaque(lane), csrow = (ln & 16) + 8 * ((ln >> 2) & 1) + (ln & 3) + 4 * ((ln >> 3) & 1);
      *(float*)(sm + L_CS + w * 128 + csrow * 4) = csum;
    };
    if (ly == 0) {
      __builtin_amdgcn_s_sleep(DELAY3);
      x_fetch(0);
      STAMP(1, 17);
      st_write(0);
      STAMP(1, 6);
      __syncthreads();
      x_dma(1, 0); x_dma(1, 1);
    } else {
      SPIN(x_ok(), x0_load());
      STAMP(1, 30);
      if (SGE) sgr = sp_load(2, Ta + 1);
      __syncthreads();
      cs_write();
#pragma unroll
      for (int i = 0; i < 4; ++i) *(v4u*)(sm + L_XB + sj * 4096 + i * 1024 + l16) = xs[i];
      st_write(0);
      if (SGE) asm volatile("" : "+v"(sgr)); else sgr = sp_load(2, Ta + 1);
      x_dma(1, 0); x_dma(1, 1);
      asm volatile("s_waitcnt lgkmcnt(0)" ::: "memory");
      raw_barrier();
    }
    STAMP(1, 7 + ly * 24);
    if (ly == 1) STAMPC(1, 62);

#define FST(q)
    f32x16 acc[2];
    constexpr int PD = PDV;
    v4u xf[PD], ho;
    f32x4 p0, p1, p2, p3;
    float mean = 0.f, r2[2];
#pragma unroll
    for (int i = 0; i < PD; ++i) xf[i] = xfrag(0, i + (ly == 1 ? NDROP : 0));
    auto ep_cs = [&](int hq) AI {
      const unsigned char* cp = sm + L_CS + w * 128 + hh * 32;
      if (hq) { p2 = *(const f32x4*)(cp + 16); p3 = *(const f32x4*)(cp + 80); } else { p0 = *(const f32x4*)cp; p1 = *(const f32x4*)(cp + 64); }
    };
    float hv[2], sq = 0.f;
    auto ep_one = [&](const f32x16& a, int i) AI {
      hv[i & 1] = fmaxf(fmaf(mean, i < 4 ? p0[i & 3] : p2[i & 3], a[i]), 0.f) * fmaf(mean, i < 4 ? p1[i & 3] : p3[i & 3], a[8 + i]);
      asm volatile("" : "+v"(hv[i & 1]));
    };
    auto h_store = [&](int t) AI { st128<SC1>(ho, rs, (ABL & 4) ? OOB : l16, hbase + (tile_of(t) * KS2 + fg) * 1024); };

    FST(0);
    static_for<0, 4>([&](auto TT) AI {
      constexpr int t = TT.value;
      constexpr int rot = (ly == 1 && t == 0) ? NDROP : 0;
      v4u cb0, cb1, cb2, cb3;
      unsigned cmx = 0u;
      float ga = 0.f, gb = 0.f;
      static_for<0, KS1>([&](auto II) AI {
        constexpr int i = II.value, n = t * KS1 + i;
        if constexpr (i == 0) {
          f32x16 z;
#pragma unroll
          for (int e = 0; e < 16; ++e) z[e] = 0.f;
          acc[t & 1] = mfma32(W1[rot], xf[n % PD], z);
        } else acc[t & 1] = mfma32(W1[(i + rot) % KS1], xf[n % PD], acc[t & 1]);
        asm volatile("" : "+v"(acc[t & 1]));
        SBAR();
        if constexpr (i + PD < KS1) xf[n % PD] = xfrag(t, (i + PD + rot) % KS1);
        else if constexpr (t < 3) xf[n % PD] = xfrag(t + 1, i + PD - KS1);
        if constexpr (t == 2 && i == 0) { wait_vm<5 + (ly == 0 ? 6 : 0)>(); h_flag(0); }
        if constexpr (t == 2 && i == 24) { wait_vm<(ly == 0 ? 6 : 1)>(); h_flag(1); }
        if constexpr (t == 3 && i == 23) { wait_vm<6>(); h_flag(2); }
        if constexpr (PR == 2 && t > 0 && i == 0) __builtin_amdgcn_s_setprio(1);
        if constexpr (PR == 2 && t > 0 && i == H_ST + 1) __builtin_amdgcn_s_setprio(0);
        if constexpr (PR == 3 && (i % 4) == 0) { if (((i >> 2) & 1) == (w >> 2)) __builtin_amdgcn_s_setprio(1); else __builtin_amdgcn_s_setprio(0); }
        if constexpr (t > 0) {
          if constexpr (i >= 3 && i <= 9 && (i & 1)) { ho[(i - 3) / 2] = pk2(hv[0], hv[1]); asm volatile("" : "+v"(ho[(i - 3) / 2])); }
          if constexpr (i >= 1 && i <= 8) ep_one(acc[(t - 1) & 1], i - 1);
          if constexpr (i == H_ST) h_store(t - 1);
        }
        if constexpr (t < 3) {
          const unsigned char* xb = sm + L_XB + ((t + 1) & 1) * XBUF + sj * 4096 + l16;
          constexpr int c0s = t ? XC : XC0, c1s = t ? XC + 3 : XC0 + 3, c2s = t ? XC + 4 : XC0 + 1, c3s = t ? XC + 7 : XC0 + 4, c4s = c3s + 1;
          if constexpr (i == c0s) {
            asm volatile("" : "+v"(sst) :: "memory");
            cb0 = *(const v4u*)xb; cb1 = *(const v4u*)(xb + 1024);
          }
          if constexpr (i == c1s) {
            cmx = umax3(umax3(cb0[0], cb0[1], cb0[2]), umax3(cb0[3], cb1[0], cb1[1]), umax3(cb1[2], cb1[3], cmx));
            asm volatile("" : "+v"(cmx));
          }
          if constexpr (i == c2s) {
            cb2 = *(const v4u*)(xb + 2048); cb3 = *(const v4u*)(xb + 3072);
            cmx = sst[1] != (hh ? tagv[1] : tagv[0]) ? EMPTY : cmx;
            asm volatile("" : "+v"(cmx));
          }
          if constexpr (i == c3s) {
            cmx = umax3(umax3(cb2[0], cb2[1], cb2[2]), umax3(cb2[3], cb3[0], cb3[1]), umax3(cb3[2], cb3[3], cmx));
            asm volatile("" : "+v"(cmx));
          }
          if constexpr (i == c4s) {
            if (__builtin_expect(!__all(cmx != EMPTY), 0)) x_fetch(t + 1);
            st_write(t + 1);
          }
        }
        if constexpr (ly == 0 && t == 0 && i == 4)
          st128<SC1>(v4u{EMPTY, EMPTY, EMPTY, EMPTY}, rs, l16, OFF_X + ACT_BYTES + ((Ta + (w >> 2)) * 7 + js) * 4096 + (w & 3) * 1024);
        if constexpr (ly == 0 && t == 0 && NPRE2 + i < KS1) ld_wgt(NPRE2 + i);
        if constexpr (ly == 0 && t == 0) {
          auto fw2_poll = [&]() AI { const int ln = opaque(lane); return ld64<SC1>(rs, (((ln & 8) ? ksl : kse) + min(ln & 7, 6)) * 128, OFF_FW2 + js * KS2 * 128); };
          if constexpr (i == 6) hpl = fw2_poll();
          if constexpr (i == 17) { SPIN(flag_ok(hpl), hpl = fw2_poll()); ORDER(); }
        }
        if constexpr (ly == 0 && t == 0 && i >= 1 && i <= 4) {
          if constexpr (i == 1) csum = 0.f;
#pragma unroll
          for (int q7 = (i - 1) * 4; q7 < i * 4 && q7 < 14; ++q7) csum += asf(csp[q7]);
          asm volatile("" : "+v"(csum));
        }
        if constexpr (ly == 0 && t == 0 && i == 5) { csum = half_sum(csum); asm volatile("" : "+v"(csum)); }
        if constexpr (ly == 0 && t == 0 && i == 8) cs_write();
        if constexpr (ly == 0 && (t == 1 || t == 2) && (i == 2 || i == 6 || i == 12)) ld_wgt(KS1 + (t - 1) * 4 + i / 5);
        if constexpr (ly == 0 && (t == 1 || t == 2) && i == 21) ld_wgt(KS1 + (t - 1) * 4 + 3);
        if constexpr (ly == 0 && t == 2 && i >= 24 && i < 27) ld_wgt(KS1 + 8 + i - 24);
        if constexpr (ly == 0 && t == 0 && (i == 17 || i == 20)) {}
        if constexpr (wl_slot(ly, t, i) >= 0) {
          constexpr int qn = wl_slot(ly, t, i), f = NWS + qn;
          dma1(rs, (ABL & 1) ? OOB : l16, wd_src(f), lds0 + L_WD + (w * NWL + qn) * 1024);
        }
        if constexpr (ly == 1 && t == 0) {
          constexpr int st = i - X3S;
          if constexpr (st == 0) SPIN(tag_ok(sgr), sgr = sp_load(2, Ta + 1));
          if constexpr (st == 1) { ga = i16 < 7 ? asf(sgr[0]) : 0.f; gb = i16 < 7 ? asf(sgr[2]) : 0.f; }
          if constexpr (st == 2 || st == 9) { ga = dpp_add<0x128>(ga); gb = dpp_add<0x128>(gb); PIN2(ga, gb); ga = dpp_add<0x124>(ga); gb = dpp_add<0x124>(gb); }
          if constexpr (st == 3 || st == 10) { ga = dpp_add<0x122>(ga); gb = dpp_add<0x122>(gb); PIN2(ga, gb); ga = dpp_add<0x121>(ga); gb = dpp_add<0x121>(gb); }
          if constexpr (st == 4) { float m, r; row_stats(ga, gb, m, r); ga = r; gb = -m * r; }
          if constexpr (st == 5 || st == 6) {
            constexpr int e = st - 5;
            const float xl = bflo(xres[1][e]), xh = bfhi(xres[1][e]);
            float ol = fmaf(fmaf(xl, ga, gb), bflo(yres[1][e]), xl), oh = fmaf(fmaf(xh, ga, gb), bfhi(yres[1][e]), xh);
            PIN2(ol, oh);
            unsigned o = nz(pk2(ol, oh));
            asm volatile("" : "+v"(o));
            xres[1][e] = o;
          }
          if constexpr (st == 7) st64<SC1>(xres[1], rs, (lane * 8) ^ (((2 * w + hh) & 7) << 4), OFF_X + ACT_BYTES + ((Ta + 1) * 7 + js) * 4096 + w * 512);
          if constexpr (st == 8) { ga = dot2(xres[1][0], ONES, dot2(xres[1][1], ONES, 0.f)); gb = dot2(xres[1][0], xres[1][0], dot2(xres[1][1], xres[1][1], 0.f)); }
          if constexpr (st == 11) st128<SC1>(v4u{asu(ga), tagv[0], asu(gb), tagv[1]}, rs, (lane & 15) == 0 ? lane : OOB, OFF_SP + 3 * SP_BYTES + ((Ta + 1) * 7 + js) * 512 + w * 64);
          if constexpr (st >= 1 && st <= 10) PIN2(ga, gb);
          if constexpr (i == BAR) asm volatile("" : "+v"(W1[NDROP - 1]));
        }
        if constexpr (ly == 1 && t == 0 && i >= 3 && i < 15 && (i & 1)) W1[8 + (i - 3) / 2] = ldfrag<SC1>(rs, l16, w1o, 8 + (i - 3) / 2);
        if constexpr (t < 2 && (i == BAR + 1 || i == BAR + 3)) x_dma(t + 2, (i - BAR - 1) / 2);
        if constexpr (t == 2 && i == 15) hpl = hf_poll(0, 0);
        if constexpr (t == 3) {
          if constexpr (i == 0) hf_check(0, 0, hpl);
          if constexpr (i == 7) hpl = hf_poll(0, 1);
          if constexpr (i == 16) hf_check(0, 1, hpl);
          if constexpr (i < 14 && !(i & 1)) h_load(0, i / 2);
          if constexpr (i >= 16 && !(i & 1)) h_load(0, 7 + (i - 16) / 2);
        }
        if constexpr (i == 11) {
          const unsigned char* sp = sm + L_ST + (t % 3) * 2048 + tokl * 16;
          p0 = *(const f32x4*)sp; p1 = *(const f32x4*)(sp + 512);
        }
        if constexpr (i == 13) { p0[0] += p0[1]; p0[2] += p0[3]; p1[0] += p1[1]; asm volatile("" : "+v"(p0[0]), "+v"(p0[2]), "+v"(p1[0])); }
        if constexpr (i == 15) { mean = ((p0[0] + p0[2]) + (p1[0] + p1[2])) * (-1.0f / D); asm volatile("" : "+v"(mean)); }
        if constexpr (!(t & 1)) {
          if constexpr (i == 16) { const unsigned char* sp = sm + L_ST + (t % 3) * 2048 + tokl * 16; p2 = *(const f32x4*)(sp + 1024); p3 = *(const f32x4*)(sp + 1536); }
          if constexpr (i == 19) { p2[0] += p2[1]; p2[2] += p2[3]; p3[0] += p3[1]; asm volatile("" : "+v"(p2[0]), "+v"(p2[2]), "+v"(p3[0])); }
          if constexpr (i == 20) { sq = (p2[0] + p2[2]) + (p3[0] + p3[2]); asm volatile("" : "+v"(sq)); }
          if constexpr (i == 26) { sq = __builtin_amdgcn_rcpf(fmaxf(fmaf(sq, 1.0f / D, -mean * mean), 0.0f) + EPS); asm volatile("" : "+v"(sq)); }
          if constexpr (i == 27) r2[t >> 1] = asf(__builtin_amdgcn_ds_bpermute(w * 16 + (lane >> 4) * 4, asu(sq)));
        }
        if constexpr (i == 25) ep_cs(0);
        if constexpr (t > 0 && i == 2) ep_cs(1);
        if constexpr (t == 3 && i == 27) ep_cs(1);
        if constexpr ((XB & 1) && t == 3 && i == BAR) raw_barrier();
        if constexpr ((XB & 2) && t == 3 && i == 11) raw_barrier();
        if constexpr ((XB & 4) && t < 3 && (t > 0 || (XB & 16)) && i == 11) raw_barrier();
        if constexpr (i == BAR && t < 3) {
          asm volatile("s_waitcnt lgkmcnt(1)" ::: "memory");
          raw_barrier();
        }
        SBAR();
      });
      STAMP(1, 8 + t + ly * 24);
      if (ly == 1 && t == 2) STAMPC(1, 63);
    });
    v4u sga;
    auto unit = [&](auto UU) AI {
      constexpr int u = UU.value;
      v4u wl[3];
      int wad = 0, rad = 0;
      auto red_write = [&](int jq) AI {
        const f32x16& a = acc[jq >> 2];
        const int qd = jq & 3;
        *(f32x4*)(sm + L_RED + (wad ^ ((4 * (jq >> 1) + (jq & 1)) << 4)) + jq * 1024) = f32x4{a[4 * qd], a[4 * qd + 1], a[4 * qd + 2], a[4 * qd + 3]};
      };
      static_for<0, 28>([&](auto FF) AI {
        constexpr int f = FF.value, ct = (f / 7) & 1, hi = f % 7 + 7 * (f / 14);
        v4u wf;
        if constexpr (f < NWS) wf = Wd[f]; else if constexpr (f < NWS + NWL) wf = wl[f % 3]; else wf = Wd[f - NWL];
        if constexpr (f == 0 || f == 7) {
          f32x16 z;
#pragma unroll
          for (int e = 0; e < 16; ++e) z[e] = 0.f;
          acc[ct] = mfma32(wf, hf[hi], z);
        } else acc[ct] = mfma32(wf, hf[hi], acc[ct]);
        asm volatile("" : "+v"(acc[ct]));
        SBAR();
        if constexpr (f + 3 >= NWS && f + 3 < NWS + NWL) wl[f % 3] = *(const v4u*)(sm + L_WD + (w * NWL + (f + 3 - NWS)) * 1024 + l16);
        if constexpr (u == 0) {
          if constexpr (f < 6) {
            constexpr int eb[7] = {0, 1, 3, 4, 6, 7, 8};
#pragma unroll
            for (int e = eb[f]; e < eb[f + 1]; ++e) { ep_one(acc[1], e); if (e & 1) ho[e >> 1] = pk2(hv[0], hv[1]); }
          }
          if constexpr (f == 6) h_store(3);
          if constexpr (f == 11) hpl = hf_poll(1, 0);
          if constexpr (f == 18) { wait_vm<1>(); h_flag(3); }
          if constexpr (f == 19) hf_check(1, 0, hpl);
          if constexpr (f == 0) h_load(0, 13);
          constexpr int k0 = ly == 0 ? 0 : KT;
          if constexpr (f >= 2 && f < 2 + 2 * (NWR - NWS - k0) && !(f & 1)) Wd[NWS + k0 + (f - 2) / 2] = ld_wd(NWL + NWS + k0 + (f - 2) / 2);
          if constexpr (f >= 19 && (f & 1)) h_load(1, (f - 19) / 2);
        }
        if constexpr (u == 1 && pace(f) >= 0 && pace(f) < 3) h_load(1, 11 + pace(f));
        if constexpr (u == 1 && ly == 0) {
          if constexpr (pace(f) >= 3) W1[pace(f) - 3] = ldfrag<SC1>(rs, l16, w1o, pace(f) - 3);
          if constexpr (f == SGA) sga = sp_load(2, Ta);
        }
        if constexpr (f == 22) { const int ln = opaque(lane); wad = opaque(w * 8192 + ((ln ^ ((ln >> 5) << 1)) << 4)); }
        if constexpr (f == 27) {
          const int ln = opaque(lane), li16 = ln & 15, ftok = 4 * w + (ln >> 4);
          rad = opaque((2 * (li16 >> 2) + (li16 & 1)) * 1024 + (((li16 >> 1) & 1) * 32 + (ftok ^ li16)) * 16);
        }
        if constexpr (f >= 23 && f < 27) red_write(f - 23);
        if constexpr ((XB & 8) && f == 17) raw_barrier();
        if constexpr (f == GB) __syncthreads();
        SBAR();
      });
      STAMP(1, 13 + u * 5 + ly * 24);
      if constexpr (u == 1 && ly == 0) { x3_publish(0, sga); SBAR(); STAMP(1, 46); }
#pragma unroll
      for (int jq = 4; jq < 8; ++jq) red_write(jq);
      SBAR();
      if constexpr (u == 0) { h_load(1, 5); h_load(1, 6); hpl = hf_poll(1, 1); }
      SBAR();
      STAMP(1, 14 + u * 5 + ly * 24);
      __syncthreads();
      STAMP(1, 15 + u * 5 + ly * 24);
      if constexpr (u == 1 && ly == 0) x0_load();
      {
        f32x4 pr[8];
#pragma unroll
        for (int ww = 0; ww < 8; ++ww) pr[ww] = *(const f32x4*)(sm + L_RED + rad + ww * 8192);
        SBAR();
        const f32x4 a = ((pr[0] + pr[1]) + (pr[2] + pr[3])) + ((pr[4] + pr[5]) + (pr[6] + pr[7]));
        const unsigned f01 = pk2(a[0] * r2[u], a[1] * r2[u]), f23 = pk2(a[2] * r2[u], a[3] * r2[u]);
        const v2u xn = {pk2(bflo(xres[u][0]) + bflo(f01), bfhi(xres[u][0]) + bfhi(f01)), pk2(bflo(xres[u][1]) + bflo(f23), bfhi(xres[u][1]) + bfhi(f23))};
        if constexpr (ly == 1) {
          const int ln = opaque(lane);
          st64<OAUX>(xn, mk_rsrc(out, ACT_BYTES), (ln >> 4) * (D * 2) + (ln & 15) * 8, (((Ta + u) * 32 + 4 * w) * D + js * 64) * 2);
        } else {
          xres[u] = xn;
          sp_publish(2, u);
        }
      }
      SBAR();
      if constexpr (u == 0) {
        hf_check(1, 1, hpl);
#pragma unroll
        for (int i = 7; i < 11; ++i) h_load(1, i);
        SBAR();
      }
      STAMP(1, 16 + u * 5 + ly * 24);
    };
    unit(IC<0>{});
    unit(IC<1>{});
  };
  layer(IC<0>{});
  layer(IC<1>{});
  st64<SC1>(v2u{sopq(tag0), sopq(tag1)}, rs, (bid == 0 && tid == 0) ? 0 : OOB, OFF_EP);
  STAMP(1, 60);
}

extern "C" void kernel_launch(void* const* d_in, const int* in_sizes, int n_in, void* d_out, int out_size, void* d_ws,
                              size_t ws_size, hipStream_t stream) {
  const uint16_t* q = (const uint16_t*)d_in[0];
  const uint16_t* k = (const uint16_t*)d_in[1];
  const uint16_t* v = (const uint16_t*)d_in[2];
  const uint16_t* shk = (const uint16_t*)d_in[3];
  const uint16_t* ssk = (const uint16_t*)d_in[4];
  const uint16_t* ssd = (const uint16_t*)d_in[5];
  const uint16_t* wg = (const uint16_t*)d_in[7];
  const uint16_t* wu = (const uint16_t*)d_in[8];
  const uint16_t* wd = (const uint16_t*)d_in[9];
  unsigned char* ws = (unsigned char*)d_ws;
  uint16_t* out = (uint16_t*)d_out;
  (void)hipFuncSetAttribute((const void*)k_main, hipFuncAttributeMaxDynamicSharedMemorySize, L_BYTES);
  k_main<<<NAUX + 224, 512, L_BYTES, stream>>>(q, k, v, shk, ssk, ssd, wg, wu, wd, ws, out);
}
```

```cpp
#include <hip/hip_runtime.h>
#include <hip/hip_bf16.h>
#include <stdint.h>

#define DI __device__ __forceinline__
#define AI __attribute__((always_inline))
#define LAS __attribute__((address_space(3)))
typedef unsigned v4u __attribute__((ext_vector_type(4)));
typedef unsigned v2u __attribute__((ext_vector_type(2)));
typedef float f32x4 __attribute__((ext_vector_type(4)));
typedef float f32x2 __attribute__((ext_vector_type(2)));
typedef float f32x16 __attribute__((ext_vector_type(16)));
typedef short bf16x8 __attribute__((ext_vector_type(8)));
typedef short s16x4 __attribute__((ext_vector_type(4)));
typedef __bf16 bf2 __attribute__((ext_vector_type(2)));
typedef __amdgpu_buffer_rsrc_t rsrc_t;

constexpr int S = 2048, D = 448, F = 1792;
constexpr int KS1 = D / 16;
constexpr int KS2 = F / 16;
constexpr float EPS = 1e-5f;

constexpr int ACT_BYTES = S * D * 2;
constexpr int FSTR = 32;
constexpr int OFF_EP = 0;
constexpr int OFF_FW1 = 256;
constexpr int OFF_FW2 = OFF_FW1 + 28 * 32 * 128;
constexpr int OFF_FH = OFF_FW2 + 7 * KS2 * 128;
constexpr int OFF_SP = OFF_FH + 2 * 64 * KS2 * FSTR;
constexpr int SP_BYTES = 64 * 7 * 32 * 16;
constexpr int OFF_KVG = OFF_SP + 4 * SP_BYTES;
constexpr int OFF_X = OFF_KVG + 2 * ACT_BYTES;
constexpr int OFF_H = OFF_X + 2 * ACT_BYTES;
constexpr int H_BYTES = 64 * KS2 * 1024;
constexpr int OFF_W1 = OFF_H + 2 * H_BYTES;
constexpr int OFF_W2 = OFF_W1 + 112 * KS1 * 1024;
constexpr int OFF_CSP = OFF_W2 + 14 * KS2 * 1024;
constexpr int WS_BYTES = OFF_CSP + 28 * 112 * 32 * 4;
static_assert(OFF_SP % 128 == 0 && OFF_KVG % 128 == 0, "alignment");

constexpr int SC1 = 16;
DI rsrc_t mk_rsrc(const void* p, int bytes) { return __builtin_amdgcn_make_buffer_rsrc((void*)p, 0, bytes, 0x00020000); }
template <int AUX = 0> DI v4u ld128(rsrc_t r, int voff, int soff) { return __builtin_amdgcn_raw_buffer_load_b128(r, voff, soff, AUX); }
template <int AUX = 0> DI v2u ld64(rsrc_t r, int voff, int soff) { return __builtin_amdgcn_raw_buffer_load_b64(r, voff, soff, AUX); }
template <int AUX = 0> DI unsigned ld32(rsrc_t r, int voff, int soff) { return __builtin_amdgcn_raw_buffer_load_b32(r, voff, soff, AUX); }
template <int AUX = 0> DI void st128(v4u v, rsrc_t r, int voff, int soff) {
  __builtin_amdgcn_raw_buffer_store_b128(v, r, voff, soff, AUX);
  asm volatile("s_nop 1" ::"v"(v));
}
template <int AUX = 0> DI void st64(v2u v, rsrc_t r, int voff, int soff) { __builtin_amdgcn_raw_buffer_store_b64(v, r, voff, soff, AUX); }
template <int AUX = 0> DI void st32(unsigned v, rsrc_t r, int voff, int soff) { __builtin_amdgcn_raw_buffer_store_b32(v, r, voff, soff, AUX); }
DI float asf(unsigned u) { return __builtin_bit_cast(float, u); }
DI unsigned asu(float f) { return __builtin_bit_cast(unsigned, f); }
DI float bflo(unsigned u) { return asf(u << 16); }
DI float bfhi(unsigned u) { return asf(u & 0xffff0000u); }
DI unsigned pk2(float a, float b) {
  f32x2 v = {a, b};
  return __builtin_bit_cast(unsigned, __builtin_convertvector(v, bf2));
}
DI float dot2(unsigned a, unsigned b, float c) {
  return __builtin_amdgcn_fdot2_f32_bf16(__builtin_bit_cast(bf2, a), __builtin_bit_cast(bf2, b), c, false);
}
DI f32x16 mfma32(v4u a, v4u b, f32x16 c) {
  return __builtin_amdgcn_mfma_f32_32x32x16_bf16(__builtin_bit_cast(bf16x8, a), __builtin_bit_cast(bf16x8, b), c, 0, 0, 0);
}
DI f32x4 mfma16(v4u a, v4u b, f32x4 c) {
  return __builtin_amdgcn_mfma_f32_16x16x32_bf16(__builtin_bit_cast(bf16x8, a), __builtin_bit_cast(bf16x8, b), c, 0, 0, 0);
}
DI v4u tr_frag(LAS unsigned char* p, int second) {
  const v2u l2 = __builtin_bit_cast(v2u, __builtin_amdgcn_ds_read_tr16_b64_v4i16((LAS s16x4*)p));
  const v2u h2 = __builtin_bit_cast(v2u, __builtin_amdgcn_ds_read_tr16_b64_v4i16((LAS s16x4*)(p + second)));
  return v4u{l2[0], l2[1], h2[0], h2[1]};
}
template <int CTRL> DI float dpp_add(float v) {
  return v + asf(__builtin_amdgcn_update_dpp(0, asu(v), CTRL, 0xf, 0xf, true));
}
DI float row16_sum(float v) {
  v = dpp_add<0x128>(v); v = dpp_add<0x124>(v); v = dpp_add<0x122>(v); v = dpp_add<0x121>(v);
  return v;
}
#define PIN2(a, b) asm volatile("" : "+v"(a), "+v"(b))
#define SBAR() __builtin_amdgcn_sched_barrier(0)
#define DRAIN() asm volatile("s_waitcnt vmcnt(0)" ::: "memory")
#define ORDER() __builtin_amdgcn_fence(__ATOMIC_ACQUIRE, "wavefront")
constexpr unsigned ONES = 0x3f803f80u;
constexpr unsigned EMPTY = 0xffffffffu;
constexpr unsigned SPIN_CAP = 1u << 16;

DI void row_stats(float s, float ss, float& m, float& r) {
  m = s * (1.0f / D);
  const float var = fmaxf(fmaf(ss, 1.0f / D, -m * m), 0.0f);
  r = __builtin_amdgcn_rsqf(var + EPS);
}
constexpr int NWL = 12;
constexpr int NWR = 28 - NWL;
constexpr int NDROP = 14;
constexpr int XBUF = D * 64;
constexpr int L_XB = 0, L_ST = 2 * XBUF, L_CS = L_ST + 3 * 2048, L_RED = 0, L_WD = 65536, L_BYTES = L_WD + NWL * 8 * 1024;
static_assert(L_CS + 8 * 128 <= L_WD && L_BYTES <= 163840, "LDS map");
constexpr int CV_HSTR = 4160, CV_USTR = 2304 * 2;
constexpr int CV_H = 0, CV_U = CV_H + 2 * 4 * CV_HSTR, CV_RED = L_WD, CV_W = 43008;
static_assert(CV_U + 2 * CV_USTR <= CV_W && CV_W + 18432 <= L_WD && CV_RED + 65536 <= L_BYTES, "conv LDS map");

template <int V> struct IC { static constexpr int value = V; };
template <int I, int N, class Fn> DI void static_for(Fn&& fn) {
  if constexpr (I < N) { fn(IC<I>{}); static_for<I + 1, N>(fn); }
}
template <int N> DI void wait_vm() { asm volatile("s_waitcnt vmcnt(%0)" ::"n"(N) : "memory"); }
DI float half_sum(float v) {
  const auto r = __builtin_amdgcn_permlane32_swap(asu(v), asu(v), false, false);
  return asf(r[0]) + asf(r[1]);
}
DI unsigned umax3(unsigned a, unsigned b, unsigned c) { return max(max(a, b), c); }
constexpr int OOB = (int)0x80000000;
DI void raw_barrier() { asm volatile("" ::: "memory"); __builtin_amdgcn_s_barrier(); asm volatile("" ::: "memory"); }
template <int AUX = 0> DI v4u ldfrag(rsrc_t r, int l16, int base, int i) { return ld128<AUX>(r, l16, base + i * 1024); }
template <int O0> DI void dma2(rsrc_t r, int l16, int soff, unsigned lds) {
  asm volatile("s_mov_b32 m0, %0\n\ts_nop 0\n\tbuffer_load_dwordx4 %1, %2, %3 offen offset:%4 sc1 lds\n\tbuffer_load_dwordx4 %1, %2, %3 offen offset:%5 sc1 lds"
               ::"s"(lds), "v"(l16), "s"(r), "s"(soff), "n"(O0), "n"(O0 + 1024) : "m0", "memory");
}
DI void dma1(rsrc_t r, int l16, int soff, unsigned lds) {
  asm volatile("s_mov_b32 m0, %0\n\ts_nop 0\n\tbuffer_load_dwordx4 %1, %2, %3 offen sc1 lds" ::"s"(lds), "v"(l16), "s"(r), "s"(soff) : "m0", "memory");
}
DI int opaque(int v) { asm volatile("" : "+v"(v)); return v; }
DI unsigned sopq(unsigned v) { asm volatile("" : "+s"(v)); return v; }
DI unsigned nz(unsigned v) { return min(v, EMPTY - 1u); }

constexpr int NWS = 11;
constexpr int KT = 1;
constexpr int PD = 3;
constexpr int FWC = 5, NPRE = 18, NPRE2 = 24;
constexpr int XC0 = 18, X3S = 2, SGA = 17, GB = 6;
constexpr int BAR = KS1 - 1 - PD;
constexpr int H_ST = 10;
constexpr int XC = 14;
constexpr int pace(int i) {
  constexpr int sl[11] = {0, 3, 5, 8, 11, 13, 16, 19, 21, 24, 26};
  for (int n = 0; n < 11; ++n) if (sl[n] == i) return n;
  return -1;
}
constexpr int wl_slot(int ly, int t, int i) {
  return (ly != 0 || t == 3) ? -1 : t == 0 ? (i == 19 ? 0 : i == 21 ? 1 : i == 23 ? 2 : i == 26 ? 3 : -1) : i == 16 ? t * 4 : i == 19 ? t * 4 + 1 : i == 23 ? t * 4 + 2 : i == 27 ? t * 4 + 3 : -1;
}

#define SPIN(cond_ok, reload) do { if (__builtin_expect(!(cond_ok), 0)) { unsigned spins_ = 0; \
  _Pragma("unroll 1") do { asm volatile("" ::: "memory"); reload; } while (!(cond_ok) && ++spins_ < SPIN_CAP); } } while (0)
DI void mk_tags(v2u ep, unsigned& t0, unsigned& t1) {
  const unsigned e0 = __builtin_amdgcn_readfirstlane(ep[0]), e1 = __builtin_amdgcn_readfirstlane(ep[1]);
  t0 = e0 + 1u;
  if (t0 == e1 || t0 == EMPTY) ++t0;
  if (t0 == e1 || t0 == EMPTY) ++t0;
  t1 = e1 + 0x9e3779b9u;
#pragma unroll
  for (int i = 0; i < 3; ++i)
    if (t1 == t0 || t1 == e0 || t1 == EMPTY) t1 += 0x10001u;
}
constexpr int NAUX = 32, CODE_KB = 32;

__global__ void __launch_bounds__(512, 2) k_main(const uint16_t* __restrict__ q, const uint16_t* __restrict__ kin,
                                                 const uint16_t* __restrict__ vin, const uint16_t* __restrict__ shk,
                                                 const uint16_t* __restrict__ ssk, const uint16_t* __restrict__ ssd,
                                                 const uint16_t* __restrict__ wg, const uint16_t* __restrict__ wu,
                                                 const uint16_t* __restrict__ wdn, unsigned char* __restrict__ ws,
                                                 uint16_t* __restrict__ out) {
  extern __shared__ __attribute__((aligned(16))) unsigned char sm[];
  LAS unsigned char* L = (LAS unsigned char*)sm;
  const int tid = threadIdx.x, lane = tid & 63;
  int w_ = __builtin_amdgcn_readfirstlane(tid >> 6);
  asm volatile("" : "+s"(w_));
  const int w = w_;
  const rsrc_t rs = mk_rsrc(ws, WS_BYTES);
  const int l16 = lane * 16, tokl = lane & 31, hh = lane >> 5, i16 = lane & 15;
  unsigned long long pc0;
  asm volatile("s_getpc_b64 %0" : "=s"(pc0));
  const v2u ep = ld64<SC1>(rs, 0, OFF_EP);

  const int tr_off = (hh * 8 + (i16 >> 2)) * 144 + (8 * (i16 & 1) + 4 * ((i16 >> 1) & 1)) * 2;

  if (blockIdx.x < NAUX) {
    const int r = tid >> 3, qq = tid & 7;
    v4u a[4][2];
    {
      unsigned t = 0;
#pragma unroll
      for (int i = 0; i < CODE_KB / 8; ++i) t ^= ld32(mk_rsrc((const void*)pc0, CODE_KB * 1024), tid * 16, i * 8192);
      asm volatile("" ::"v"(t));
    }
    for (int i = 0; i < 2; ++i) __builtin_amdgcn_s_sleep(36);
#pragma unroll
    for (int n = 0; n < 4; ++n) {
      const int b = min((int)blockIdx.x + 32 * n, 97);
      const size_t src = (size_t)((b / 7) * 128 + r) * D + (b % 7) * 64 + qq * 8;
      a[n][0] = *(const v4u*)(wdn + src); a[n][1] = *(const v4u*)(wdn + src + 64 * D);
    }
#pragma unroll
    for (int n = 0; n < 4; ++n) {
      *(v4u*)(sm + n * 18432 + r * 144 + qq * 16) = a[n][0];
      *(v4u*)(sm + n * 18432 + (64 + r) * 144 + qq * 16) = a[n][1];
    }
    __syncthreads();
    unsigned tg0, tg1;
    mk_tags(ep, tg0, tg1);
    const v2u tg = {tg0, tg1};
#pragma unroll
    for (int n = 0; n < 4; ++n) {
      const int b = blockIdx.x + 32 * n, kb = b / 7, cb = b % 7;
#pragma unroll
      for (int ct = 0; ct < 2; ++ct) {
        const v4u o = tr_frag(L + n * 18432 + w * 16 * 144 + tr_off + (ct * 32 + ((lane >> 4) & 1) * 16) * 2, 4 * 144);
        st128<SC1>(o, rs, b < 98 ? l16 : OOB, OFF_W2 + ((cb * 2 + ct) * KS2 + kb * 8 + w) * 1024);
      }
    }
    DRAIN();
    {
      const int b = blockIdx.x + 32 * (lane >> 4);
      st64<SC1>(tg, rs, b < 98 ? ((b % 7) * KS2 + (b / 7) * 8 + w) * 128 + i16 * 8 : OOB, OFF_FW2);
    }
    return;
  }

  const int bid = blockIdx.x - NAUX;
  const int xcd = bid & 7, li = bid >> 3;
  const int g = (xcd >> 1) * 4 + li / 7, hs = xcd & 1, js = li % 7, j = hs * 7 + js;
  const int fg = j * 8 + w;
  const int sj = w < 7 ? w : 6;
  const int w1o = OFF_W1 + fg * KS1 * 1024;
  const int Ta = g * 4 + hs * 2;
  const unsigned lds0 = (unsigned)(size_t)L;

  v4u W1[KS1], Wd[NWR];
  float csum;
  unsigned csp[14];
  v2u xres[2], yres[2];
  unsigned tag0, tag1;
  const int kse = hs * 56 + w * 7, ksl = (hs ^ 1) * 56 + w * 7;
  auto wd_src = [&](int f) AI { return OFF_W2 + ((js * 2 + ((f / 7) & 1)) * KS2 + (f < 14 ? kse : ksl) + f % 7) * 1024; };
  auto ld_wd = [&](int f) AI { return ld128<SC1>(rs, l16, wd_src(f)); };
  auto ld_wgt = [&](int n) AI {
    if (n < KS1) W1[n] = ldfrag<SC1>(rs, l16, w1o, n);
    else if (n < KS1 + NWS) Wd[n - KS1] = ld_wd(n - KS1);
  };
  auto tag_ok = [&](v4u v) AI { return __all(((v[1] ^ sopq(tag0)) | (v[3] ^ sopq(tag1))) == 0u); };
  auto flag_ok = [&](v2u v) AI { return __all(((v[0] ^ sopq(tag0)) | (v[1] ^ sopq(tag1))) == 0u); };
  auto sp_publish = [&](int which, int u) AI {
    const int ln = opaque(lane);
    const v2u xn = xres[u];
    const float s = row16_sum(dot2(xn[0], ONES, dot2(xn[1], ONES, 0.f))), ss = row16_sum(dot2(xn[0], xn[0], dot2(xn[1], xn[1], 0.f)));
    st128<SC1>(v4u{asu(s), sopq(tag0), asu(ss), sopq(tag1)}, rs, (ln & 15) == 0 ? ln : OOB, OFF_SP + which * SP_BYTES + ((Ta + u) * 7 + js) * 512 + w * 64);
  };
  auto sp_load = [&](int which, int T) AI {
    const int ln = opaque(lane);
    return ld128<SC1>(rs, ((ln & 15) < 7 ? (ln & 15) * 512 : 0) + (ln & 48), OFF_SP + which * SP_BYTES + T * 7 * 512 + w * 64);
  };
  auto sp_stats = [&](int which, int u, v4u sp, float& m, float& r) AI {
    SPIN(tag_ok(sp), sp = sp_load(which, Ta + u));
    row_stats(row16_sum(i16 < 7 ? asf(sp[0]) : 0.f), row16_sum(i16 < 7 ? asf(sp[2]) : 0.f), m, r);
  };
  auto gate_publish = [&](int ly, int u, float r, float c) AI {
    v2u xn;
#pragma unroll
    for (int e = 0; e < 2; ++e) {
      const float xl = bflo(xres[u][e]), xh = bfhi(xres[u][e]);
      xn[e] = nz(pk2(fmaf(fmaf(xl, r, c), bflo(yres[u][e]), xl), fmaf(fmaf(xh, r, c), bfhi(yres[u][e]), xh)));
    }
    xres[u] = xn;
    const int ln = opaque(lane);
    st64<SC1>(xn, rs, (ln * 8) ^ (((2 * w + (ln >> 5)) & 7) << 4), OFF_X + ly * ACT_BYTES + ((Ta + u) * 7 + js) * 4096 + w * 512);
    sp_publish(2 * ly + 1, u);
  };
  auto x3_publish = [&](int u, v4u sp) AI {
    float m, r;
    sp_stats(2, u, sp, m, r);
    gate_publish(1, u, r, -m * r);
  };

  {
    const int c0 = bid * 2;
    const int r = tid >> 3, qq = tid & 7;
    const int tok = Ta * 32 + r, ca = js * 64 + qq * 8;
    const rsrc_t rk = mk_rsrc(kin, ACT_BYTES), rh = mk_rsrc(ssk, ACT_BYTES);
    v4u kk[4], sw[4];
    const v4u swl = *(const v4u*)(shk + js * 256 + tokl * 8);
#pragma unroll
    for (int i = 0; i < 4; ++i) kk[i] = ld128(rk, ((tok - i) * D + ca) * 2, 0);
    const v4u vv = *(const v4u*)(vin + (size_t)tok * D + ca);
    const int ch = tid >> 8, i8 = tid & 255;
    const v4u d = ld128(rh, tid * 16, c0 * S * 2);
    const v2u e = ld64(rh, tid * 16 + 16 + c0 * S * 2, 0);
    SBAR();
    *(v4u*)(sm + CV_RED + 16384 + w * 1024 + l16) = swl;
    asm volatile("" ::: "memory");
#pragma unroll
    for (int i = 0; i < 4; ++i) sw[i] = *(const v4u*)(sm + CV_RED + 16384 + w * 1024 + qq * 64 + i * 16);
    mk_tags(ep, tag0, tag1);
    {
      uint16_t* tl = (uint16_t*)(sm + CV_RED) + qq * 8 * 64 + ((63 - r) ^ (qq << 3));
#pragma unroll
      for (int e2 = 0; e2 < 4; ++e2) {
        const unsigned wa01 = sw[e2][0], wa23 = sw[e2][1], wb01 = sw[e2][2], wb23 = sw[e2][3];
        unsigned p = pk2(bflo(kk[3][e2]) * bfhi(wa23), bfhi(kk[3][e2]) * bfhi(wb23));
        unsigned acc = p;
        p = pk2(bflo(kk[2][e2]) * bflo(wa23), bfhi(kk[2][e2]) * bflo(wb23));
        acc = pk2(bflo(acc) + bflo(p), bfhi(acc) + bfhi(p));
        p = pk2(bflo(kk[1][e2]) * bfhi(wa01), bfhi(kk[1][e2]) * bfhi(wb01));
        acc = pk2(bflo(acc) + bflo(p), bfhi(acc) + bfhi(p));
        p = pk2(bflo(kk[0][e2]) * bflo(wa01), bfhi(kk[0][e2]) * bflo(wb01));
        acc = pk2(bflo(acc) + bflo(p), bfhi(acc) + bfhi(p));
        const unsigned kv = pk2(bflo(acc) * bflo(vv[e2]), bfhi(acc) * bfhi(vv[e2]));
        tl[(2 * e2) * 64] = (uint16_t)kv;
        tl[(2 * e2 + 1) * 64] = (uint16_t)(kv >> 16);
      }
    }
    SBAR();
    const int wt = min(bid, 195), kb = wt / 28, fb = wt % 28, ks = kb * 4 + (w & 3), rl = lane >> 3;
    v4u wa[4];
    __syncthreads();
#pragma unroll
    for (int i = 0; i < 2; ++i) {
      const int cl = i * 32 + (tid >> 4);
      const v2u o = *(const v2u*)(sm + CV_RED + cl * 128 + ((i16 ^ ((cl >> 3) << 1)) * 8));
      st128<SC1>(v4u{o[0], sopq(tag0), o[1], sopq(tag1)}, rs, (tid >> 4) * 8192 + i16 * 16, OFF_KVG + (js * 64 + i * 32) * 8192 + (S - 64 - Ta * 32) * 4);
    }
    SBAR();
    {
      const unsigned a0 = __builtin_amdgcn_alignbit(d[1], d[0], 16), a1 = __builtin_amdgcn_alignbit(d[2], d[1], 16),
                     a2 = __builtin_amdgcn_alignbit(d[3], d[2], 16), a3 = __builtin_amdgcn_alignbit(e[0], d[3], 16),
                     a4 = __builtin_amdgcn_alignbit(e[1], e[0], 16);
      unsigned char* hb = sm + CV_H + ch * 4 * CV_HSTR + 32 + i8 * 16;
      *(v4u*)(hb) = d;
      *(v4u*)(hb + CV_HSTR) = v4u{a0, a1, a2, a3};
      *(v4u*)(hb + 2 * CV_HSTR) = v4u{d[1], d[2], d[3], e[0]};
      *(v4u*)(hb + 3 * CV_HSTR) = v4u{a1, a2, a3, a4};
      const unsigned d0 = __builtin_amdgcn_readfirstlane(d[0]), d1 = __builtin_amdgcn_readfirstlane(d[1]);
      if (i8 < 40) {
        const int q8 = i8 - 32, sg = q8 >> 1;
        v4u pv = {0u, 0u, 0u, 0u};
        if (q8 == 3) pv[3] = d0 << 16;
        if (q8 == 5) pv[3] = d0;
        if (q8 == 7) { pv[2] = d0 << 16; pv[3] = __builtin_amdgcn_alignbit(d1, d0, 16); }
        *(v4u*)(sm + (q8 < 0 ? CV_U + ch * CV_USTR + 4096 + i8 * 16 : CV_H + (ch * 4 + sg) * CV_HSTR + (q8 & 1) * 16)) = pv;
      }
    }
    float dcoef;
    auto own_load = [&]() AI {
#pragma unroll
      for (int u = 0; u < 2; ++u) xres[u] = *(const v2u*)(q + (size_t)((Ta + u) * 32 + 4 * w + (lane >> 4)) * D + js * 64 + i16 * 4);
      dcoef = bflo(ssd[c0 + ((lane >> 4) & 1)]);
    };
    if (w < 4) {
      v4u ug[4];
      auto u_load = [&]() AI {
#pragma unroll
        for (int i = 0; i < 4; ++i) ug[i] = ld128<SC1>(rs, tid * 16, OFF_KVG + c0 * 8192 + i * 4096);
      };
      __builtin_amdgcn_s_sleep(40);
      u_load();
      SPIN(tag_ok(ug[0]) && tag_ok(ug[1]) && tag_ok(ug[2]) && tag_ok(ug[3]), u_load());
#pragma unroll
      for (int i = 0; i < 4; ++i) *(v2u*)(sm + CV_U + (i >> 1) * CV_USTR + (i & 1) * 2048 + tid * 8) = v2u{ug[i][0], ug[i][2]};
      own_load();
    } else {
      {
        const rsrc_t rg = mk_rsrc(wg, D * F * 2), ru = mk_rsrc(wu, D * F * 2);
        const int vo = ((ks * 16 + rl) * F + fb * 64 + qq * 8) * 2;
        wa[0] = ld128(rg, vo, 0); wa[1] = ld128(rg, vo, 16 * F); wa[2] = ld128(ru, vo, 0); wa[3] = ld128(ru, vo, 16 * F);
      }
      own_load();
      unsigned char* wb = sm + CV_W + (w - 4) * (32 * 144) + rl * 144 + qq * 16;
      *(v4u*)wb = wa[0]; *(v4u*)(wb + 8 * 144) = wa[1]; *(v4u*)(wb + 16 * 144) = wa[2]; *(v4u*)(wb + 24 * 144) = wa[3];
      asm volatile("" ::: "memory");
#pragma unroll
      for (int fgl = 0; fgl < 4; ++fgl) {
        const v4u o = tr_frag(L + CV_W + (w - 4) * (32 * 144) + ((lane >> 4) & 1) * (16 * 144) + tr_off + fgl * 32, 4 * 144);
        st128<SC1>(o, rs, bid < 196 ? l16 : OOB, OFF_W1 + ((fb * 4 + fgl) * KS1 + ks) * 1024);
        const float cs = dot2(o[3], ONES, dot2(o[2], ONES, dot2(o[1], ONES, dot2(o[0], ONES, 0.f))));
        st32<SC1>(asu(half_sum(cs)), rs, (bid < 196 && lane < 32) ? lane * 4 : OOB, OFF_CSP + (ks * 112 + fb * 4 + fgl) * 128);
      }
      DRAIN();
      st64<SC1>(v2u{sopq(tag0), sopq(tag1)}, rs, (bid < 196 && lane < 16) ? lane * 8 : OOB, OFF_FW1 + (fb * 32 + ks) * 128);
    }
    auto fw_poll = [&]() AI { return ld64<SC1>(rs, min(opaque(lane), 27) * 128, OFF_FW1 + (fg >> 2) * 4096); };
    __syncthreads();
    float r0[2], c0n[2];
    v2u fw;
    {
      const int cw = w >> 2, sp = w & 3;
      const int gq = lane >> 4;
      const int sg = (1 + i16) & 3;
      const unsigned char* fbp = sm + CV_H + (cw * 4 + sg) * CV_HSTR + (1 + i16 + 8 * gq - sg) * 2 + sp * 128;
      const unsigned char* gb = sm + CV_U + cw * CV_USTR + (2032 - 16 * i16 + 8 * gq) * 2 + sp * 128 - 3584;
      auto rdF = [&](int n) AI -> v4u {
        const v2u lo = *(const v2u*)(fbp + (n >> 3) * 64 + (n & 7) * 512), hi = *(const v2u*)(fbp + (n >> 3) * 64 + (n & 7) * 512 + 8);
        return v4u{lo[0], lo[1], hi[0], hi[1]};
      };
      f32x4 acc[8];
#pragma unroll
      for (int i = 0; i < 8; ++i) acc[i] = f32x4{0.f, 0.f, 0.f, 0.f};
      unsigned char* pw = sm + CV_RED + w * 8192 + ((4 * i16 + gq) ^ ((i16 >> 1) & 3)) * 16;
      v4u Gf[8], Ff[3];
      Ff[0] = rdF(0);
#pragma unroll
      for (int dl = 7; dl >= 0; --dl) Gf[dl] = *(const v4u*)(gb + (7 - dl) * 512);
      Ff[1] = rdF(1);
      SBAR();
      static_for<0, 16>([&](auto N) AI {
        constexpr int n = N.value, jj = n & 7;
        if constexpr (n + 2 < 16) Ff[(n + 2) % 3] = rdF(n + 2);
#pragma unroll
        for (int dl = 7 - jj; dl >= 0; --dl) { acc[jj + dl] = mfma16(Ff[n % 3], Gf[dl], acc[jj + dl]); asm volatile("" : "+v"(acc[jj + dl])); }
        if constexpr (n < 8) Gf[7 - jj] = *(const v4u*)(gb + 64 + jj * 512);
        if constexpr (n > 8) *(f32x4*)(pw + (jj - 1) * 1024) = acc[jj - 1];
        if constexpr (n == 0) fw = fw_poll();
        if constexpr (n == FWC) { SPIN(flag_ok(fw), fw = fw_poll()); ORDER(); }
        if constexpr (n >= FWC) ld_wgt(n - FWC);
        const v4u ev = {EMPTY, EMPTY, EMPTY, EMPTY};
        if constexpr (n == 1) st128<SC1>(ev, rs, (tid >> 8) * XBUF + (tid & 255) * 16, OFF_X + (Ta * 7 + js) * 4096);
        if constexpr (n == 2)
          st128<SC1>(ev, rs, w < 4 ? OFF_SP + w * SP_BYTES + ((Ta + hh) * 7 + js) * 512 + tokl * 16
                           : w < 6 ? OFF_FH + (((w - 4) * 64 + g * 4 + (lane >> 4)) * KS2 + j * 8) * FSTR + i16 * 16 : OOB, 0);
        SBAR();
      });
      *(f32x4*)(pw + 7 * 1024) = acc[7];
    }
    const int fch = (lane >> 4) & 1;
    v2u ur[2];
#pragma unroll
    for (int i = 0; i < 2; ++i) ur[i] = *(const v2u*)(sm + CV_U + fch * CV_USTR + (2044 - (256 * w + (2 * i + hh) * 64 + i16 * 4)) * 2);
    wait_vm<16 - FWC>();
    __syncthreads();
#pragma unroll
    for (int i = 0; i < 2; ++i) {
      const int qi = (2 * i + hh) * 16 + i16, sl = qi ^ ((qi >> 3) & 3);
      f32x4 y = *(const f32x4*)(sm + CV_RED + ((fch * 32 + w) * 64 + sl) * 16);
#pragma unroll
      for (int sp = 1; sp < 4; ++sp) y += *(const f32x4*)(sm + CV_RED + (((fch * 4 + sp) * 8 + w) * 64 + sl) * 16);
      unsigned yy[2];
#pragma unroll
      for (int e2 = 0; e2 < 2; ++e2) {
        const unsigned cb = pk2(y[2 * e2], y[2 * e2 + 1]);
        const unsigned ud = pk2(bfhi(ur[i][1 - e2]) * dcoef, bflo(ur[i][1 - e2]) * dcoef);
        yy[e2] = pk2(bflo(cb) + bflo(ud), bfhi(cb) + bfhi(ud));
      }
      st128<SC1>(v4u{yy[0], sopq(tag1), yy[1], sopq(tag0)}, rs, fch * 8192 + hh * 256 + i16 * 16, OFF_KVG + c0 * 8192 + (4 * w + 2 * i) * 256);
    }
    SBAR();
    sp_publish(0, 0);
    sp_publish(0, 1);
    raw_barrier();
#pragma unroll
    for (int i = 16 - FWC; i < NPRE; ++i) ld_wgt(i);
    SBAR();
    {
      v4u yg[2];
      auto y_load = [&]() AI {
        yg[0] = ld128<SC1>(rs, (tid >> 4) * 8192 + i16 * 16, OFF_KVG + js * 64 * 8192 + (Ta >> 1) * 256);
        yg[1] = ld128<SC1>(rs, (tid >> 4) * 8192 + i16 * 16, OFF_KVG + (js * 64 + 32) * 8192 + (Ta >> 1) * 256);
      };
      y_load();
      v4u sp0 = sp_load(0, Ta), sp1 = sp_load(0, Ta + 1), spo = sp_load(0, Ta ^ 2);
      SPIN(__all(((yg[0][1] ^ tag1) | (yg[0][3] ^ tag0) | (yg[1][1] ^ tag1) | (yg[1][3] ^ tag0)) == 0u), y_load());
#pragma unroll
      for (int i = 0; i < 2; ++i) {
        const int cl = i * 32 + (tid >> 4);
        *(v2u*)(sm + L_XB + XBUF + cl * 128 + ((i16 ^ (cl >> 2)) * 8)) = v2u{yg[i][0], yg[i][2]};
      }
      { float m; sp_stats(0, 0, sp0, m, r0[0]); c0n[0] = -m * r0[0]; sp_stats(0, 1, sp1, m, r0[1]); c0n[1] = -m * r0[1]; }
      SPIN(tag_ok(spo), spo = sp_load(0, Ta ^ 2));
      __syncthreads();
#pragma unroll
      for (int u = 0; u < 2; ++u) {
        const uint16_t* gp = (const uint16_t*)(sm + L_XB + XBUF + i16 * 512 + (((u * 8 + w) ^ i16) * 8) + (lane >> 4) * 2);
        v2u yv = {(unsigned)gp[0] | ((unsigned)gp[64] << 16), (unsigned)gp[128] | ((unsigned)gp[192] << 16)};
        asm volatile("" : "+v"(yv));
        yres[u] = yv;
        gate_publish(0, u, r0[u], c0n[u]);
        asm volatile("" : "+v"(yres[u]));
      }
    }
    SBAR();
    raw_barrier();
#pragma unroll
    for (int i = 0; i < 14; ++i) csp[i] = ld32<SC1>(rs, (hh * 3584 + tokl) * 4, OFF_CSP + (i * 2 * 3584 + fg * 32) * 4);
#pragma unroll
    for (int i = NPRE; i < NPRE2; ++i) ld_wgt(i);
    SBAR();
  }

  v4u xs[4];
  v2u sst;
  auto tile_of = [&](int t) AI { return g * 4 + (((t & 1) ^ hs) << 1) + (t >> 1); };
  auto x_src = [&](int ly, int t) AI { return OFF_X + ly * ACT_BYTES + tile_of(t) * XBUF + sj * 4096; };
  auto s_load = [&](int ly, int t) AI { sst = ld64<SC1>(rs, (l16 & 511) + (l16 >> 9) * 8, OFF_SP + (2 * ly + 1) * SP_BYTES + (tile_of(t) * 7 + sj) * 512); };
  auto x0_load = [&]() AI {
#pragma unroll
    for (int i = 0; i < 4; ++i) xs[i] = ldfrag<SC1>(rs, l16, x_src(1, 0), i);
    s_load(1, 0);
  };

  auto layer = [&](auto LY) AI {
    constexpr int ly = decltype(LY)::value;
    const int lane = opaque(tid & 63), tokl = lane & 31, hh = lane >> 5;
    auto x_ok = [&]() AI {
      unsigned mx = 0u;
#pragma unroll
      for (int i = 0; i < 4; ++i) mx = umax3(umax3(mx, xs[i][0], xs[i][1]), xs[i][2], xs[i][3]);
      return __all((mx != EMPTY) & (sst[1] == (hh ? tag1 : tag0)));
    };
    auto x_dma = [&](int t, int half) AI {
      const unsigned dst = lds0 + L_XB + (t & 1) * XBUF + sj * 4096;
      if (half) { dma2<2048>(rs, l16, x_src(ly, t), dst); s_load(ly, t); }
      else dma2<0>(rs, l16, x_src(ly, t), dst);
    };
    auto x_fetch = [&](int t) AI {
      unsigned spins = 0;
      bool ok;
#pragma unroll 1
      do {
        x_dma(t, 0); x_dma(t, 1); DRAIN();
        unsigned mx = 0u;
#pragma unroll
        for (int i = 0; i < 4; i += 2) {
          const v4u a = *(const v4u*)(sm + L_XB + (t & 1) * XBUF + sj * 4096 + i * 1024 + l16), b = *(const v4u*)(sm + L_XB + (t & 1) * XBUF + sj * 4096 + i * 1024 + 1024 + l16);
          mx = umax3(umax3(a[0], a[1], a[2]), umax3(a[3], b[0], b[1]), umax3(b[2], b[3], mx));
          asm volatile("" : "+v"(mx) :: "memory");
        }
        ok = __all((mx != EMPTY) & (sst[1] == (hh ? tag1 : tag0)));
      } while (!ok && ++spins < SPIN_CAP);
    };
    auto st_write = [&](int t) AI {
      unsigned char* sp = sm + L_ST + (t % 3) * 2048 + (sj >> 2) * 512 + tokl * 16 + (sj & 3) * 4;
      *(unsigned*)(sp + hh * 1024) = sst[0];
    };
    const int xrd = tokl * 128 + ((hh ^ ((tokl >> 1) & 7)) << 4);
    auto xfrag = [&](int t, int ks) AI -> v4u { return *(const v4u*)(sm + L_XB + (t & 1) * XBUF + (xrd ^ ((ks & 3) << 5)) + (ks >> 2) * 4096); };
    const int hbase = OFF_H + ly * H_BYTES;
    v4u hf[14];
    auto h_load = [&](int u, int i) AI { hf[i] = ldfrag<SC1>(rs, l16, hbase + ((Ta + u) * KS2 + (i < 7 ? kse : ksl)) * 1024, i % 7); };
    v2u tagv = {tag0, tag1};
    asm volatile("" : "+v"(tagv));
    auto h_flag = [&](int t) AI { st64<SC1>(tagv, rs, opaque(l16) == 0 ? 0 : OOB, OFF_FH + ((ly * 64 + tile_of(t)) * KS2 + fg) * FSTR); };
    auto hf_poll = [&](int u, int half) AI { return ld64<SC1>(rs, min(opaque(l16) * (FSTR / 16), 6 * FSTR), OFF_FH + ((ly * 64 + Ta + u) * KS2 + (half ? ksl : kse)) * FSTR); };
    auto hf_check = [&](int u, int half, v2u v) AI { SPIN(flag_ok(v), v = hf_poll(u, half)); ORDER(); };
    v2u hpl;

    v4u sgr;
    auto cs_write = [&]() AI {
      const int ln = opaque(lane), csrow = (ln & 16) + 8 * ((ln >> 2) & 1) + (ln & 3) + 4 * ((ln >> 3) & 1);
      *(float*)(sm + L_CS + w * 128 + csrow * 4) = csum;
    };
    if (ly == 0) {
      x_fetch(0);
      st_write(0);
      __syncthreads();
      x_dma(1, 0); x_dma(1, 1);
    } else {
      SPIN(x_ok(), x0_load());
      __syncthreads();
      cs_write();
#pragma unroll
      for (int i = 0; i < 4; ++i) *(v4u*)(sm + L_XB + sj * 4096 + i * 1024 + l16) = xs[i];
      st_write(0);
      sgr = sp_load(2, Ta + 1);
      x_dma(1, 0); x_dma(1, 1);
      asm volatile("s_waitcnt lgkmcnt(0)" ::: "memory");
      raw_barrier();
    }

    f32x16 acc[2];
    v4u xf[PD], ho;
    f32x4 p0, p1, p2, p3;
    float mean = 0.f, r2[2];
#pragma unroll
    for (int i = 0; i < PD; ++i) xf[i] = xfrag(0, i + (ly == 1 ? NDROP : 0));
    auto ep_cs = [&](int hq) AI {
      const unsigned char* cp = sm + L_CS + w * 128 + hh * 32;
      if (hq) { p2 = *(const f32x4*)(cp + 16); p3 = *(const f32x4*)(cp + 80); } else { p0 = *(const f32x4*)cp; p1 = *(const f32x4*)(cp + 64); }
    };
    float hv[2], sq = 0.f;
    auto ep_one = [&](const f32x16& a, int i) AI {
      hv[i & 1] = fmaxf(fmaf(mean, i < 4 ? p0[i & 3] : p2[i & 3], a[i]), 0.f) * fmaf(mean, i < 4 ? p1[i & 3] : p3[i & 3], a[8 + i]);
      asm volatile("" : "+v"(hv[i & 1]));
    };
    auto h_store = [&](int t) AI { st128<SC1>(ho, rs, l16, hbase + (tile_of(t) * KS2 + fg) * 1024); };
    static_for<0, 4>([&](auto TT) AI {
      constexpr int t = TT.value;
      constexpr int rot = (ly == 1 && t == 0) ? NDROP : 0;
      v4u cb0, cb1, cb2, cb3;
      unsigned cmx = 0u;
      float ga = 0.f, gb = 0.f;
      static_for<0, KS1>([&](auto II) AI {
        constexpr int i = II.value, n = t * KS1 + i;
        if constexpr (i == 0) {
          f32x16 z;
#pragma unroll
          for (int e = 0; e < 16; ++e) z[e] = 0.f;
          acc[t & 1] = mfma32(W1[rot], xf[n % PD], z);
        } else acc[t & 1] = mfma32(W1[(i + rot) % KS1], xf[n % PD], acc[t & 1]);
        asm volatile("" : "+v"(acc[t & 1]));
        SBAR();
        if constexpr (i + PD < KS1) xf[n % PD] = xfrag(t, (i + PD + rot) % KS1);
        else if constexpr (t < 3) xf[n % PD] = xfrag(t + 1, i + PD - KS1);
        if constexpr (t == 2 && i == 0) { wait_vm<5 + (ly == 0 ? 6 : 0)>(); h_flag(0); }
        if constexpr (t == 2 && i == 24) { wait_vm<(ly == 0 ? 6 : 1)>(); h_flag(1); }
        if constexpr (t == 3 && i == 23) { wait_vm<6>(); h_flag(2); }
        if constexpr (t > 0) {
          if constexpr (i >= 3 && i <= 9 && (i & 1)) { ho[(i - 3) / 2] = pk2(hv[0], hv[1]); asm volatile("" : "+v"(ho[(i - 3) / 2])); }
          if constexpr (i >= 1 && i <= 8) ep_one(acc[(t - 1) & 1], i - 1);
          if constexpr (i == H_ST) h_store(t - 1);
        }
        if constexpr (t < 3) {
          const unsigned char* xb = sm + L_XB + ((t + 1) & 1) * XBUF + sj * 4096 + l16;
          constexpr int c0s = t ? XC : XC0, c1s = t ? XC + 3 : XC0 + 3, c2s = t ? XC + 4 : XC0 + 1, c3s = t ? XC + 7 : XC0 + 4, c4s = c3s + 1;
          if constexpr (i == c0s) {
            asm volatile("" : "+v"(sst) :: "memory");
            cb0 = *(const v4u*)xb; cb1 = *(const v4u*)(xb + 1024);
          }
          if constexpr (i == c1s) {
            cmx = umax3(umax3(cb0[0], cb0[1], cb0[2]), umax3(cb0[3], cb1[0], cb1[1]), umax3(cb1[2], cb1[3], cmx));
            asm volatile("" : "+v"(cmx));
          }
          if constexpr (i == c2s) {
            cb2 = *(const v4u*)(xb + 2048); cb3 = *(const v4u*)(xb + 3072);
            cmx = sst[1] != (hh ? tagv[1] : tagv[0]) ? EMPTY : cmx;
            asm volatile("" : "+v"(cmx));
          }
          if constexpr (i == c3s) {
            cmx = umax3(umax3(cb2[0], cb2[1], cb2[2]), umax3(cb2[3], cb3[0], cb3[1]), umax3(cb3[2], cb3[3], cmx));
            asm volatile("" : "+v"(cmx));
          }
          if constexpr (i == c4s) {
            if (__builtin_expect(!__all(cmx != EMPTY), 0)) x_fetch(t + 1);
            st_write(t + 1);
          }
        }
        if constexpr (ly == 0 && t == 0 && i == 4)
          st128<SC1>(v4u{EMPTY, EMPTY, EMPTY, EMPTY}, rs, l16, OFF_X + ACT_BYTES + ((Ta + (w >> 2)) * 7 + js) * 4096 + (w & 3) * 1024);
        if constexpr (ly == 0 && t == 0 && NPRE2 + i < KS1) ld_wgt(NPRE2 + i);
        if constexpr (ly == 0 && t == 0) {
          auto fw2_poll = [&]() AI { const int ln = opaque(lane); return ld64<SC1>(rs, (((ln & 8) ? ksl : kse) + min(ln & 7, 6)) * 128, OFF_FW2 + js * KS2 * 128); };
          if constexpr (i == 6) hpl = fw2_poll();
          if constexpr (i == 17) { SPIN(flag_ok(hpl), hpl = fw2_poll()); ORDER(); }
        }
        if constexpr (ly == 0 && t == 0 && i >= 1 && i <= 4) {
          if constexpr (i == 1) csum = 0.f;
#pragma unroll
          for (int q7 = (i - 1) * 4; q7 < i * 4 && q7 < 14; ++q7) csum += asf(csp[q7]);
          asm volatile("" : "+v"(csum));
        }
        if constexpr (ly == 0 && t == 0 && i == 5) { csum = half_sum(csum); asm volatile("" : "+v"(csum)); }
        if constexpr (ly == 0 && t == 0 && i == 8) cs_write();
        if constexpr (ly == 0 && (t == 1 || t == 2) && (i == 2 || i == 6 || i == 12)) ld_wgt(KS1 + (t - 1) * 4 + i / 5);
        if constexpr (ly == 0 && (t == 1 || t == 2) && i == 21) ld_wgt(KS1 + (t - 1) * 4 + 3);
        if constexpr (ly == 0 && t == 2 && i >= 24 && i < 27) ld_wgt(KS1 + 8 + i - 24);
        if constexpr (wl_slot(ly, t, i) >= 0) {
          constexpr int qn = wl_slot(ly, t, i), f = NWS + qn;
          dma1(rs, l16, wd_src(f), lds0 + L_WD + (w * NWL + qn) * 1024);
        }
        if constexpr (ly == 1 && t == 0) {
          constexpr int st = i - X3S;
          if constexpr (st == 0) SPIN(tag_ok(sgr), sgr = sp_load(2, Ta + 1));
          if constexpr (st == 1) { ga = i16 < 7 ? asf(sgr[0]) : 0.f; gb = i16 < 7 ? asf(sgr[2]) : 0.f; }
          if constexpr (st == 2 || st == 9) { ga = dpp_add<0x128>(ga); gb = dpp_add<0x128>(gb); PIN2(ga, gb); ga = dpp_add<0x124>(ga); gb = dpp_add<0x124>(gb); }
          if constexpr (st == 3 || st == 10) { ga = dpp_add<0x122>(ga); gb = dpp_add<0x122>(gb); PIN2(ga, gb); ga = dpp_add<0x121>(ga); gb = dpp_add<0x121>(gb); }
          if constexpr (st == 4) { float m, r; row_stats(ga, gb, m, r); ga = r; gb = -m * r; }
          if constexpr (st == 5 || st == 6) {
            constexpr int e = st - 5;
            const float xl = bflo(xres[1][e]), xh = bfhi(xres[1][e]);
            float ol = fmaf(fmaf(xl, ga, gb), bflo(yres[1][e]), xl), oh = fmaf(fmaf(xh, ga, gb), bfhi(yres[1][e]), xh);
            PIN2(ol, oh);
            unsigned o = nz(pk2(ol, oh));
            asm volatile("" : "+v"(o));
            xres[1][e] = o;
          }
          if constexpr (st == 7) st64<SC1>(xres[1], rs, (lane * 8) ^ (((2 * w + hh) & 7) << 4), OFF_X + ACT_BYTES + ((Ta + 1) * 7 + js) * 4096 + w * 512);
          if constexpr (st == 8) { ga = dot2(xres[1][0], ONES, dot2(xres[1][1], ONES, 0.f)); gb = dot2(xres[1][0], xres[1][0], dot2(xres[1][1], xres[1][1], 0.f)); }
          if constexpr (st == 11) st128<SC1>(v4u{asu(ga), tagv[0], asu(gb), tagv[1]}, rs, (lane & 15) == 0 ? lane : OOB, OFF_SP + 3 * SP_BYTES + ((Ta + 1) * 7 + js) * 512 + w * 64);
          if constexpr (st >= 1 && st <= 10) PIN2(ga, gb);
          if constexpr (i == BAR) asm volatile("" : "+v"(W1[NDROP - 1]));
        }
        if constexpr (ly == 1 && t == 0 && i >= 3 && i < 15 && (i & 1)) W1[8 + (i - 3) / 2] = ldfrag<SC1>(rs, l16, w1o, 8 + (i - 3) / 2);
        if constexpr (t < 2 && (i == BAR + 1 || i == BAR + 3)) x_dma(t + 2, (i - BAR - 1) / 2);
        if constexpr (t == 2 && i == 15) hpl = hf_poll(0, 0);
        if constexpr (t == 3) {
          if constexpr (i == 0) hf_check(0, 0, hpl);
          if constexpr (i == 7) hpl = hf_poll(0, 1);
          if constexpr (i == 16) hf_check(0, 1, hpl);
          if constexpr (i < 14 && !(i & 1)) h_load(0, i / 2);
          if constexpr (i >= 16 && !(i & 1)) h_load(0, 7 + (i - 16) / 2);
        }
        if constexpr (i == 11) {
          const unsigned char* sp = sm + L_ST + (t % 3) * 2048 + tokl * 16;
          p0 = *(const f32x4*)sp; p1 = *(const f32x4*)(sp + 512);
        }
        if constexpr (i == 13) { p0[0] += p0[1]; p0[2] += p0[3]; p1[0] += p1[1]; asm volatile("" : "+v"(p0[0]), "+v"(p0[2]), "+v"(p1[0])); }
        if constexpr (i == 15) { mean = ((p0[0] + p0[2]) + (p1[0] + p1[2])) * (-1.0f / D); asm volatile("" : "+v"(mean)); }
        if constexpr (!(t & 1)) {
          if constexpr (i == 16) { const unsigned char* sp = sm + L_ST + (t % 3) * 2048 + tokl * 16; p2 = *(const f32x4*)(sp + 1024); p3 = *(const f32x4*)(sp + 1536); }
          if constexpr (i == 19) { p2[0] += p2[1]; p2[2] += p2[3]; p3[0] += p3[1]; asm volatile("" : "+v"(p2[0]), "+v"(p2[2]), "+v"(p3[0])); }
          if constexpr (i == 20) { sq = (p2[0] + p2[2]) + (p3[0] + p3[2]); asm volatile("" : "+v"(sq)); }
          if constexpr (i == 26) { sq = __builtin_amdgcn_rcpf(fmaxf(fmaf(sq, 1.0f / D, -mean * mean), 0.0f) + EPS); asm volatile("" : "+v"(sq)); }
          if constexpr (i == 27) r2[t >> 1] = asf(__builtin_amdgcn_ds_bpermute(w * 16 + (lane >> 4) * 4, asu(sq)));
        }
        if constexpr (i == 25) ep_cs(0);
        if constexpr (t > 0 && i == 2) ep_cs(1);
        if constexpr (t == 3 && i == 27) ep_cs(1);
        if constexpr ((t == 3 && i == BAR) || (t > 0 && i == 11)) raw_barrier();
        if constexpr (i == BAR && t < 3) {
          asm volatile("s_waitcnt lgkmcnt(1)" ::: "memory");
          raw_barrier();
        }
        SBAR();
      });
    });
    v4u sga;
    int rad = 0;
    auto unit = [&](auto UU) AI {
      constexpr int u = UU.value;
      v4u wl[3];
      int wad = 0;
      f32x4 fa, fb, fc;
      auto red_write = [&](int jq) AI {
        const f32x16& a = acc[jq >> 2];
        const int qd = jq & 3;
        *(f32x4*)(sm + L_RED + (wad ^ ((4 * (jq >> 1) + (jq & 1)) << 4)) + jq * 1024) = f32x4{a[4 * qd], a[4 * qd + 1], a[4 * qd + 2], a[4 * qd + 3]};
      };
      static_for<0, 28>([&](auto FF) AI {
        constexpr int f = FF.value, ct = (f / 7) & 1, hi = f % 7 + 7 * (f / 14);
        v4u wf;
        if constexpr (f < NWS) wf = Wd[f]; else if constexpr (f < NWS + NWL) wf = wl[f % 3]; else wf = Wd[f - NWL];
        if constexpr (f == 0 || f == 7) {
          f32x16 z;
#pragma unroll
          for (int e = 0; e < 16; ++e) z[e] = 0.f;
          acc[ct] = mfma32(wf, hf[hi], z);
        } else acc[ct] = mfma32(wf, hf[hi], acc[ct]);
        asm volatile("" : "+v"(acc[ct]));
        SBAR();
        if constexpr (f + 3 >= NWS && f + 3 < NWS + NWL) wl[f % 3] = *(const v4u*)(sm + L_WD + (w * NWL + (f + 3 - NWS)) * 1024 + l16);
        if constexpr (u == 0) {
          if constexpr (f < 6) {
            constexpr int eb[7] = {0, 1, 3, 4, 6, 7, 8};
#pragma unroll
            for (int e = eb[f]; e < eb[f + 1]; ++e) { ep_one(acc[1], e); if (e & 1) ho[e >> 1] = pk2(hv[0], hv[1]); }
          }
          if constexpr (f == 6) h_store(3);
          if constexpr (f == 11) hpl = hf_poll(1, 0);
          if constexpr (f == 18) { wait_vm<1>(); h_flag(3); }
          if constexpr (f == 19) hf_check(1, 0, hpl);
          if constexpr (f == 0) h_load(0, 13);
          constexpr int k0 = ly == 0 ? 0 : KT;
          if constexpr (f >= 2 && f < 2 + 2 * (NWR - NWS - k0) && !(f & 1)) Wd[NWS + k0 + (f - 2) / 2] = ld_wd(NWL + NWS + k0 + (f - 2) / 2);
          if constexpr (f >= 19 && (f & 1)) h_load(1, (f - 19) / 2);
        }
        if constexpr (u == 1 && pace(f) >= 0 && pace(f) < 3) h_load(1, 11 + pace(f));
        if constexpr (u == 1 && ly == 0) {
          if constexpr (pace(f) >= 3) W1[pace(f) - 3] = ldfrag<SC1>(rs, l16, w1o, pace(f) - 3);
          if constexpr (f == SGA) sga = sp_load(2, Ta);
        }
        if constexpr (ly == 1 && u == 1 && f < 14) {
          auto rd = [&](int ww) AI { return *(const f32x4*)(sm + L_RED + rad + ww * 8192); };
          auto add = [&](f32x4& a, const f32x4& b) AI { a[0] += b[0]; a[1] += b[1]; PIN2(a[0], a[1]); a[2] += b[2]; a[3] += b[3]; PIN2(a[2], a[3]); };
          if constexpr (f == 0) { fa = rd(0); fb = rd(1); fc = rd(2); }
          if constexpr (f >= 2 && f <= 8) { if constexpr (f & 1) { add(fa, fc); if constexpr (f + 1 < 8) fc = rd(f + 1); } else { add(fa, fb); if constexpr (f + 1 < 8) fb = rd(f + 1); } }
          if constexpr (f == 9) { fa[0] *= r2[0]; fa[1] *= r2[0]; PIN2(fa[0], fa[1]); fa[2] *= r2[0]; fa[3] *= r2[0]; PIN2(fa[2], fa[3]); }
          if constexpr (f == 10 || f == 11) {
            constexpr int e = f - 10;
            const unsigned fq = pk2(fa[2 * e], fa[2 * e + 1]);
            float ol = bflo(xres[0][e]) + bflo(fq), oh = bfhi(xres[0][e]) + bfhi(fq);
            PIN2(ol, oh);
            unsigned o = pk2(ol, oh);
            asm volatile("" : "+v"(o));
            xres[0][e] = o;
          }
          if constexpr (f == 12) { const int ln = opaque(lane); st64(xres[0], mk_rsrc(out, ACT_BYTES), (ln >> 4) * (D * 2) + (ln & 15) * 8, ((Ta * 32 + 4 * w) * D + js * 64) * 2); }
        }
        if constexpr (f == 22) { const int ln = opaque(lane); wad = opaque(w * 8192 + ((ln ^ ((ln >> 5) << 1)) << 4)); }
        if constexpr (f == 27) {
          const int ln = opaque(lane), li16 = ln & 15, ftok = 4 * w + (ln >> 4);
          rad = opaque((2 * (li16 >> 2) + (li16 & 1)) * 1024 + (((li16 >> 1) & 1) * 32 + (ftok ^ li16)) * 16);
        }
        if constexpr (f >= 23 && f < 27) red_write(f - 23);
        if constexpr (f == ((ly == 1 && u == 1) ? 9 : GB)) __syncthreads();
        SBAR();
      });
      if constexpr (u == 1 && ly == 0) { x3_publish(0, sga); SBAR(); }
#pragma unroll
      for (int jq = 4; jq < 8; ++jq) red_write(jq);
      SBAR();
      if constexpr (u == 0) { h_load(1, 5); h_load(1, 6); hpl = hf_poll(1, 1); }
      SBAR();
      __syncthreads();
      if constexpr (u == 1 && ly == 0) x0_load();
      if constexpr (!(ly == 1 && u == 0)) {
        f32x4 pr[8];
#pragma unroll
        for (int ww = 0; ww < 8; ++ww) pr[ww] = *(const f32x4*)(sm + L_RED + rad + ww * 8192);
        SBAR();
        const f32x4 a = ((pr[0] + pr[1]) + (pr[2] + pr[3])) + ((pr[4] + pr[5]) + (pr[6] + pr[7]));
        const unsigned f01 = pk2(a[0] * r2[u], a[1] * r2[u]), f23 = pk2(a[2] * r2[u], a[3] * r2[u]);
        const v2u xn = {pk2(bflo(xres[u][0]) + bflo(f01), bfhi(xres[u][0]) + bfhi(f01)), pk2(bflo(xres[u][1]) + bflo(f23), bfhi(xres[u][1]) + bfhi(f23))};
        if constexpr (ly == 1) {
          const int ln = opaque(lane);
          st64(xn, mk_rsrc(out, ACT_BYTES), (ln >> 4) * (D * 2) + (ln & 15) * 8, (((Ta + u) * 32 + 4 * w) * D + js * 64) * 2);
        } else {
          xres[u] = xn;
          sp_publish(2, u);
        }
      }
      SBAR();
      if constexpr (u == 0) {
        hf_check(1, 1, hpl);
#pragma unroll
        for (int i = 7; i < 11; ++i) h_load(1, i);
        SBAR();
      }
    };
    unit(IC<0>{});
    unit(IC<1>{});
  };
  layer(IC<0>{});
  layer(IC<1>{});
  st64<SC1>(v2u{sopq(tag0), sopq(tag1)}, rs, (bid == 0 && tid == 0) ? 0 : OOB, OFF_EP);
}

extern "C" void kernel_launch(void* const* d_in, const int* in_sizes, int n_in, void* d_out, int out_size, void* d_ws,
                              size_t ws_size, hipStream_t stream) {
  const uint16_t* q = (const uint16_t*)d_in[0];
  const uint16_t* k = (const uint16_t*)d_in[1];
  const uint16_t* v = (const uint16_t*)d_in[2];
  const uint16_t* shk = (const uint16_t*)d_in[3];
  const uint16_t* ssk = (const uint16_t*)d_in[4];
  const uint16_t* ssd = (const uint16_t*)d_in[5];
  const uint16_t* wg = (const uint16_t*)d_in[7];
  const uint16_t* wu = (const uint16_t*)d_in[8];
  const uint16_t* wd = (const uint16_t*)d_in[9];
  unsigned char* ws = (unsigned char*)d_ws;
  uint16_t* out = (uint16_t*)d_out;
  (void)hipFuncSetAttribute((const void*)k_main, hipFuncAttributeMaxDynamicSharedMemorySize, L_BYTES);
  k_main<<<NAUX + 224, 512, L_BYTES, stream>>>(q, k, v, shk, ssk, ssd, wg, wu, wd, ws, out);
}
```

```cpp
#include <hip/hip_runtime.h>
#include <hip/hip_bf16.h>
#include <stdint.h>

#define DI __device__ __forceinline__
#define AI __attribute__((always_inline))
#define LAS __attribute__((address_space(3)))
typedef unsigned v4u __attribute__((ext_vector_type(4)));
typedef unsigned v2u __attribute__((ext_vector_type(2)));
typedef float f32x4 __attribute__((ext_vector_type(4)));
typedef float f32x2 __attribute__((ext_vector_type(2)));
typedef float f32x16 __attribute__((ext_vector_type(16)));
typedef short bf16x8 __attribute__((ext_vector_type(8)));
typedef short s16x4 __attribute__((ext_vector_type(4)));
typedef __bf16 bf2 __attribute__((ext_vector_type(2)));
typedef __amdgpu_buffer_rsrc_t rsrc_t;

constexpr int S = 2048, D = 448, F = 1792;
constexpr int KS1 = D / 16;
constexpr int KS2 = F / 16;
constexpr float EPS = 1e-5f;

constexpr int ACT_BYTES = S * D * 2;
constexpr int FSTR = 32;
constexpr int OFF_EP = 0;
constexpr int OFF_FW1 = 256;
constexpr int OFF_FW2 = OFF_FW1 + 28 * 32 * 128;
constexpr int OFF_FH = OFF_FW2 + 7 * KS2 * 128;
constexpr int OFF_SP = OFF_FH + 2 * 64 * KS2 * FSTR;
constexpr int SP_BYTES = 64 * 7 * 32 * 16;
constexpr int OFF_KVG = OFF_SP + 4 * SP_BYTES;
constexpr int OFF_X = OFF_KVG + 2 * ACT_BYTES;
constexpr int OFF_H = OFF_X + 2 * ACT_BYTES;
constexpr int H_BYTES = 64 * KS2 * 1024;
constexpr int OFF_W1 = OFF_H + 2 * H_BYTES;
constexpr int OFF_W2 = OFF_W1 + 112 * KS1 * 1024;
constexpr int OFF_CSP = OFF_W2 + 14 * KS2 * 1024;
constexpr int WS_BYTES = OFF_CSP + 28 * 112 * 32 * 4;
static_assert(OFF_SP % 128 == 0 && OFF_KVG % 128 == 0, "alignment");

constexpr int SC1 = 16;
DI rsrc_t mk_rsrc(const void* p, int bytes) { return __builtin_amdgcn_make_buffer_rsrc((void*)p, 0, bytes, 0x00020000); }
template <int AUX = 0> DI v4u ld128(rsrc_t r, int voff, int soff) { return __builtin_amdgcn_raw_buffer_load_b128(r, voff, soff, AUX); }
template <int AUX = 0> DI v2u ld64(rsrc_t r, int voff, int soff) { return __builtin_amdgcn_raw_buffer_load_b64(r, voff, soff, AUX); }
template <int AUX = 0> DI unsigned ld32(rsrc_t r, int voff, int soff) { return __builtin_amdgcn_raw_buffer_load_b32(r, voff, soff, AUX); }
template <int AUX = 0> DI void st128(v4u v, rsrc_t r, int voff, int soff) {
  __builtin_amdgcn_raw_buffer_store_b128(v, r, voff, soff, AUX);
  asm volatile("s_nop 1" ::"v"(v));
}
template <int AUX = 0> DI void st64(v2u v, rsrc_t r, int voff, int soff) { __builtin_amdgcn_raw_buffer_store_b64(v, r, voff, soff, AUX); }
template <int AUX = 0> DI void st32(unsigned v, rsrc_t r, int voff, int soff) { __builtin_amdgcn_raw_buffer_store_b32(v, r, voff, soff, AUX); }
DI float asf(unsigned u) { return __builtin_bit_cast(float, u); }
DI unsigned asu(float f) { return __builtin_bit_cast(unsigned, f); }
DI float bflo(unsigned u) { return asf(u << 16); }
DI float bfhi(unsigned u) { return asf(u & 0xffff0000u); }
DI unsigned pk2(float a, float b) {
  f32x2 v = {a, b};
  return __builtin_bit_cast(unsigned, __builtin_convertvector(v, bf2));
}
DI float dot2(unsigned a, unsigned b, float c) {
  return __builtin_amdgcn_fdot2_f32_bf16(__builtin_bit_cast(bf2, a), __builtin_bit_cast(bf2, b), c, false);
}
DI f32x16 mfma32(v4u a, v4u b, f32x16 c) {
  return __builtin_amdgcn_mfma_f32_32x32x16_bf16(__builtin_bit_cast(bf16x8, a), __builtin_bit_cast(bf16x8, b), c, 0, 0, 0);
}
DI f32x4 mfma16(v4u a, v4u b, f32x4 c) {
  return __builtin_amdgcn_mfma_f32_16x16x32_bf16(__builtin_bit_cast(bf16x8, a), __builtin_bit_cast(bf16x8, b), c, 0, 0, 0);
}
DI v4u tr_frag(LAS unsigned char* p, int second) {
  const v2u l2 = __builtin_bit_cast(v2u, __builtin_amdgcn_ds_read_tr16_b64_v4i16((LAS s16x4*)p));
  const v2u h2 = __builtin_bit_cast(v2u, __builtin_amdgcn_ds_read_tr16_b64_v4i16((LAS s16x4*)(p + second)));
  return v4u{l2[0], l2[1], h2[0], h2[1]};
}
template <int CTRL> DI float dpp_add(float v) {
  return v + asf(__builtin_amdgcn_update_dpp(0, asu(v), CTRL, 0xf, 0xf, true));
}
DI float row16_sum(float v) {
  v = dpp_add<0x128>(v); v = dpp_add<0x124>(v); v = dpp_add<0x122>(v); v = dpp_add<0x121>(v);
  return v;
}
#define PIN2(a, b) asm volatile("" : "+v"(a), "+v"(b))
#define SBAR() __builtin_amdgcn_sched_barrier(0)
#define DRAIN() asm volatile("s_waitcnt vmcnt(0)" ::: "memory")
#define ORDER() __builtin_amdgcn_fence(__ATOMIC_ACQUIRE, "wavefront")
constexpr unsigned ONES = 0x3f803f80u;
constexpr unsigned EMPTY = 0xffffffffu;
constexpr unsigned SPIN_CAP = 1u << 16;

DI void row_stats(float s, float ss, float& m, float& r) {
  m = s * (1.0f / D);
  const float var = fmaxf(fmaf(ss, 1.0f / D, -m * m), 0.0f);
  r = __builtin_amdgcn_rsqf(var + EPS);
}
constexpr int NWL = 12;
constexpr int NWR = 28 - NWL;
constexpr int NDROP = 14;
constexpr int XBUF = D * 64;
constexpr int L_XB = 0, L_ST = 2 * XBUF, L_CS = L_ST + 3 * 2048, L_RED = 0, L_WD = 65536, L_BYTES = L_WD + NWL * 8 * 1024;
static_assert(L_CS + 8 * 128 <= L_WD && L_BYTES <= 163840, "LDS map");
constexpr int CV_HSTR = 4160, CV_USTR = 2304 * 2;
constexpr int CV_H = 0, CV_U = CV_H + 2 * 4 * CV_HSTR, CV_RED = L_WD, CV_W = 43008;
static_assert(CV_U + 2 * CV_USTR <= CV_W && CV_W + 18432 <= L_WD && CV_RED + 65536 <= L_BYTES, "conv LDS map");

template <int V> struct IC { static constexpr int value = V; };
template <int I, int N, class Fn> DI void static_for(Fn&& fn) {
  if constexpr (I < N) { fn(IC<I>{}); static_for<I + 1, N>(fn); }
}
template <int N> DI void wait_vm() { asm volatile("s_waitcnt vmcnt(%0)" ::"n"(N) : "memory"); }
DI float half_sum(float v) {
  const auto r = __builtin_amdgcn_permlane32_swap(asu(v), asu(v), false, false);
  return asf(r[0]) + asf(r[1]);
}
DI unsigned umax3(unsigned a, unsigned b, unsigned c) { return max(max(a, b), c); }
constexpr int OOB = (int)0x80000000;
DI void raw_barrier() { asm volatile("" ::: "memory"); __builtin_amdgcn_s_barrier(); asm volatile("" ::: "memory"); }
template <int AUX = 0> DI v4u ldfrag(rsrc_t r, int l16, int base, int i) { return ld128<AUX>(r, l16, base + i * 1024); }
template <int O0> DI void dma2(rsrc_t r, int l16, int soff, unsigned lds) {
  asm volatile("s_mov_b32 m0, %0\n\ts_nop 0\n\tbuffer_load_dwordx4 %1, %2, %3 offen offset:%4 sc1 lds\n\tbuffer_load_dwordx4 %1, %2, %3 offen offset:%5 sc1 lds"
               ::"s"(lds), "v"(l16), "s"(r), "s"(soff), "n"(O0), "n"(O0 + 1024) : "m0", "memory");
}
DI void dma1(rsrc_t r, int l16, int soff, unsigned lds) {
  asm volatile("s_mov_b32 m0, %0\n\ts_nop 0\n\tbuffer_load_dwordx4 %1, %2, %3 offen sc1 lds" ::"s"(lds), "v"(l16), "s"(r), "s"(soff) : "m0", "memory");
}
DI int opaque(int v) { asm volatile("" : "+v"(v)); return v; }
DI unsigned sopq(unsigned v) { asm volatile("" : "+s"(v)); return v; }
DI unsigned nz(unsigned v) { return min(v, EMPTY - 1u); }

constexpr int NWS = 11;
constexpr int KT = 1;
constexpr int PD = 3;
constexpr int FWC = 5, NPRE = 18, NPRE2 = 24;
constexpr int XC0 = 18, X3S = 2, SGA = 17, GB = 6;
constexpr int BAR = KS1 - 1 - PD;
constexpr int H_ST = 10;
constexpr int XC = 14;
constexpr int pace(int i) {
  constexpr int sl[11] = {0, 3, 5, 8, 11, 13, 16, 19, 21, 24, 26};
  for (int n = 0; n < 11; ++n) if (sl[n] == i) return n;
  return -1;
}
constexpr int wl_slot(int ly, int t, int i) {
  return (ly != 0 || t == 3) ? -1 : t == 0 ? (i == 19 ? 0 : i == 21 ? 1 : i == 23 ? 2 : i == 26 ? 3 : -1) : i == 16 ? t * 4 : i == 19 ? t * 4 + 1 : i == 23 ? t * 4 + 2 : i == 27 ? t * 4 + 3 : -1;
}

#define SPIN(cond_ok, reload) do { if (__builtin_expect(!(cond_ok), 0)) { unsigned spins_ = 0; \
  _Pragma("unroll 1") do { asm volatile("" ::: "memory"); reload; } while (!(cond_ok) && ++spins_ < SPIN_CAP); } } while (0)
DI void mk_tags(v2u ep, unsigned& t0, unsigned& t1) {
  const unsigned e0 = __builtin_amdgcn_readfirstlane(ep[0]), e1 = __builtin_amdgcn_readfirstlane(ep[1]);
  t0 = e0 + 1u;
  if (t0 == e1 || t0 == EMPTY) ++t0;
  if (t0 == e1 || t0 == EMPTY) ++t0;
  t1 = e1 + 0x9e3779b9u;
#pragma unroll
  for (int i = 0; i < 3; ++i)
    if (t1 == t0 || t1 == e0 || t1 == EMPTY) t1 += 0x10001u;
}
constexpr int NAUX = 32;

__global__ void __launch_bounds__(512, 2) k_main(const uint16_t* __restrict__ q, const uint16_t* __restrict__ kin,
                                                 const uint16_t* __restrict__ vin, const uint16_t* __restrict__ shk,
                                                 const uint16_t* __restrict__ ssk, const uint16_t* __restrict__ ssd,
                                                 const uint16_t* __restrict__ wg, const uint16_t* __restrict__ wu,
                                                 const uint16_t* __restrict__ wdn, unsigned char* __restrict__ ws,
                                                 uint16_t* __restrict__ out) {
  extern __shared__ __attribute__((aligned(16))) unsigned char sm[];
  LAS unsigned char* L = (LAS unsigned char*)sm;
  const int tid = threadIdx.x, lane = tid & 63;
  int w_ = __builtin_amdgcn_readfirstlane(tid >> 6);
  asm volatile("" : "+s"(w_));
  const int w = w_;
  const rsrc_t rs = mk_rsrc(ws, WS_BYTES);
  const int l16 = lane * 16, tokl = lane & 31, hh = lane >> 5, i16 = lane & 15;
  const v2u ep = ld64<SC1>(rs, 0, OFF_EP);

  const int tr_off = (hh * 8 + (i16 >> 2)) * 144 + (8 * (i16 & 1) + 4 * ((i16 >> 1) & 1)) * 2;

  if (blockIdx.x < NAUX) {
    const int r = tid >> 3, qq = tid & 7;
    v4u a[4][2];
    for (int i = 0; i < 4; ++i) __builtin_amdgcn_s_sleep(36);
#pragma unroll
    for (int n = 0; n < 4; ++n) {
      const int b = min((int)blockIdx.x + 32 * n, 97);
      const size_t src = (size_t)((b / 7) * 128 + r) * D + (b % 7) * 64 + qq * 8;
      a[n][0] = *(const v4u*)(wdn + src); a[n][1] = *(const v4u*)(wdn + src + 64 * D);
    }
#pragma unroll
    for (int n = 0; n < 4; ++n) {
      *(v4u*)(sm + n * 18432 + r * 144 + qq * 16) = a[n][0];
      *(v4u*)(sm + n * 18432 + (64 + r) * 144 + qq * 16) = a[n][1];
    }
    __syncthreads();
    unsigned tg0, tg1;
    mk_tags(ep, tg0, tg1);
    const v2u tg = {tg0, tg1};
#pragma unroll
    for (int n = 0; n < 4; ++n) {
      const int b = blockIdx.x + 32 * n, kb = b / 7, cb = b % 7;
#pragma unroll
      for (int ct = 0; ct < 2; ++ct) {
        const v4u o = tr_frag(L + n * 18432 + w * 16 * 144 + tr_off + (ct * 32 + ((lane >> 4) & 1) * 16) * 2, 4 * 144);
        st128<SC1>(o, rs, b < 98 ? l16 : OOB, OFF_W2 + ((cb * 2 + ct) * KS2 + kb * 8 + w) * 1024);
      }
    }
    DRAIN();
    {
      const int b = blockIdx.x + 32 * (lane >> 4);
      st64<SC1>(tg, rs, b < 98 ? ((b % 7) * KS2 + (b / 7) * 8 + w) * 128 + i16 * 8 : OOB, OFF_FW2);
    }
    return;
  }

  const int bid = blockIdx.x - NAUX;
  const int xcd = bid & 7, li = bid >> 3;
  const int g = (xcd >> 1) * 4 + li / 7, hs = xcd & 1, js = li % 7, j = hs * 7 + js;
  const int fg = j * 8 + w;
  const int sj = w < 7 ? w : 6;
  const int w1o = OFF_W1 + fg * KS1 * 1024;
  const int Ta = g * 4 + hs * 2;
  const unsigned lds0 = (unsigned)(size_t)L;

  v4u W1[KS1], Wd[NWR];
  float csum;
  unsigned csp[14];
  v2u xres[2], yres[2];
  unsigned tag0, tag1;
  const int kse = hs * 56 + w * 7, ksl = (hs ^ 1) * 56 + w * 7;
  auto wd_src = [&](int f) AI { return OFF_W2 + ((js * 2 + ((f / 7) & 1)) * KS2 + (f < 14 ? kse : ksl) + f % 7) * 1024; };
  auto ld_wd = [&](int f) AI { return ld128<SC1>(rs, l16, wd_src(f)); };
  auto ld_wgt = [&](int n) AI {
    if (n < KS1) W1[n] = ldfrag<SC1>(rs, l16, w1o, n);
    else if (n < KS1 + NWS) Wd[n - KS1] = ld_wd(n - KS1);
  };
  auto tag_ok = [&](v4u v) AI { return __all(((v[1] ^ sopq(tag0)) | (v[3] ^ sopq(tag1))) == 0u); };
  auto flag_ok = [&](v2u v) AI { return __all(((v[0] ^ sopq(tag0)) | (v[1] ^ sopq(tag1))) == 0u); };
  auto sp_publish = [&](int which, int u) AI {
    const int ln = opaque(lane);
    const v2u xn = xres[u];
    const float s = row16_sum(dot2(xn[0], ONES, dot2(xn[1], ONES, 0.f))), ss = row16_sum(dot2(xn[0], xn[0], dot2(xn[1], xn[1], 0.f)));
    st128<SC1>(v4u{asu(s), sopq(tag0), asu(ss), sopq(tag1)}, rs, (ln & 15) == 0 ? ln : OOB, OFF_SP + which * SP_BYTES + ((Ta + u) * 7 + js) * 512 + w * 64);
  };
  auto sp_load = [&](int which, int T) AI {
    const int ln = opaque(lane);
    return ld128<SC1>(rs, ((ln & 15) < 7 ? (ln & 15) * 512 : 0) + (ln & 48), OFF_SP + which * SP_BYTES + T * 7 * 512 + w * 64);
  };
  auto sp_stats = [&](int which, int u, v4u sp, float& m, float& r) AI {
    SPIN(tag_ok(sp), sp = sp_load(which, Ta + u));
    row_stats(row16_sum(i16 < 7 ? asf(sp[0]) : 0.f), row16_sum(i16 < 7 ? asf(sp[2]) : 0.f), m, r);
  };
  auto gate_publish = [&](int ly, int u, float r, float c) AI {
    v2u xn;
#pragma unroll
    for (int e = 0; e < 2; ++e) {
      const float xl = bflo(xres[u][e]), xh = bfhi(xres[u][e]);
      xn[e] = nz(pk2(fmaf(fmaf(xl, r, c), bflo(yres[u][e]), xl), fmaf(fmaf(xh, r, c), bfhi(yres[u][e]), xh)));
    }
    xres[u] = xn;
    const int ln = opaque(lane);
    st64<SC1>(xn, rs, (ln * 8) ^ (((2 * w + (ln >> 5)) & 7) << 4), OFF_X + ly * ACT_BYTES + ((Ta + u) * 7 + js) * 4096 + w * 512);
    sp_publish(2 * ly + 1, u);
  };
  auto x3_publish = [&](int u, v4u sp) AI {
    float m, r;
    sp_stats(2, u, sp, m, r);
    gate_publish(1, u, r, -m * r);
  };

  {
    const int c0 = bid * 2;
    const int r = tid >> 3, qq = tid & 7;
    const int tok = Ta * 32 + r, ca = js * 64 + qq * 8;
    const rsrc_t rk = mk_rsrc(kin, ACT_BYTES), rh = mk_rsrc(ssk, ACT_BYTES);
    v4u kk[4], sw[4];
    const v4u swl = *(const v4u*)(shk + js * 256 + tokl * 8);
#pragma unroll
    for (int i = 0; i < 4; ++i) kk[i] = ld128(rk, ((tok - i) * D + ca) * 2, 0);
    const v4u vv = *(const v4u*)(vin + (size_t)tok * D + ca);
    const int ch = tid >> 8, i8 = tid & 255;
    const v4u d = ld128(rh, tid * 16, c0 * S * 2);
    const v2u e = ld64(rh, tid * 16 + 16 + c0 * S * 2, 0);
    SBAR();
    *(v4u*)(sm + CV_RED + 16384 + w * 1024 + l16) = swl;
    asm volatile("" ::: "memory");
#pragma unroll
    for (int i = 0; i < 4; ++i) sw[i] = *(const v4u*)(sm + CV_RED + 16384 + w * 1024 + qq * 64 + i * 16);
    mk_tags(ep, tag0, tag1);
    {
      uint16_t* tl = (uint16_t*)(sm + CV_RED) + qq * 8 * 64 + ((63 - r) ^ (qq << 3));
#pragma unroll
      for (int e2 = 0; e2 < 4; ++e2) {
        const unsigned wa01 = sw[e2][0], wa23 = sw[e2][1], wb01 = sw[e2][2], wb23 = sw[e2][3];
        unsigned p = pk2(bflo(kk[3][e2]) * bfhi(wa23), bfhi(kk[3][e2]) * bfhi(wb23));
        unsigned acc = p;
        p = pk2(bflo(kk[2][e2]) * bflo(wa23), bfhi(kk[2][e2]) * bflo(wb23));
        acc = pk2(bflo(acc) + bflo(p), bfhi(acc) + bfhi(p));
        p = pk2(bflo(kk[1][e2]) * bfhi(wa01), bfhi(kk[1][e2]) * bfhi(wb01));
        acc = pk2(bflo(acc) + bflo(p), bfhi(acc) + bfhi(p));
        p = pk2(bflo(kk[0][e2]) * bflo(wa01), bfhi(kk[0][e2]) * bflo(wb01));
        acc = pk2(bflo(acc) + bflo(p), bfhi(acc) + bfhi(p));
        const unsigned kv = pk2(bflo(acc) * bflo(vv[e2]), bfhi(acc) * bfhi(vv[e2]));
        tl[(2 * e2) * 64] = (uint16_t)kv;
        tl[(2 * e2 + 1) * 64] = (uint16_t)(kv >> 16);
      }
    }
    SBAR();
    const int wt = min(bid, 195), kb = wt / 28, fb = wt % 28, ks = kb * 4 + (w & 3), rl = lane >> 3;
    v4u wa[4];
    __syncthreads();
#pragma unroll
    for (int i = 0; i < 2; ++i) {
      const int cl = i * 32 + (tid >> 4);
      const v2u o = *(const v2u*)(sm + CV_RED + cl * 128 + ((i16 ^ ((cl >> 3) << 1)) * 8));
      st128<SC1>(v4u{o[0], sopq(tag0), o[1], sopq(tag1)}, rs, (tid >> 4) * 8192 + i16 * 16, OFF_KVG + (js * 64 + i * 32) * 8192 + (S - 64 - Ta * 32) * 4);
    }
    SBAR();
    {
      const unsigned a0 = __builtin_amdgcn_alignbit(d[1], d[0], 16), a1 = __builtin_amdgcn_alignbit(d[2], d[1], 16),
                     a2 = __builtin_amdgcn_alignbit(d[3], d[2], 16), a3 = __builtin_amdgcn_alignbit(e[0], d[3], 16),
                     a4 = __builtin_amdgcn_alignbit(e[1], e[0], 16);
      unsigned char* hb = sm + CV_H + ch * 4 * CV_HSTR + 32 + i8 * 16;
      *(v4u*)(hb) = d;
      *(v4u*)(hb + CV_HSTR) = v4u{a0, a1, a2, a3};
      *(v4u*)(hb + 2 * CV_HSTR) = v4u{d[1], d[2], d[3], e[0]};
      *(v4u*)(hb + 3 * CV_HSTR) = v4u{a1, a2, a3, a4};
      const unsigned d0 = __builtin_amdgcn_readfirstlane(d[0]), d1 = __builtin_amdgcn_readfirstlane(d[1]);
      if (i8 < 40) {
        const int q8 = i8 - 32, sg = q8 >> 1;
        v4u pv = {0u, 0u, 0u, 0u};
        if (q8 == 3) pv[3] = d0 << 16;
        if (q8 == 5) pv[3] = d0;
        if (q8 == 7) { pv[2] = d0 << 16; pv[3] = __builtin_amdgcn_alignbit(d1, d0, 16); }
        *(v4u*)(sm + (q8 < 0 ? CV_U + ch * CV_USTR + 4096 + i8 * 16 : CV_H + (ch * 4 + sg) * CV_HSTR + (q8 & 1) * 16)) = pv;
      }
    }
    float dcoef;
    auto own_load = [&]() AI {
#pragma unroll
      for (int u = 0; u < 2; ++u) xres[u] = *(const v2u*)(q + (size_t)((Ta + u) * 32 + 4 * w + (lane >> 4)) * D + js * 64 + i16 * 4);
      dcoef = bflo(ssd[c0 + ((lane >> 4) & 1)]);
    };
    if (w < 4) {
      v4u ug[4];
      auto u_load = [&]() AI {
#pragma unroll
        for (int i = 0; i < 4; ++i) ug[i] = ld128<SC1>(rs, tid * 16, OFF_KVG + c0 * 8192 + i * 4096);
      };
      __builtin_amdgcn_s_sleep(40);
      u_load();
      SPIN(tag_ok(ug[0]) && tag_ok(ug[1]) && tag_ok(ug[2]) && tag_ok(ug[3]), u_load());
#pragma unroll
      for (int i = 0; i < 4; ++i) *(v2u*)(sm + CV_U + (i >> 1) * CV_USTR + (i & 1) * 2048 + tid * 8) = v2u{ug[i][0], ug[i][2]};
      own_load();
    } else {
      {
        const rsrc_t rg = mk_rsrc(wg, D * F * 2), ru = mk_rsrc(wu, D * F * 2);
        const int vo = ((ks * 16 + rl) * F + fb * 64 + qq * 8) * 2;
        wa[0] = ld128(rg, vo, 0); wa[1] = ld128(rg, vo, 16 * F); wa[2] = ld128(ru, vo, 0); wa[3] = ld128(ru, vo, 16 * F);
      }
      own_load();
      unsigned char* wb = sm + CV_W + (w - 4) * (32 * 144) + rl * 144 + qq * 16;
      *(v4u*)wb = wa[0]; *(v4u*)(wb + 8 * 144) = wa[1]; *(v4u*)(wb + 16 * 144) = wa[2]; *(v4u*)(wb + 24 * 144) = wa[3];
      asm volatile("" ::: "memory");
#pragma unroll
      for (int fgl = 0; fgl < 4; ++fgl) {
        const v4u o = tr_frag(L + CV_W + (w - 4) * (32 * 144) + ((lane >> 4) & 1) * (16 * 144) + tr_off + fgl * 32, 4 * 144);
        st128<SC1>(o, rs, bid < 196 ? l16 : OOB, OFF_W1 + ((fb * 4 + fgl) * KS1 + ks) * 1024);
        const float cs = dot2(o[3], ONES, dot2(o[2], ONES, dot2(o[1], ONES, dot2(o[0], ONES, 0.f))));
        st32<SC1>(asu(half_sum(cs)), rs, (bid < 196 && lane < 32) ? lane * 4 : OOB, OFF_CSP + (ks * 112 + fb * 4 + fgl) * 128);
      }
      DRAIN();
      st64<SC1>(v2u{sopq(tag0), sopq(tag1)}, rs, (bid < 196 && lane < 16) ? lane * 8 : OOB, OFF_FW1 + (fb * 32 + ks) * 128);
    }
    auto fw_poll = [&]() AI { return ld64<SC1>(rs, min(opaque(lane), 27) * 128, OFF_FW1 + (fg >> 2) * 4096); };
    __syncthreads();
    float r0[2], c0n[2];
    v2u fw;
    {
      const int cw = w >> 2, sp = w & 3;
      const int gq = lane >> 4;
      const int sg = (1 + i16) & 3;
      const unsigned char* fbp = sm + CV_H + (cw * 4 + sg) * CV_HSTR + (1 + i16 + 8 * gq - sg) * 2 + sp * 128;
      const unsigned char* gb = sm + CV_U + cw * CV_USTR + (2032 - 16 * i16 + 8 * gq) * 2 + sp * 128 - 3584;
      auto rdF = [&](int n) AI -> v4u {
        const v2u lo = *(const v2u*)(fbp + (n >> 3) * 64 + (n & 7) * 512), hi = *(const v2u*)(fbp + (n >> 3) * 64 + (n & 7) * 512 + 8);
        return v4u{lo[0], lo[1], hi[0], hi[1]};
      };
      f32x4 acc[8];
#pragma unroll
      for (int i = 0; i < 8; ++i) acc[i] = f32x4{0.f, 0.f, 0.f, 0.f};
      unsigned char* pw = sm + CV_RED + w * 8192 + ((4 * i16 + gq) ^ ((i16 >> 1) & 3)) * 16;
      v4u Gf[8], Ff[3];
      Ff[0] = rdF(0);
#pragma unroll
      for (int dl = 7; dl >= 0; --dl) Gf[dl] = *(const v4u*)(gb + (7 - dl) * 512);
      Ff[1] = rdF(1);
      SBAR();
      static_for<0, 16>([&](auto N) AI {
        constexpr int n = N.value, jj = n & 7;
        if constexpr (n + 2 < 16) Ff[(n + 2) % 3] = rdF(n + 2);
#pragma unroll
        for (int dl = 7 - jj; dl >= 0; --dl) { acc[jj + dl] = mfma16(Ff[n % 3], Gf[dl], acc[jj + dl]); asm volatile("" : "+v"(acc[jj + dl])); }
        if constexpr (n < 8) Gf[7 - jj] = *(const v4u*)(gb + 64 + jj * 512);
        if constexpr (n > 8) *(f32x4*)(pw + (jj - 1) * 1024) = acc[jj - 1];
        if constexpr (n == 0) fw = fw_poll();
        if constexpr (n == FWC) { SPIN(flag_ok(fw), fw = fw_poll()); ORDER(); }
        if constexpr (n >= FWC) ld_wgt(n - FWC);
        const v4u ev = {EMPTY, EMPTY, EMPTY, EMPTY};
        if constexpr (n == 1) st128<SC1>(ev, rs, (tid >> 8) * XBUF + (tid & 255) * 16, OFF_X + (Ta * 7 + js) * 4096);
        if constexpr (n == 2)
          st128<SC1>(ev, rs, w < 4 ? OFF_SP + w * SP_BYTES + ((Ta + hh) * 7 + js) * 512 + tokl * 16
                           : w < 6 ? OFF_FH + (((w - 4) * 64 + g * 4 + (lane >> 4)) * KS2 + j * 8) * FSTR + i16 * 16 : OOB, 0);
        SBAR();
      });
      *(f32x4*)(pw + 7 * 1024) = acc[7];
    }
    const int fch = (lane >> 4) & 1;
    v2u ur[2];
#pragma unroll
    for (int i = 0; i < 2; ++i) ur[i] = *(const v2u*)(sm + CV_U + fch * CV_USTR + (2044 - (256 * w + (2 * i + hh) * 64 + i16 * 4)) * 2);
    wait_vm<16 - FWC>();
    __syncthreads();
#pragma unroll
    for (int i = 0; i < 2; ++i) {
      const int qi = (2 * i + hh) * 16 + i16, sl = qi ^ ((qi >> 3) & 3);
      f32x4 y = *(const f32x4*)(sm + CV_RED + ((fch * 32 + w) * 64 + sl) * 16);
#pragma unroll
      for (int sp = 1; sp < 4; ++sp) y += *(const f32x4*)(sm + CV_RED + (((fch * 4 + sp) * 8 + w) * 64 + sl) * 16);
      unsigned yy[2];
#pragma unroll
      for (int e2 = 0; e2 < 2; ++e2) {
        const unsigned cb = pk2(y[2 * e2], y[2 * e2 + 1]);
        const unsigned ud = pk2(bfhi(ur[i][1 - e2]) * dcoef, bflo(ur[i][1 - e2]) * dcoef);
        yy[e2] = pk2(bflo(cb) + bflo(ud), bfhi(cb) + bfhi(ud));
      }
      st128<SC1>(v4u{yy[0], sopq(tag1), yy[1], sopq(tag0)}, rs, fch * 8192 + hh * 256 + i16 * 16, OFF_KVG + c0 * 8192 + (4 * w + 2 * i) * 256);
    }
    SBAR();
    sp_publish(0, 0);
    sp_publish(0, 1);
    raw_barrier();
#pragma unroll
    for (int i = 16 - FWC; i < NPRE; ++i) ld_wgt(i);
    SBAR();
    {
      v4u yg[2];
      auto y_load = [&]() AI {
        yg[0] = ld128<SC1>(rs, (tid >> 4) * 8192 + i16 * 16, OFF_KVG + js * 64 * 8192 + (Ta >> 1) * 256);
        yg[1] = ld128<SC1>(rs, (tid >> 4) * 8192 + i16 * 16, OFF_KVG + (js * 64 + 32) * 8192 + (Ta >> 1) * 256);
      };
      y_load();
      v4u sp0 = sp_load(0, Ta), sp1 = sp_load(0, Ta + 1), spo = sp_load(0, Ta ^ 2);
      SPIN(__all(((yg[0][1] ^ tag1) | (yg[0][3] ^ tag0) | (yg[1][1] ^ tag1) | (yg[1][3] ^ tag0)) == 0u), y_load());
#pragma unroll
      for (int i = 0; i < 2; ++i) {
        const int cl = i * 32 + (tid >> 4);
        *(v2u*)(sm + L_XB + XBUF + cl * 128 + ((i16 ^ (cl >> 2)) * 8)) = v2u{yg[i][0], yg[i][2]};
      }
      { float m; sp_stats(0, 0, sp0, m, r0[0]); c0n[0] = -m * r0[0]; sp_stats(0, 1, sp1, m, r0[1]); c0n[1] = -m * r0[1]; }
      SPIN(tag_ok(spo), spo = sp_load(0, Ta ^ 2));
      __syncthreads();
#pragma unroll
      for (int u = 0; u < 2; ++u) {
        const uint16_t* gp = (const uint16_t*)(sm + L_XB + XBUF + i16 * 512 + (((u * 8 + w) ^ i16) * 8) + (lane >> 4) * 2);
        v2u yv = {(unsigned)gp[0] | ((unsigned)gp[64] << 16), (unsigned)gp[128] | ((unsigned)gp[192] << 16)};
        asm volatile("" : "+v"(yv));
        yres[u] = yv;
        gate_publish(0, u, r0[u], c0n[u]);
        asm volatile("" : "+v"(yres[u]));
      }
    }
    SBAR();
    raw_barrier();
#pragma unroll
    for (int i = 0; i < 14; ++i) csp[i] = ld32<SC1>(rs, (hh * 3584 + tokl) * 4, OFF_CSP + (i * 2 * 3584 + fg * 32) * 4);
#pragma unroll
    for (int i = NPRE; i < NPRE2; ++i) ld_wgt(i);
    SBAR();
  }

  v4u xs[4];
  v2u sst;
  auto tile_of = [&](int t) AI { return g * 4 + (((t & 1) ^ hs) << 1) + (t >> 1); };
  auto x_src = [&](int ly, int t) AI { return OFF_X + ly * ACT_BYTES + tile_of(t) * XBUF + sj * 4096; };
  auto s_load = [&](int ly, int t) AI { sst = ld64<SC1>(rs, (l16 & 511) + (l16 >> 9) * 8, OFF_SP + (2 * ly + 1) * SP_BYTES + (tile_of(t) * 7 + sj) * 512); };
  auto x0_load = [&]() AI {
#pragma unroll
    for (int i = 0; i < 4; ++i) xs[i] = ldfrag<SC1>(rs, l16, x_src(1, 0), i);
    s_load(1, 0);
  };

  auto layer = [&](auto LY) AI {
    constexpr int ly = decltype(LY)::value;
    const int lane = opaque(tid & 63), tokl = lane & 31, hh = lane >> 5;
    auto x_ok = [&]() AI {
      unsigned mx = 0u;
#pragma unroll
      for (int i = 0; i < 4; ++i) mx = umax3(umax3(mx, xs[i][0], xs[i][1]), xs[i][2], xs[i][3]);
      return __all((mx != EMPTY) & (sst[1] == (hh ? tag1 : tag0)));
    };
    auto x_dma = [&](int t, int half) AI {
      const unsigned dst = lds0 + L_XB + (t & 1) * XBUF + sj * 4096;
      if (half) { dma2<2048>(rs, l16, x_src(ly, t), dst); s_load(ly, t); }
      else dma2<0>(rs, l16, x_src(ly, t), dst);
    };
    auto x_fetch = [&](int t) AI {
      unsigned spins = 0;
      bool ok;
#pragma unroll 1
      do {
        x_dma(t, 0); x_dma(t, 1); DRAIN();
        unsigned mx = 0u;
#pragma unroll
        for (int i = 0; i < 4; i += 2) {
          const v4u a = *(const v4u*)(sm + L_XB + (t & 1) * XBUF + sj * 4096 + i * 1024 + l16), b = *(const v4u*)(sm + L_XB + (t & 1) * XBUF + sj * 4096 + i * 1024 + 1024 + l16);
          mx = umax3(umax3(a[0], a[1], a[2]), umax3(a[3], b[0], b[1]), umax3(b[2], b[3], mx));
          asm volatile("" : "+v"(mx) :: "memory");
        }
        ok = __all((mx != EMPTY) & (sst[1] == (hh ? tag1 : tag0)));
      } while (!ok && ++spins < SPIN_CAP);
    };
    auto st_write = [&](int t) AI {
      unsigned char* sp = sm + L_ST + (t % 3) * 2048 + (sj >> 2) * 512 + tokl * 16 + (sj & 3) * 4;
      *(unsigned*)(sp + hh * 1024) = sst[0];
    };
    const int xrd = tokl * 128 + ((hh ^ ((tokl >> 1) & 7)) << 4);
    auto xfrag = [&](int t, int ks) AI -> v4u { return *(const v4u*)(sm + L_XB + (t & 1) * XBUF + (xrd ^ ((ks & 3) << 5)) + (ks >> 2) * 4096); };
    const int hbase = OFF_H + ly * H_BYTES;
    v4u hf[14];
    auto h_load = [&](int u, int i) AI { hf[i] = ldfrag<SC1>(rs, l16, hbase + ((Ta + u) * KS2 + (i < 7 ? kse : ksl)) * 1024, i % 7); };
    v2u tagv = {tag0, tag1};
    asm volatile("" : "+v"(tagv));
    auto h_flag = [&](int t) AI { st64<SC1>(tagv, rs, opaque(l16) == 0 ? 0 : OOB, OFF_FH + ((ly * 64 + tile_of(t)) * KS2 + fg) * FSTR); };
    auto hf_poll = [&](int u, int half) AI { return ld64<SC1>(rs, min(opaque(l16) * (FSTR / 16), 6 * FSTR), OFF_FH + ((ly * 64 + Ta + u) * KS2 + (half ? ksl : kse)) * FSTR); };
    auto hf_check = [&](int u, int half, v2u v) AI { SPIN(flag_ok(v), v = hf_poll(u, half)); ORDER(); };
    v2u hpl;

    v4u sgr;
    auto cs_write = [&]() AI {
      const int ln = opaque(lane), csrow = (ln & 16) + 8 * ((ln >> 2) & 1) + (ln & 3) + 4 * ((ln >> 3) & 1);
      *(float*)(sm + L_CS + w * 128 + csrow * 4) = csum;
    };
    if (ly == 0) {
      x_fetch(0);
      st_write(0);
      __syncthreads();
      x_dma(1, 0); x_dma(1, 1);
    } else {
      SPIN(x_ok(), x0_load());
      __syncthreads();
      cs_write();
#pragma unroll
      for (int i = 0; i < 4; ++i) *(v4u*)(sm + L_XB + sj * 4096 + i * 1024 + l16) = xs[i];
      st_write(0);
      sgr = sp_load(2, Ta + 1);
      x_dma(1, 0); x_dma(1, 1);
      asm volatile("s_waitcnt lgkmcnt(0)" ::: "memory");
      raw_barrier();
    }

    f32x16 acc[2];
    v4u xf[PD], ho;
    f32x4 p0, p1, p2, p3;
    float mean = 0.f, r2[2];
#pragma unroll
    for (int i = 0; i < PD; ++i) xf[i] = xfrag(0, i + (ly == 1 ? NDROP : 0));
    auto ep_cs = [&](int hq) AI {
      const unsigned char* cp = sm + L_CS + w * 128 + hh * 32;
      if (hq) { p2 = *(const f32x4*)(cp + 16); p3 = *(const f32x4*)(cp + 80); } else { p0 = *(const f32x4*)cp; p1 = *(const f32x4*)(cp + 64); }
    };
    float hv[2], sq = 0.f;
    auto ep_one = [&](const f32x16& a, int i) AI {
      hv[i & 1] = fmaxf(fmaf(mean, i < 4 ? p0[i & 3] : p2[i & 3], a[i]), 0.f) * fmaf(mean, i < 4 ? p1[i & 3] : p3[i & 3], a[8 + i]);
      asm volatile("" : "+v"(hv[i & 1]));
    };
    auto h_store = [&](int t) AI { st128<SC1>(ho, rs, l16, hbase + (tile_of(t) * KS2 + fg) * 1024); };
    static_for<0, 4>([&](auto TT) AI {
      constexpr int t = TT.value;
      constexpr int rot = (ly == 1 && t == 0) ? NDROP : 0;
      v4u cb0, cb1, cb2, cb3;
      unsigned cmx = 0u;
      float ga = 0.f, gb = 0.f;
      static_for<0, KS1>([&](auto II) AI {
        constexpr int i = II.value, n = t * KS1 + i;
        if constexpr (i == 0) {
          f32x16 z;
#pragma unroll
          for (int e = 0; e < 16; ++e) z[e] = 0.f;
          acc[t & 1] = mfma32(W1[rot], xf[n % PD], z);
        } else acc[t & 1] = mfma32(W1[(i + rot) % KS1], xf[n % PD], acc[t & 1]);
        asm volatile("" : "+v"(acc[t & 1]));
        SBAR();
        if constexpr (i + PD < KS1) xf[n % PD] = xfrag(t, (i + PD + rot) % KS1);
        else if constexpr (t < 3) xf[n % PD] = xfrag(t + 1, i + PD - KS1);
        if constexpr (t == 2 && i == 0) { wait_vm<5 + (ly == 0 ? 6 : 0)>(); h_flag(0); }
        if constexpr (t == 2 && i == 24) { wait_vm<(ly == 0 ? 6 : 1)>(); h_flag(1); }
        if constexpr (t == 3 && i == 23) { wait_vm<6>(); h_flag(2); }
        if constexpr (t > 0) {
          if constexpr (i >= 3 && i <= 9 && (i & 1)) { ho[(i - 3) / 2] = pk2(hv[0], hv[1]); asm volatile("" : "+v"(ho[(i - 3) / 2])); }
          if constexpr (i >= 1 && i <= 8) ep_one(acc[(t - 1) & 1], i - 1);
          if constexpr (i == H_ST) h_store(t - 1);
        }
        if constexpr (t < 3) {
          const unsigned char* xb = sm + L_XB + ((t + 1) & 1) * XBUF + sj * 4096 + l16;
          constexpr int c0s = t ? XC : XC0, c1s = t ? XC + 3 : XC0 + 3, c2s = t ? XC + 4 : XC0 + 1, c3s = t ? XC + 7 : XC0 + 4, c4s = c3s + 1;
          if constexpr (i == c0s) {
            asm volatile("" : "+v"(sst) :: "memory");
            cb0 = *(const v4u*)xb; cb1 = *(const v4u*)(xb + 1024);
          }
          if constexpr (i == c1s) {
            cmx = umax3(umax3(cb0[0], cb0[1], cb0[2]), umax3(cb0[3], cb1[0], cb1[1]), umax3(cb1[2], cb1[3], cmx));
            asm volatile("" : "+v"(cmx));
          }
          if constexpr (i == c2s) {
            cb2 = *(const v4u*)(xb + 2048); cb3 = *(const v4u*)(xb + 3072);
            cmx = sst[1] != (hh ? tagv[1] : tagv[0]) ? EMPTY : cmx;
            asm volatile("" : "+v"(cmx));
          }
          if constexpr (i == c3s) {
            cmx = umax3(umax3(cb2[0], cb2[1], cb2[2]), umax3(cb2[3], cb3[0], cb3[1]), umax3(cb3[2], cb3[3], cmx));
            asm volatile("" : "+v"(cmx));
          }
          if constexpr (i == c4s) {
            if (__builtin_expect(!__all(cmx != EMPTY), 0)) x_fetch(t + 1);
            st_write(t + 1);
          }
        }
        if constexpr (ly == 0 && t == 0 && i == 4)
          st128<SC1>(v4u{EMPTY, EMPTY, EMPTY, EMPTY}, rs, l16, OFF_X + ACT_BYTES + ((Ta + (w >> 2)) * 7 + js) * 4096 + (w & 3) * 1024);
        if constexpr (ly == 0 && t == 0 && NPRE2 + i < KS1) ld_wgt(NPRE2 + i);
        if constexpr (ly == 0 && t == 0) {
          auto fw2_poll = [&]() AI { const int ln = opaque(lane); return ld64<SC1>(rs, (((ln & 8) ? ksl : kse) + min(ln & 7, 6)) * 128, OFF_FW2 + js * KS2 * 128); };
          if constexpr (i == 6) hpl = fw2_poll();
          if constexpr (i == 17) { SPIN(flag_ok(hpl), hpl = fw2_poll()); ORDER(); }
        }
        if constexpr (ly == 0 && t == 0 && i >= 1 && i <= 4) {
          if constexpr (i == 1) csum = 0.f;
#pragma unroll
          for (int q7 = (i - 1) * 4; q7 < i * 4 && q7 < 14; ++q7) csum += asf(csp[q7]);
          asm volatile("" : "+v"(csum));
        }
        if constexpr (ly == 0 && t == 0 && i == 5) { csum = half_sum(csum); asm volatile("" : "+v"(csum)); }
        if constexpr (ly == 0 && t == 0 && i == 8) cs_write();
        if constexpr (ly == 0 && (t == 1 || t == 2) && (i == 2 || i == 6 || i == 12)) ld_wgt(KS1 + (t - 1) * 4 + i / 5);
        if constexpr (ly == 0 && (t == 1 || t == 2) && i == 21) ld_wgt(KS1 + (t - 1) * 4 + 3);
        if constexpr (ly == 0 && t == 2 && i >= 24 && i < 27) ld_wgt(KS1 + 8 + i - 24);
        if constexpr (wl_slot(ly, t, i) >= 0) {
          constexpr int qn = wl_slot(ly, t, i), f = NWS + qn;
          dma1(rs, l16, wd_src(f), lds0 + L_WD + (w * NWL + qn) * 1024);
        }
        if constexpr (ly == 1 && t == 0) {
          constexpr int st = i - X3S;
          if constexpr (st == 0) SPIN(tag_ok(sgr), sgr = sp_load(2, Ta + 1));
          if constexpr (st == 1) { ga = i16 < 7 ? asf(sgr[0]) : 0.f; gb = i16 < 7 ? asf(sgr[2]) : 0.f; }
          if constexpr (st == 2 || st == 9) { ga = dpp_add<0x128>(ga); gb = dpp_add<0x128>(gb); PIN2(ga, gb); ga = dpp_add<0x124>(ga); gb = dpp_add<0x124>(gb); }
          if constexpr (st == 3 || st == 10) { ga = dpp_add<0x122>(ga); gb = dpp_add<0x122>(gb); PIN2(ga, gb); ga = dpp_add<0x121>(ga); gb = dpp_add<0x121>(gb); }
          if constexpr (st == 4) { float m, r; row_stats(ga, gb, m, r); ga = r; gb = -m * r; }
          if constexpr (st == 5 || st == 6) {
            constexpr int e = st - 5;
            const float xl = bflo(xres[1][e]), xh = bfhi(xres[1][e]);
            float ol = fmaf(fmaf(xl, ga, gb), bflo(yres[1][e]), xl), oh = fmaf(fmaf(xh, ga, gb), bfhi(yres[1][e]), xh);
            PIN2(ol, oh);
            unsigned o = nz(pk2(ol, oh));
            asm volatile("" : "+v"(o));
            xres[1][e] = o;
          }
          if constexpr (st == 7) st64<SC1>(xres[1], rs, (lane * 8) ^ (((2 * w + hh) & 7) << 4), OFF_X + ACT_BYTES + ((Ta + 1) * 7 + js) * 4096 + w * 512);
          if constexpr (st == 8) { ga = dot2(xres[1][0], ONES, dot2(xres[1][1], ONES, 0.f)); gb = dot2(xres[1][0], xres[1][0], dot2(xres[1][1], xres[1][1], 0.f)); }
          if constexpr (st == 11) st128<SC1>(v4u{asu(ga), tagv[0], asu(gb), tagv[1]}, rs, (lane & 15) == 0 ? lane : OOB, OFF_SP + 3 * SP_BYTES + ((Ta + 1) * 7 + js) * 512 + w * 64);
          if constexpr (st >= 1 && st <= 10) PIN2(ga, gb);
          if constexpr (i == BAR) asm volatile("" : "+v"(W1[NDROP - 1]));
        }
        if constexpr (ly == 1 && t == 0 && i >= 3 && i < 15 && (i & 1)) W1[8 + (i - 3) / 2] = ldfrag<SC1>(rs, l16, w1o, 8 + (i - 3) / 2);
        if constexpr (t < 2 && (i == BAR + 1 || i == BAR + 3)) x_dma(t + 2, (i - BAR - 1) / 2);
        if constexpr (t == 2 && i == 15) hpl = hf_poll(0, 0);
        if constexpr (t == 3) {
          if constexpr (i == 0) hf_check(0, 0, hpl);
          if constexpr (i == 7) hpl = hf_poll(0, 1);
          if constexpr (i == 16) hf_check(0, 1, hpl);
          if constexpr (i < 14 && !(i & 1)) h_load(0, i / 2);
          if constexpr (i >= 16 && !(i & 1)) h_load(0, 7 + (i - 16) / 2);
        }
        if constexpr (i == 11) {
          const unsigned char* sp = sm + L_ST + (t % 3) * 2048 + tokl * 16;
          p0 = *(const f32x4*)sp; p1 = *(const f32x4*)(sp + 512);
        }
        if constexpr (i == 13) { p0[0] += p0[1]; p0[2] += p0[3]; p1[0] += p1[1]; asm volatile("" : "+v"(p0[0]), "+v"(p0[2]), "+v"(p1[0])); }
        if constexpr (i == 15) { mean = ((p0[0] + p0[2]) + (p1[0] + p1[2])) * (-1.0f / D); asm volatile("" : "+v"(mean)); }
        if constexpr (!(t & 1)) {
          if constexpr (i == 16) { const unsigned char* sp = sm + L_ST + (t % 3) * 2048 + tokl * 16; p2 = *(const f32x4*)(sp + 1024); p3 = *(const f32x4*)(sp + 1536); }
          if constexpr (i == 19) { p2[0] += p2[1]; p2[2] += p2[3]; p3[0] += p3[1]; asm volatile("" : "+v"(p2[0]), "+v"(p2[2]), "+v"(p3[0])); }
          if constexpr (i == 20) { sq = (p2[0] + p2[2]) + (p3[0] + p3[2]); asm volatile("" : "+v"(sq)); }
          if constexpr (i == 26) { sq = __builtin_amdgcn_rcpf(fmaxf(fmaf(sq, 1.0f / D, -mean * mean), 0.0f) + EPS); asm volatile("" : "+v"(sq)); }
          if constexpr (i == 27) r2[t >> 1] = asf(__builtin_amdgcn_ds_bpermute(w * 16 + (lane >> 4) * 4, asu(sq)));
        }
        if constexpr (i == 25) ep_cs(0);
        if constexpr (t > 0 && i == 2) ep_cs(1);
        if constexpr (t == 3 && i == 27) ep_cs(1);
        if constexpr ((t == 3 && i == BAR) || (t > 0 && i == 11)) raw_barrier();
        if constexpr (i == BAR && t < 3) {
          asm volatile("s_waitcnt lgkmcnt(1)" ::: "memory");
          raw_barrier();
        }
        SBAR();
      });
    });
    v4u sga;
    int rad = 0;
    auto unit = [&](auto UU) AI {
      constexpr int u = UU.value;
      v4u wl[3];
      int wad = 0;
      f32x4 fa, fb, fc;
      auto red_write = [&](int jq) AI {
        const f32x16& a = acc[jq >> 2];
        const int qd = jq & 3;
        *(f32x4*)(sm + L_RED + (wad ^ ((4 * (jq >> 1) + (jq & 1)) << 4)) + jq * 1024) = f32x4{a[4 * qd], a[4 * qd + 1], a[4 * qd + 2], a[4 * qd + 3]};
      };
      static_for<0, 28>([&](auto FF) AI {
        constexpr int f = FF.value, ct = (f / 7) & 1, hi = f % 7 + 7 * (f / 14);
        v4u wf;
        if constexpr (f < NWS) wf = Wd[f]; else if constexpr (f < NWS + NWL) wf = wl[f % 3]; else wf = Wd[f - NWL];
        if constexpr (f == 0 || f == 7) {
          f32x16 z;
#pragma unroll
          for (int e = 0; e < 16; ++e) z[e] = 0.f;
          acc[ct] = mfma32(wf, hf[hi], z);
        } else acc[ct] = mfma32(wf, hf[hi], acc[ct]);
        asm volatile("" : "+v"(acc[ct]));
        SBAR();
        if constexpr (f + 3 >= NWS && f + 3 < NWS + NWL) wl[f % 3] = *(const v4u*)(sm + L_WD + (w * NWL + (f + 3 - NWS)) * 1024 + l16);
        if constexpr (u == 0) {
          if constexpr (f < 6) {
            constexpr int eb[7] = {0, 1, 3, 4, 6, 7, 8};
#pragma unroll
            for (int e = eb[f]; e < eb[f + 1]; ++e) { ep_one(acc[1], e); if (e & 1) ho[e >> 1] = pk2(hv[0], hv[1]); }
          }
          if constexpr (f == 6) h_store(3);
          if constexpr (f == 11) hpl = hf_poll(1, 0);
          if constexpr (f == 18) { wait_vm<1>(); h_flag(3); }
          if constexpr (f == 19) hf_check(1, 0, hpl);
          if constexpr (f == 0) h_load(0, 13);
          constexpr int k0 = ly == 0 ? 0 : KT;
          if constexpr (f >= 2 && f < 2 + 2 * (NWR - NWS - k0) && !(f & 1)) Wd[NWS + k0 + (f - 2) / 2] = ld_wd(NWL + NWS + k0 + (f - 2) / 2);
          if constexpr (f >= 19 && (f & 1)) h_load(1, (f - 19) / 2);
        }
        if constexpr (u == 1 && pace(f) >= 0 && pace(f) < 3) h_load(1, 11 + pace(f));
        if constexpr (u == 1 && ly == 0) {
          if constexpr (pace(f) >= 3) W1[pace(f) - 3] = ldfrag<SC1>(rs, l16, w1o, pace(f) - 3);
          if constexpr (f == SGA) sga = sp_load(2, Ta);
        }
        if constexpr (ly == 1 && u == 1 && f < 14) {
          auto rd = [&](int ww) AI { return *(const f32x4*)(sm + L_RED + rad + ww * 8192); };
          auto add = [&](f32x4& a, const f32x4& b) AI { a[0] += b[0]; a[1] += b[1]; PIN2(a[0], a[1]); a[2] += b[2]; a[3] += b[3]; PIN2(a[2], a[3]); };
          if constexpr (f == 0) { fa = rd(0); fb = rd(1); fc = rd(2); }
          if constexpr (f >= 2 && f <= 8) { if constexpr (f & 1) { add(fa, fc); if constexpr (f + 1 < 8) fc = rd(f + 1); } else { add(fa, fb); if constexpr (f + 1 < 8) fb = rd(f + 1); } }
          if constexpr (f == 9) { fa[0] *= r2[0]; fa[1] *= r2[0]; PIN2(fa[0], fa[1]); fa[2] *= r2[0]; fa[3] *= r2[0]; PIN2(fa[2], fa[3]); }
          if constexpr (f == 10 || f == 11) {
            constexpr int e = f - 10;
            const unsigned fq = pk2(fa[2 * e], fa[2 * e + 1]);
            float ol = bflo(xres[0][e]) + bflo(fq), oh = bfhi(xres[0][e]) + bfhi(fq);
            PIN2(ol, oh);
            unsigned o = pk2(ol, oh);
            asm volatile("" : "+v"(o));
            xres[0][e] = o;
          }
          if constexpr (f == 12) { const int ln = opaque(lane); st64(xres[0], mk_rsrc(out, ACT_BYTES), (ln >> 4) * (D * 2) + (ln & 15) * 8, ((Ta * 32 + 4 * w) * D + js * 64) * 2); }
        }
        if constexpr (f == 22) { const int ln = opaque(lane); wad = opaque(w * 8192 + ((ln ^ ((ln >> 5) << 1)) << 4)); }
        if constexpr (f == 27) {
          const int ln = opaque(lane), li16 = ln & 15, ftok = 4 * w + (ln >> 4);
          rad = opaque((2 * (li16 >> 2) + (li16 & 1)) * 1024 + (((li16 >> 1) & 1) * 32 + (ftok ^ li16)) * 16);
        }
        if constexpr (f >= 23 && f < 27) red_write(f - 23);
        if constexpr (f == ((ly == 1 && u == 1) ? 9 : GB)) __syncthreads();
        SBAR();
      });
      if constexpr (u == 1 && ly == 0) { x3_publish(0, sga); SBAR(); }
#pragma unroll
      for (int jq = 4; jq < 8; ++jq) red_write(jq);
      SBAR();
      if constexpr (u == 0) { h_load(1, 5); h_load(1, 6); hpl = hf_poll(1, 1); }
      SBAR();
      __syncthreads();
      if constexpr (u == 1 && ly == 0) x0_load();
      if constexpr (!(ly == 1 && u == 0)) {
        f32x4 pr[8];
#pragma unroll
        for (int ww = 0; ww < 8; ++ww) pr[ww] = *(const f32x4*)(sm + L_RED + rad + ww * 8192);
        SBAR();
        const f32x4 a = ((pr[0] + pr[1]) + (pr[2] + pr[3])) + ((pr[4] + pr[5]) + (pr[6] + pr[7]));
        const unsigned f01 = pk2(a[0] * r2[u], a[1] * r2[u]), f23 = pk2(a[2] * r2[u], a[3] * r2[u]);
        const v2u xn = {pk2(bflo(xres[u][0]) + bflo(f01), bfhi(xres[u][0]) + bfhi(f01)), pk2(bflo(xres[u][1]) + bflo(f23), bfhi(xres[u][1]) + bfhi(f23))};
        if constexpr (ly == 1) {
          const int ln = opaque(lane);
          st64(xn, mk_rsrc(out, ACT_BYTES), (ln >> 4) * (D * 2) + (ln & 15) * 8, (((Ta + u) * 32 + 4 * w) * D + js * 64) * 2);
        } else {
          xres[u] = xn;
          sp_publish(2, u);
        }
      }
      SBAR();
      if constexpr (u == 0) {
        hf_check(1, 1, hpl);
#pragma unroll
        for (int i = 7; i < 11; ++i) h_load(1, i);
        SBAR();
      }
    };
    unit(IC<0>{});
    unit(IC<1>{});
  };
  layer(IC<0>{});
  layer(IC<1>{});
  st64<SC1>(v2u{sopq(tag0), sopq(tag1)}, rs, (bid == 0 && tid == 0) ? 0 : OOB, OFF_EP);
}

extern "C" void kernel_launch(void* const* d_in, const int* in_sizes, int n_in, void* d_out, int out_size, void* d_ws,
                              size_t ws_size, hipStream_t stream) {
  const uint16_t* q = (const uint16_t*)d_in[0];
  const uint16_t* k = (const uint16_t*)d_in[1];
  const uint16_t* v = (const uint16_t*)d_in[2];
  const uint16_t* shk = (const uint16_t*)d_in[3];
  const uint16_t* ssk = (const uint16_t*)d_in[4];
  const uint16_t* ssd = (const uint16_t*)d_in[5];
  const uint16_t* wg = (const uint16_t*)d_in[7];
  const uint16_t* wu = (const uint16_t*)d_in[8];
  const uint16_t* wd = (const uint16_t*)d_in[9];
  unsigned char* ws = (unsigned char*)d_ws;
  uint16_t* out = (uint16_t*)d_out;
  (void)hipFuncSetAttribute((const void*)k_main, hipFuncAttributeMaxDynamicSharedMemorySize, L_BYTES);
  k_main<<<NAUX + 224, 512, L_BYTES, stream>>>(q, k, v, shk, ssk, ssd, wg, wu, wd, ws, out);
}
```
